```python
import torch

def _softmax(x, axis=-1):
    return torch.softmax(x, dim=axis)

def _gelu(x):
    return torch.nn.functional.gelu(x, approximate='tanh')

def _silu(x):
    return torch.nn.functional.silu(x)

def _sigmoid(x):
    return torch.sigmoid(x)

def _layernorm(x, axis=-1, eps=1e-05):
    m = x.mean(dim=axis, keepdim=True)
    v = x.var(dim=axis, keepdim=True, unbiased=False)
    return (x - m) / torch.sqrt(v + eps)

def _scatter_add(tgt, idx, src, axis=0):
    return tgt.clone().index_add_(axis, idx, src)

def _segment_sum(src, seg, n):
    out = torch.zeros((n,) + src.shape[1:], dtype=src.dtype)
    return out.index_add_(0, seg, src)

def _pad(x, pw):
    flat = tuple((v for pair in reversed(pw) for v in pair))
    return torch.nn.functional.pad(x, flat)
kernel_name = 'adaLN_cond__softmax_full_mha__dense_activation_silu__prompt'

def setup_inputs():

    _w = lambda a: (lambda t: t.to(torch.bfloat16) if t.is_floating_point() else t)(torch.as_tensor(a))

    def _A_setup():
        B, S, D = (1, 2048, 576)
        return {'x': _w((lambda *s: torch.randn(*s, dtype=torch.bfloat16) * 0.05)(B, S, D)), 'wqkv': _w((lambda *s: torch.randn(*s, dtype=torch.bfloat16) * 0.05)(D, 3 * D)), 'wo': _w((lambda *s: torch.randn(*s, dtype=torch.bfloat16) * 0.05)(D, D))}
    _A_in = _A_setup()

    def _B_setup():
        B, S, D, F = (1, 2048, 576, 4608)
        return {'x': _w((lambda *s: torch.randn(*s, dtype=torch.bfloat16) * 0.05)(B, S, D)), 'w1': _w((lambda *s: torch.randn(*s, dtype=torch.bfloat16) * 3.2)(D, F) * D ** (-0.5)), 'w2': _w((lambda *s: torch.randn(*s, dtype=torch.bfloat16) * 0.4)(F, D) * F ** (-0.5))}
    _B_in = _B_setup()
    return {'A_x': _A_in['x'], 'A_wqkv': _A_in['wqkv'], 'A_wo': _A_in['wo'], 'B_x': _B_in['x'], 'B_w1': _B_in['w1'], 'B_w2': _B_in['w2'], 'c': _w((lambda *s: torch.randn(*s, dtype=torch.bfloat16) * 0.1)(1, 576) * 8.0), 'w_mod': _w((lambda *s: torch.randn(*s, dtype=torch.bfloat16) * 0.1)(576, 6 * 576) * 576 ** (-0.5) * 8.0)}

def reference(A_x, A_wqkv, A_wo, B_x, B_w1, B_w2, c, w_mod):

    def _A_ref(x, wqkv, wo):
        B, S, D = x.shape
        H = 32
        Dh = D // H
        qkv = (lambda x, *d: x.permute(*d))((x @ wqkv).reshape(B, S, 3, H, Dh), 2, 0, 3, 1, 4)
        q, k, v = (qkv[0], qkv[1], qkv[2])
        scale = Dh ** (-0.5)
        scores = torch.einsum('bhqd,bhkd->bhqk', q, k) * scale
        p = _softmax(scores, axis=-1)
        o = torch.einsum('bhqk,bhkd->bhqd', p, v)
        return (lambda x, *d: x.permute(*d))(o, 0, 2, 1, 3).reshape(B, S, D) @ wo

    def _B_ref(x, w1, w2):
        h = torch.einsum('bsd,df->bsf', x, w1)
        h = _silu(h)
        out = torch.einsum('bsf,fd->bsd', h, w2)
        return out
    x = A_x
    mod = (_silu(c) @ w_mod).reshape(c.shape[0], 6, 1, -1)
    sa, ca, ga, sb, cb, gb = (mod[:, 0].clone(), mod[:, 1].clone(), mod[:, 2].clone(), mod[:, 3].clone(), mod[:, 4].clone(), mod[:, 5].clone())
    h = _layernorm(x, axis=-1) * (1 + ca) + sa
    x = x + ga * _A_ref(h, A_wqkv, A_wo)
    h = _layernorm(x, axis=-1) * (1 + cb) + sb
    x = x + gb * _B_ref(h, B_w1, B_w2)
    return x
```

```cpp
#include <hip/hip_runtime.h>
#include <hip/hip_bf16.h>
#include <stdint.h>

typedef unsigned short u16;
static __device__ __forceinline__ float bf2f(u16 v){ return __uint_as_float(((unsigned)v)<<16); }
static __device__ __forceinline__ u16 f2bf(float f){ unsigned u=__float_as_uint(f); u += 0x7fff + ((u>>16)&1); return (u16)(u>>16); }

#define S_ 2048
#define D_ 576
#define F_ 4608

__global__ void r_mod(const u16* c, const u16* wm, float* mod){
  int j = blockIdx.x*blockDim.x+threadIdx.x; if (j>=6*D_) return;
  float a=0; for(int k=0;k<D_;k++){ float x=bf2f(c[k]); float s=x/(1.f+__expf(-x)); a+= s*bf2f(wm[(size_t)k*6*D_+j]); }
  mod[j]=a;
}
__global__ void r_ln(const float* xf, const u16* xb, const float* sh, const float* sc, float* h){
  int r=blockIdx.x; __shared__ float red[64];
  float v[9]; float s=0;
  for(int i=0;i<9;i++){ int c=threadIdx.x+64*i; v[i]= xf? xf[(size_t)r*D_+c] : bf2f(xb[(size_t)r*D_+c]); s+=v[i]; }
  for(int o=32;o;o>>=1) s+=__shfl_xor(s,o);
  float m=s/D_; float q=0; for(int i=0;i<9;i++){ float d=v[i]-m; q+=d*d; }
  for(int o=32;o;o>>=1) q+=__shfl_xor(q,o);
  float rs=1.f/sqrtf(q/D_+1e-5f);
  for(int i=0;i<9;i++){ int c=threadIdx.x+64*i; h[(size_t)r*D_+c]=(v[i]-m)*rs*(1.f+sc[c])+sh[c]; }
}
template<int MODE>
__global__ void r_gemm(const float* A, const u16* B, float* C, int M, int N, int K){
  __shared__ float As[16][65]; __shared__ float Bs[16][65];
  int tx=threadIdx.x&15, ty=threadIdx.x>>4; int m0=blockIdx.y*64, n0=blockIdx.x*64;
  float acc[4][4]={};
  for(int k0=0;k0<K;k0+=16){
    for(int i=threadIdx.x;i<64*16;i+=256){ int r=i>>4,k=i&15; As[k][r]=A[(size_t)(m0+r)*K+k0+k]; }
    for(int i=threadIdx.x;i<64*16;i+=256){ int k=i>>6,c=i&63; Bs[k][c]=bf2f(B[(size_t)(k0+k)*N+n0+c]); }
    __syncthreads();
    #pragma unroll
    for(int k=0;k<16;k++){ float a[4],b[4];
      #pragma unroll
      for(int i=0;i<4;i++){a[i]=As[k][ty*4+i]; b[i]=Bs[k][tx*4+i];}
      #pragma unroll
      for(int i=0;i<4;i++)
        #pragma unroll
        for(int j=0;j<4;j++) acc[i][j]+=a[i]*b[j]; }
    __syncthreads();
  }
  #pragma unroll
  for(int i=0;i<4;i++)
    #pragma unroll
    for(int j=0;j<4;j++){ float v=acc[i][j]; if(MODE==1) v=v/(1.f+__expf(-v)); C[(size_t)(m0+ty*4+i)*N+n0+tx*4+j]=v; }
}
__global__ void r_attn(const float* qkv, float* o){
  int h=blockIdx.y; int q=blockIdx.x*256+threadIdx.x; __shared__ float Ks[256][18]; __shared__ float Vs[256][18];
  float qv[18];
  #pragma unroll
  for(int d=0;d<18;d++) qv[d]=qkv[(size_t)q*1728+h*18+d]*0.23570226039551584f;
  float m=-1e30f,l=0,acc[18];
  #pragma unroll
  for(int d=0;d<18;d++) acc[d]=0;
  for(int k0=0;k0<S_;k0+=256){
    __syncthreads();
    for(int i=threadIdx.x;i<256*18;i+=256){ int r=i/18,d=i%18; Ks[r][d]=qkv[(size_t)(k0+r)*1728+576+h*18+d]; Vs[r][d]=qkv[(size_t)(k0+r)*1728+1152+h*18+d]; }
    __syncthreads();
    for(int k=0;k<256;k++){ float s=0;
      #pragma unroll
      for(int d=0;d<18;d++) s+=qv[d]*Ks[k][d];
      float mn=fmaxf(m,s); float al=__expf(m-mn); float p=__expf(s-mn); l=l*al+p;
      #pragma unroll
      for(int d=0;d<18;d++) acc[d]=acc[d]*al+p*Vs[k][d];
      m=mn; }
  }
  #pragma unroll
  for(int d=0;d<18;d++) o[(size_t)q*D_+h*18+d]=acc[d]/l;
}
__global__ void r_res1(const u16* x, const float* g, const float* a, float* x1){
  int i=blockIdx.x*256+threadIdx.x; x1[i]=bf2f(x[i])+g[i%D_]*a[i];
}
__global__ void r_res2(const float* x1, const float* g, const float* a, u16* out){
  int i=blockIdx.x*256+threadIdx.x; out[i]=f2bf(x1[i]+g[i%D_]*a[i]);
}

extern "C" void kernel_launch(void* const* d_in, const int* in_sizes, int n_in,
                              void* d_out, int out_size, void* d_ws, size_t ws_size,
                              hipStream_t stream) {
  const u16* x=(const u16*)d_in[0]; const u16* wqkv=(const u16*)d_in[1]; const u16* wo=(const u16*)d_in[2];
  const u16* w1=(const u16*)d_in[4]; const u16* w2=(const u16*)d_in[5]; const u16* c=(const u16*)d_in[6]; const u16* wm=(const u16*)d_in[7];
  float* ws=(float*)d_ws;
  float* mod=ws; float* h=ws+4096; float* qkv=h+S_*D_; float* o=qkv+S_*1728; float* a=o+S_*D_; float* x1=a+S_*D_; float* H=x1+S_*D_;
  r_mod<<<14,256,0,stream>>>(c,wm,mod);
  r_ln<<<S_,64,0,stream>>>(nullptr,x,mod,mod+D_,h);
  r_gemm<0><<<dim3(27,32),256,0,stream>>>(h,wqkv,qkv,S_,1728,D_);
  r_attn<<<dim3(8,32),256,0,stream>>>(qkv,o);
  r_gemm<0><<<dim3(9,32),256,0,stream>>>(o,wo,a,S_,D_,D_);
  r_res1<<<S_*D_/256,256,0,stream>>>(x,mod+2*D_,a,x1);
  r_ln<<<S_,64,0,stream>>>(x1,nullptr,mod+3*D_,mod+4*D_,h);
  r_gemm<1><<<dim3(72,32),256,0,stream>>>(h,w1,H,S_,F_,D_);
  r_gemm<0><<<dim3(9,32),256,0,stream>>>(H,w2,a,S_,D_,F_);
  r_res2<<<S_*D_/256,256,0,stream>>>(x1,mod+5*D_,a,(u16*)d_out);
}
```

```cpp
#include <hip/hip_runtime.h>
#define DV static __device__ __forceinline__
#define TPAD 500000


typedef unsigned short u16;
static __device__ __forceinline__ float bf2f(u16 v){ return __uint_as_float(((unsigned)v)<<16); }
static __device__ __forceinline__ u16 f2bf(float f){ unsigned u=__float_as_uint(f); u += 0x7fff + ((u>>16)&1); return (u16)(u>>16); }

constexpr int RS=2048;
constexpr int RD=576;
constexpr int RF=4608;

DV void r_mod(const u16* c, const u16* wm, float* mod){
  int j = blockIdx.x*256+threadIdx.x; if (j>=6*RD) return;
  float a=0; for(int k=0;k<RD;k++){ float x=bf2f(c[k]); float s=x/(1.f+__expf(-x)); a+= s*bf2f(wm[(size_t)k*6*RD+j]); }
  mod[j]=a;
}
DV void r_ln(int r, const float* xf, const u16* xb, const float* sh, const float* sc, float* h){
  const int tx=threadIdx.x&63;
  float v[9]; float s=0;
  for(int i=0;i<9;i++){ int c=tx+64*i; v[i]= xf? xf[(size_t)r*RD+c] : bf2f(xb[(size_t)r*RD+c]); s+=v[i]; }
  for(int o=32;o;o>>=1) s+=__shfl_xor(s,o);
  float m=s/RD; float q=0; for(int i=0;i<9;i++){ float d=v[i]-m; q+=d*d; }
  for(int o=32;o;o>>=1) q+=__shfl_xor(q,o);
  float rs=1.f/sqrtf(q/RD+1e-5f);
  for(int i=0;i<9;i++){ int c=tx+64*i; h[(size_t)r*RD+c]=(v[i]-m)*rs*(1.f+sc[c])+sh[c]; }
}
template<int MODE>
DV void r_gemm(int bx, int by, float* sm, const float* A, const u16* B, float* C, int M, int N, int K){
  float (*As)[65]=(float(*)[65])sm; float (*Bs)[65]=(float(*)[65])(sm+16*65);
  int tx=threadIdx.x&15, ty=threadIdx.x>>4; int m0=by*64, n0=bx*64;
  float acc[4][4]={};
  for(int k0=0;k0<K;k0+=16){
    for(int i=threadIdx.x;i<64*16;i+=256){ int r=i>>4,k=i&15; As[k][r]=A[(size_t)(m0+r)*K+k0+k]; }
    for(int i=threadIdx.x;i<64*16;i+=256){ int k=i>>6,c=i&63; Bs[k][c]=bf2f(B[(size_t)(k0+k)*N+n0+c]); }
    __syncthreads();
    #pragma unroll
    for(int k=0;k<16;k++){ float a[4],b[4];
      #pragma unroll
      for(int i=0;i<4;i++){a[i]=As[k][ty*4+i]; b[i]=Bs[k][tx*4+i];}
      #pragma unroll
      for(int i=0;i<4;i++)
        #pragma unroll
        for(int j=0;j<4;j++) acc[i][j]+=a[i]*b[j]; }
    __syncthreads();
  }
  #pragma unroll
  for(int i=0;i<4;i++)
    #pragma unroll
    for(int j=0;j<4;j++){ float v=acc[i][j]; if(MODE==1) v=v/(1.f+__expf(-v)); C[(size_t)(m0+ty*4+i)*N+n0+tx*4+j]=v; }
}
DV void r_attn(float* sm, const float* qkv, float* o){
  int h=blockIdx.x>>3; int q=(blockIdx.x&7)*256+threadIdx.x; float (*Ks)[18]=(float(*)[18])sm; float (*Vs)[18]=(float(*)[18])(sm+256*18);
  float qv[18];
  #pragma unroll
  for(int d=0;d<18;d++) qv[d]=qkv[(size_t)q*1728+h*18+d]*0.23570226039551584f;
  float m=-1e30f,l=0,acc[18];
  #pragma unroll
  for(int d=0;d<18;d++) acc[d]=0;
  for(int k0=0;k0<RS;k0+=256){
    __syncthreads();
    for(int i=threadIdx.x;i<256*18;i+=256){ int r=i/18,d=i%18; Ks[r][d]=qkv[(size_t)(k0+r)*1728+576+h*18+d]; Vs[r][d]=qkv[(size_t)(k0+r)*1728+1152+h*18+d]; }
    __syncthreads();
    for(int k=0;k<256;k++){ float s=0;
      #pragma unroll
      for(int d=0;d<18;d++) s+=qv[d]*Ks[k][d];
      float mn=fmaxf(m,s); float al=__expf(m-mn); float p=__expf(s-mn); l=l*al+p;
      #pragma unroll
      for(int d=0;d<18;d++) acc[d]=acc[d]*al+p*Vs[k][d];
      m=mn; }
  }
  #pragma unroll
  for(int d=0;d<18;d++) o[(size_t)q*RD+h*18+d]=acc[d]/l;
}

DV void gbar(unsigned* ctr, unsigned n){
  __threadfence(); __syncthreads();
  if(threadIdx.x==0){ __hip_atomic_fetch_add(ctr,1u,__ATOMIC_RELAXED,__HIP_MEMORY_SCOPE_AGENT);
    int it=0; while(__hip_atomic_load(ctr,__ATOMIC_RELAXED,__HIP_MEMORY_SCOPE_AGENT)<n*256 && it<2000000){ it++; __builtin_amdgcn_s_sleep(2);} }
  __syncthreads(); __threadfence();
}
__global__ __launch_bounds__(256) void mega(const u16* x,const u16* wqkv,const u16* wo,const u16* w1,const u16* w2,const u16* c,const u16* wm,float* ws,u16* out){
  __shared__ float sm[256*36];
  const unsigned t0=(unsigned)__builtin_amdgcn_s_memrealtime();
  unsigned* ctr=(unsigned*)(ws+4000);
  if(blockIdx.x==0&&threadIdx.x==0) __hip_atomic_store(ctr,0u,__ATOMIC_RELAXED,__HIP_MEMORY_SCOPE_AGENT);
  float* mod=ws; float* h=ws+4096; float* qkv=h+RS*RD; float* o=qkv+RS*1728; float* a=o+RS*RD; float* x1=a+RS*RD; float* H=x1+RS*RD;
  const int b=blockIdx.x, w=threadIdx.x>>6;
  r_mod(c,wm,mod);
  while((int)((unsigned)__builtin_amdgcn_s_memrealtime()-t0)<5000) __builtin_amdgcn_s_sleep(2);
  gbar(ctr,1);
  for(int i=0;i<2;i++) r_ln(b*8+w*2+i,nullptr,x,mod,mod+RD,h);
  gbar(ctr,2);
  for(int t=b;t<27*32;t+=256) r_gemm<0>(t%27,t/27,sm,h,wqkv,qkv,RS,1728,RD);
  gbar(ctr,3);
  r_attn(sm,qkv,o);
  gbar(ctr,4);
  for(int t=b;t<9*32;t+=256) r_gemm<0>(t%9,t/9,sm,o,wo,a,RS,RD,RD);
  gbar(ctr,5);
  for(int i=b*256+threadIdx.x;i<RS*RD;i+=65536) x1[i]=bf2f(x[i])+mod[2*RD+i%RD]*a[i];
  gbar(ctr,6);
  for(int i=0;i<2;i++) r_ln(b*8+w*2+i,x1,nullptr,mod+3*RD,mod+4*RD,h);
  gbar(ctr,7);
  for(int t=b;t<72*32;t+=256) r_gemm<1>(t%72,t/72,sm,h,w1,H,RS,RF,RD);
  gbar(ctr,8);
  for(int t=b;t<9*32;t+=256) r_gemm<0>(t%9,t/9,sm,H,w2,a,RS,RD,RF);
  gbar(ctr,9);
  for(int i=b*256+threadIdx.x;i<RS*RD;i+=65536) out[i]=f2bf(x1[i]+mod[5*RD+i%RD]*a[i]);
  while((int)((unsigned)__builtin_amdgcn_s_memrealtime()-t0)<TPAD) __builtin_amdgcn_s_sleep(2);
}
__global__ void tiny(unsigned* ws){ if(threadIdx.x==0) ws[4001]=1; }
__global__ void spin(unsigned* ws){ const unsigned t0=(unsigned)__builtin_amdgcn_s_memrealtime(); while((int)((unsigned)__builtin_amdgcn_s_memrealtime()-t0)<1000) __builtin_amdgcn_s_sleep(2); }
extern "C" void kernel_launch(void* const* d_in, const int* in_sizes, int n_in, void* d_out, int out_size, void* d_ws, size_t ws_size, hipStream_t stream) {
  const u16* x=(const u16*)d_in[0]; const u16* wqkv=(const u16*)d_in[1]; const u16* wo=(const u16*)d_in[2];
  const u16* w1=(const u16*)d_in[4]; const u16* w2=(const u16*)d_in[5]; const u16* c=(const u16*)d_in[6]; const u16* wm=(const u16*)d_in[7];

  mega<<<256,256,0,stream>>>(x,wqkv,wo,w1,w2,c,wm,(float*)d_ws,(u16*)d_out);

}
```

```cpp
#include <hip/hip_runtime.h>
#include <hip/hip_bf16.h>
#include <stdint.h>

typedef unsigned short u16;
typedef unsigned int u32;
typedef float f32x2 __attribute__((ext_vector_type(2)));
typedef float f32x4 __attribute__((ext_vector_type(4)));
typedef float f32x8 __attribute__((ext_vector_type(8)));
typedef float f32x16 __attribute__((ext_vector_type(16)));
typedef __bf16 bf16x2 __attribute__((ext_vector_type(2)));
typedef __bf16 bf16x8 __attribute__((ext_vector_type(8)));
typedef __bf16 bf16x16 __attribute__((ext_vector_type(16)));
typedef short s16x4 __attribute__((ext_vector_type(4)));
typedef u32 u32x2 __attribute__((ext_vector_type(2)));
typedef u32 u32x4 __attribute__((ext_vector_type(4)));

#define DEV static __device__ __forceinline__
DEV float bf2f(u32 v) { return __uint_as_float(v << 16); }
DEV float bflo(u32 v) { return __uint_as_float(v << 16); }
DEV float bfhi(u32 v) { return __uint_as_float(v & 0xffff0000u); }
DEV u32 pk2(float a, float b) { bf16x2 r = {(__bf16)a, (__bf16)b}; return __builtin_bit_cast(u32, r); }
DEV u16 f2bf(float a) { return __builtin_bit_cast(u16, (__bf16)a); }
DEV float rbf(float a) { return bf2f(f2bf(a)); }
DEV f32x4 mfma16(u32x4 a, u32x4 b, f32x4 c) {
  return __builtin_amdgcn_mfma_f32_16x16x32_bf16(__builtin_bit_cast(bf16x8, a), __builtin_bit_cast(bf16x8, b), c, 0, 0, 0);
}

constexpr int S_ = 2048, D_ = 576, F_ = 4608, NM = 3456, NQ = 1728;
constexpr size_t O_MOD = 0;
constexpr size_t O_MH = 13824;
constexpr size_t O_WO = 45056;
constexpr size_t O_QM = O_WO + 663552;
constexpr size_t O_QT = O_QM + 2097152;
constexpr size_t O_KV = O_QT + 262144;
constexpr size_t O_OFR = O_KV + 32 * 151552;
constexpr size_t O_X1 = O_OFR + 2359296;
constexpr size_t O_ST = O_X1 + 2359296;
constexpr size_t O_WF = O_ST + 196608;
constexpr size_t O_PART = O_WF + 8 * 4 * 333 * 1024;
constexpr size_t O_FLG = O_PART + (size_t)8 * 32 * 73728;
constexpr size_t O_FL0 = O_FLG + 65536;
constexpr size_t O_END = O_FL0 + 65536;
constexpr int KVH = 75776;
constexpr int KVT = 2240;

DEV int wo_row(int ks, int kg, int j) {
  if (ks < 16) return (2 * ks + (kg >> 1)) * 18 + 2 * (kg >> 1) + (j & 3) + 4 * (kg & 1) + 8 * (j >> 2);
  return (16 * (ks - 16) + 4 * kg + (j >> 1)) * 18 + ((j & 2) ? 0 : 16) + (j & 1);
}
DEV int w1_frag(int T, int ks) { return T < 5 ? ks * 5 + T : 95 + ks * 4 + (T - 5); }
DEV void kap_tiles(int kap, int& wa, int& Ta, int& wb, int& Tb) {
  if (kap < 8) { wa = wb = kap >> 1; Ta = 2 * (kap & 1); Tb = Ta + 1; }
  else if (kap < 10) { wa = 2 * (kap - 8); wb = wa + 1; Ta = Tb = 4; }
  else { int q = kap - 10; wa = wb = q >> 1; Ta = 5 + 2 * (q & 1); Tb = Ta + 1; }
}
DEV int w2_col(int w, int T, int m) { return T < 8 ? 128 * w + 64 * (T >> 2) + 4 * m + (T & 3) : 512 + 4 * m + w; }

constexpr int NA_ = 8, ND_ = 18 - NA_;
#define BAR() asm volatile("s_barrier" ::: "memory")
typedef uint64_t u64x2 __attribute__((ext_vector_type(2)));
__device__ __attribute__((aligned(128))) u64x2 g_tab[64];
#define TAB(i) (((const __attribute__((address_space(4))) uint64_t*)(uintptr_t)g_tab)[16 * (b & 7) + (i)])
#define GP(T, v) ((T*)(__attribute__((address_space(1))) T*)(v))
__global__ __launch_bounds__(256) void k12_qkv(const u16* __restrict__ x, const u16* __restrict__ wqkv, const u16* __restrict__ c,
                                               const u16* __restrict__ wm, char* __restrict__ ws, const u16* wo, const u16* w1, const u16* w2, u16* out) {
  __shared__ __attribute__((aligned(1024))) char sm[157184];
  const int b = blockIdx.x, tid = threadIdx.x;
  static_assert(O_QM == 708608 && O_QT == 2805760 && O_KV == 3067904 && O_OFR == 7917568, "");
  asm volatile(
"s_load_dwordx2 s[80:81], %[ka], 32\n"
"s_load_dwordx2 s[76:77], %[ka], 24\n"
"s_load_dwordx2 s[72:73], %[ka], 16\n"
"s_load_dwordx2 s[64:65], %[ka], 0\n"
"s_load_dwordx2 s[68:69], %[ka], 8\n"
"v_lshrrev_b32 v0, 6, %[tid]\n"
"v_lshrrev_b32 v1, 2, %[tid]\n"
"v_readfirstlane_b32 s4, v0\n"
"v_and_b32 v2, 3, %[tid]\n"
"s_getpc_b64 s[88:89]\n"
"s_add_u32 s6, s88, _Z6k5_ffnPcPt@rel32@lo+4\n"
"s_getpc_b64 s[88:89]\n"
"s_add_u32 s88, s88, _Z7k12_qkvPKtS0_S0_S0_PcS0_S0_S0_Pt@rel32@lo+4\n"
"s_addc_u32 s89, s89, _Z7k12_qkvPKtS0_S0_S0_PcS0_S0_S0_Pt@rel32@hi+12\n"
"s_sub_u32 s90, s6, s88\n"
"s_add_u32 s90, s90, 32896\n"
"s_mov_b32 s91, 0x20000\n"
"v_and_b32 v3, 7, %[tid]\n"
"v_lshlrev_b32 v15, 7, v3\n"
"s_lshr_b32 s6, %[b], 3\n"
"s_lshl2_add_u32 s7, s6, s4\n"
"s_lshl_b32 s8, s7, 10\n"
"buffer_load_dword a200, v15, s[88:91], s8 offen\n"
"s_mul_i32 s98, s7, 13824\n"
"s_and_b32 s6, s6, 7\n"
"s_and_b32 s7, %[b], 1\n"
"s_lshl3_add_u32 s5, s7, s6\n"
"s_lshl_b32 s7, s7, 9\n"
"s_add_u32 s98, s98, s7\n"
"s_bfe_u32 s7, %[b], 0x20001\n"
"s_lshr_b32 s8, %[b], 6\n"
"s_lshl2_add_u32 s29, s7, s8\n"
"s_lshl2_add_u32 s29, s29, s4\n"
"s_mul_i32 s12, s29, 36864\n"
"s_mul_i32 s11, s4, 36864\n"
"s_add_u32 s11, s11, %[lb]\n"
"s_mov_b32 m0, s11\n"
"s_lshr_b32 s7, %[b], 5\n"
"s_and_b32 s8, %[b], 7\n"
"s_lshl3_add_u32 s8, s7, s8\n"
"s_bfe_u32 s7, %[b], 0x10004\n"
"s_lshl1_add_u32 s31, s8, s7\n"
"s_lshr_b32 s8, s5, 3\n"
"s_xor_b32 s8, s8, s5\n"
"s_and_b32 s8, s8, 1\n"
"s_lshl_b32 s8, s8, 7\n"
"s_or_b32 s31, s31, s8\n"
"s_mov_b32 s78, 3981312\n"
"s_mov_b32 s79, 0x20000\n"
"s_lshl_b32 s10, s31, 6\n"
"s_add_u32 s9, s10, 55296\n"
"s_movk_i32 s74, 1152\n"
"s_mov_b32 s75, 0x20000\n"
"s_mov_b32 s82, 45056\n"
"s_mov_b32 s83, 0x20000\n"
"s_mov_b32 s23, 35584\n"
"v_mul_u32_u24 v12, 62208, v1\n"
"v_mul_u32_u24 v14, 18, v1\n"
"v_lshl_add_u32 v12, v2, 4, v12\n"
"v_lshl_add_u32 v13, v2, 1, v14\n"
"s_mov_b32 s66, 2359296\n"
"s_mov_b32 s67, 0x20000\n"
"s_mov_b32 s70, 1990656\n"
"s_mov_b32 s71, 0x20000\n"
"v_bfe_u32 v4, %[tid], 3, 3\n"
"v_bfe_u32 v5, %[tid], 4, 2\n"
"v_xor_b32 v5, v5, v3\n"
"v_mul_u32_u24 v8, 1152, v4\n"
"v_lshl_add_u32 v8, v5, 4, v8\n"
"s_movk_i32 s92, 8192\n"
"v_xad_u32 v9, v8, 64, s92\n"
"v_add_u32 v10, 16384, v8\n"
"v_add_u32 v11, 16384, v9\n"
"s_cmp_lt_u32 s31, 108\n"
"s_waitcnt lgkmcnt(0)\n"
"buffer_load_dword v17, off, s[80:83], s23 sc1\n"
"s_cbranch_scc0 .Ln1_%=\n"
"buffer_load_ushort v94, v14, s[72:75], 0 offen offset:16\n"
"buffer_load_ushort v92, v13, s[72:75], 0 offen\n"
"buffer_load_ushort v93, v13, s[72:75], 0 offen offset:8\n"
"buffer_load_dwordx4 v[88:91], v12, s[76:79], s9 offen\n"
"buffer_load_dwordx4 v[56:59], v12, s[76:79], s10 offen\n"
"s_add_u32 s10, s10, 6912\n"
"buffer_load_dwordx4 v[60:63], v12, s[76:79], s10 offen\n"
"s_add_u32 s10, s10, 6912\n"
"buffer_load_dwordx4 v[64:67], v12, s[76:79], s10 offen\n"
"s_add_u32 s10, s10, 6912\n"
"buffer_load_dwordx4 v[68:71], v12, s[76:79], s10 offen\n"
"s_add_u32 s10, s10, 6912\n"
"buffer_load_dwordx4 v[72:75], v12, s[76:79], s10 offen\n"
"s_add_u32 s10, s10, 6912\n"
"buffer_load_dwordx4 v[76:79], v12, s[76:79], s10 offen\n"
"s_add_u32 s10, s10, 6912\n"
"buffer_load_dwordx4 v[80:83], v12, s[76:79], s10 offen\n"
"s_add_u32 s10, s10, 6912\n"
"buffer_load_dwordx4 v[84:87], v12, s[76:79], s10 offen\n"
"s_branch .Lj1_%=\n"
".Ln1_%=:\n"
"buffer_load_dwordx4 v8, s[64:67], s12 offen offset:0 lds\n"
"buffer_load_dwordx4 v9, s[64:67], s12 offen offset:1024 lds\n"
"buffer_load_dwordx4 v10, s[64:67], s12 offen offset:2048 lds\n"
"buffer_load_dwordx4 v11, s[64:67], s12 offen offset:3072 lds\n"
"s_add_u32 m0, m0, 4096\n"
"s_add_u32 s12, s12, 128\n"
"buffer_load_dwordx4 v8, s[64:67], s12 offen offset:0 lds\n"
"buffer_load_dwordx4 v9, s[64:67], s12 offen offset:1024 lds\n"
"buffer_load_dwordx4 v10, s[64:67], s12 offen offset:2048 lds\n"
"buffer_load_dwordx4 v11, s[64:67], s12 offen offset:3072 lds\n"
"s_add_u32 m0, m0, 4096\n"
"s_add_u32 s12, s12, 128\n"
"buffer_load_dwordx4 v8, s[64:67], s12 offen offset:0 lds\n"
"buffer_load_dwordx4 v9, s[64:67], s12 offen offset:1024 lds\n"
"buffer_load_dwordx4 v10, s[64:67], s12 offen offset:2048 lds\n"
"buffer_load_dwordx4 v11, s[64:67], s12 offen offset:3072 lds\n"
".Lj1_%=:\n"
"v_and_b32 v18, 63, %[tid]\n"
"v_mov_b32 v19, v2\n"
"v_lshlrev_b32 v216, 4, v18\n"
"v_and_b32 v28, 15, v18\n"
"v_lshrrev_b32 v27, 4, v18\n"
"v_lshlrev_b32 v25, 3, v28\n"
"s_add_u32 s6, %[lb], 156672\n"
"v_add_u32 v23, s6, v25\n"
"s_lshl_b32 s7, s4, 7\n"
"s_add_u32 s6, s6, s7\n"
"v_lshl_add_u32 v22, v19, 5, s6\n"
"v_lshl_add_u32 v22, v27, 3, v22\n"
"s_cmp_eq_u32 s4, 0\n"
"s_cselect_b32 s7, 0, 1073741824\n"
"s_lshl_b32 s6, s31, 7\n"
"s_add_u32 s20, s6, s7\n"
"s_add_u32 s18, s20, 20736\n"
"s_lshr_b32 s19, s20, 1\n"
"s_add_u32 s19, s19, 13824\n"
"s_cmp_ge_u32 s31, 54\n"
"s_cselect_b32 s6, 72, 18\n"
"s_sub_u32 s6, s31, s6\n"
"s_cmp_lt_u32 s6, 18\n"
"s_cselect_b32 s26, 1.0, 0\n"
"v_mov_b32 v38, 0\n"
"v_bfe_u32 v3, v18, 1, 3\n"
"v_xor_b32 v3, v3, v27\n"
"v_lshl_add_u32 v20, v28, 7, s11\n"
"v_lshl_add_u32 v20, v3, 4, v20\n"
"v_xor_b32 v21, 64, v20\n"
"v_mov_b32 v32, 0x3f803f80\n"
"v_mov_b32 v33, 0x3f803f80\n"
"v_mov_b32 v34, 0x3f803f80\n"
"v_mov_b32 v35, 0x3f803f80\n"
"v_lshlrev_b32 v0, 8, v18\n"
"s_mov_b32 s92, 3355444\n"
"s_movk_i32 s93, 1072\n"
"s_and_b32 s30, s5, 1\n"
"s_xor_b32 s99, s5, 1\n"
"s_and_b32 s7, s99, 1\n"
"s_lshl_b32 s6, s30, 6\n"
"s_lshl_b32 s7, s7, 6\n"
"v_add_u32 v4, s6, v216\n"
"v_add_u32 v5, s7, v216\n"
"v_mov_b32 v188, v0\n"
"v_add_u32 v189, 16384, v0\n"
"v_add_u32 v190, 32768, v0\n"
"v_add_u32 v191, 49152, v0\n"
"v_add_u32 v192, 65536, v0\n"
"v_add_u32 v193, 81920, v0\n"
"v_add_u32 v194, 98304, v0\n"
"v_add_u32 v195, 114688, v0\n"
"v_add_u32 v196, 131072, v0\n"
"v_add_u32 v197, 147456, v0\n"
"v_add_u32 v198, 163840, v0\n"
"v_add_u32 v199, 180224, v0\n"
"v_add_u32 v200, 196608, v0\n"
"v_add_u32 v201, 212992, v0\n"
"v_add_u32 v202, 229376, v0\n"
"v_mul_hi_u32_u24 v188, s92, v188\n"
"v_mul_hi_u32_u24 v189, s92, v189\n"
"v_mul_hi_u32_u24 v190, s92, v190\n"
"v_mul_hi_u32_u24 v191, s92, v191\n"
"v_mul_hi_u32_u24 v192, s92, v192\n"
"v_mul_hi_u32_u24 v193, s92, v193\n"
"v_mul_hi_u32_u24 v194, s92, v194\n"
"v_mul_hi_u32_u24 v195, s92, v195\n"
"v_mul_hi_u32_u24 v196, s92, v196\n"
"v_mul_hi_u32_u24 v197, s92, v197\n"
"v_mul_hi_u32_u24 v198, s92, v198\n"
"v_mul_hi_u32_u24 v199, s92, v199\n"
"v_mul_hi_u32_u24 v200, s92, v200\n"
"v_mul_hi_u32_u24 v201, s92, v201\n"
"v_mul_hi_u32_u24 v202, s92, v202\n"
"s_mov_b32 vcc_lo, 0xc00f801f\n"
"s_mov_b32 vcc_hi, 0xf003e007\n"
"v_cndmask_b32 v6, v4, v5, vcc\n"
"v_mad_u32_u24 v188, v188, s93, v6\n"
"s_mov_b32 vcc_lo, 0x7c00f801\n"
"s_mov_b32 vcc_hi, 0x1f003e00\n"
"v_cndmask_b32 v6, v4, v5, vcc\n"
"v_mad_u32_u24 v189, v189, s93, v6\n"
"s_mov_b32 vcc_lo, 0x7c00f80\n"
"s_mov_b32 vcc_hi, 0x1f003e0\n"
"v_cndmask_b32 v6, v4, v5, vcc\n"
"v_mad_u32_u24 v190, v190, s93, v6\n"
"s_mov_b32 vcc_lo, 0x7c00f8\n"
"s_mov_b32 vcc_hi, 0x801f003e\n"
"v_cndmask_b32 v6, v4, v5, vcc\n"
"v_mad_u32_u24 v191, v191, s93, v6\n"
"s_mov_b32 vcc_lo, 0xe007c00f\n"
"s_mov_b32 vcc_hi, 0xf801f003\n"
"v_cndmask_b32 v6, v4, v5, vcc\n"
"v_mad_u32_u24 v192, v192, s93, v6\n"
"s_mov_b32 vcc_lo, 0x3e007c00\n"
"s_mov_b32 vcc_hi, 0xf801f00\n"
"v_cndmask_b32 v6, v4, v5, vcc\n"
"v_mad_u32_u24 v193, v193, s93, v6\n"
"s_mov_b32 vcc_lo, 0x3e007c0\n"
"s_mov_b32 vcc_hi, 0xf801f0\n"
"v_cndmask_b32 v6, v4, v5, vcc\n"
"v_mad_u32_u24 v194, v194, s93, v6\n"
"s_mov_b32 vcc_lo, 0x3e007c\n"
"s_mov_b32 vcc_hi, 0xc00f801f\n"
"v_cndmask_b32 v6, v4, v5, vcc\n"
"v_mad_u32_u24 v195, v195, s93, v6\n"
"s_mov_b32 vcc_lo, 0xf003e007\n"
"s_mov_b32 vcc_hi, 0x7c00f801\n"
"v_cndmask_b32 v6, v4, v5, vcc\n"
"v_mad_u32_u24 v196, v196, s93, v6\n"
"s_mov_b32 vcc_lo, 0x1f003e00\n"
"s_mov_b32 vcc_hi, 0x7c00f80\n"
"v_cndmask_b32 v6, v4, v5, vcc\n"
"v_mad_u32_u24 v197, v197, s93, v6\n"
"s_mov_b32 vcc_lo, 0x1f003e0\n"
"s_mov_b32 vcc_hi, 0x7c00f8\n"
"v_cndmask_b32 v6, v4, v5, vcc\n"
"v_mad_u32_u24 v198, v198, s93, v6\n"
"s_mov_b32 vcc_lo, 0x801f003e\n"
"s_mov_b32 vcc_hi, 0xe007c00f\n"
"v_cndmask_b32 v6, v4, v5, vcc\n"
"v_mad_u32_u24 v199, v199, s93, v6\n"
"s_mov_b32 vcc_lo, 0xf801f003\n"
"s_mov_b32 vcc_hi, 0x3e007c00\n"
"v_cndmask_b32 v6, v4, v5, vcc\n"
"v_mad_u32_u24 v200, v200, s93, v6\n"
"s_mov_b32 vcc_lo, 0xf801f00\n"
"s_mov_b32 vcc_hi, 0x3e007c0\n"
"v_cndmask_b32 v6, v4, v5, vcc\n"
"v_mad_u32_u24 v201, v201, s93, v6\n"
"s_mov_b32 vcc_lo, 0xf801f0\n"
"s_mov_b32 vcc_hi, 0x3e007c\n"
"v_cndmask_b32 v6, v4, v5, vcc\n"
"v_mad_u32_u24 v202, v202, s93, v6\n"
"s_lshr_b32 s6, s5, 1\n"
"s_mul_i32 s6, s6, 144\n"
"s_mul_i32 s13, s4, 221184\n"
"s_add_u32 s13, s13, s6\n"
"s_mul_i32 s15, s4, 55296\n"
"s_add_u32 s15, s15, s6\n"
"s_add_u32 s15, s15, 1769472\n"
"v_min_u32 v0, 35, v18\n"
"v_lshlrev_b32 v24, 4, v0\n"
"s_mul_i32 s28, s4, 576\n"
"s_lshl2_add_u32 s6, s28, %[lb]\n"
"s_add_u32 s6, s6, 147456\n"
"v_lshl_add_u32 v26, v0, 6, s6\n"
"s_add_u32 s28, s28, 20736\n"
"v_lshlrev_b32 v39, 4, v27\n"
"s_lshl2_add_u32 s21, %[b], 34560\n"
"s_cmp_eq_u32 s4, 3\n"
"s_cselect_b32 s21, s21, 1073741824\n"
"v_mul_u32_u24 v31, 52, v18\n"
"v_lshrrev_b32 v31, 8, v31\n"
"v_mul_u32_u24 v31, 512, v31\n"
"v_lshl_add_u32 v31, v18, 7, v31\n"
"s_cmp_lt_u32 s31, 108\n"
"s_cbranch_scc0 .Ln2_%=\n"
"s_waitcnt vmcnt(11)\n"
"v_lshlrev_b32 v94, 16, v94\n"
"s_waitcnt vmcnt(10)\n"
"v_lshlrev_b32 v92, 16, v92\n"
"s_waitcnt vmcnt(9)\n"
"v_lshlrev_b32 v93, 16, v93\n"
"v_mul_f32 v97, 0xbfb8aa3b, v94\n"
"v_mul_f32 v95, 0xbfb8aa3b, v92\n"
"v_mul_f32 v96, 0xbfb8aa3b, v93\n"
"v_exp_f32 v97, v97\n"
"v_exp_f32 v95, v95\n"
"v_exp_f32 v96, v96\n"
"v_add_f32 v97, 1.0, v97\n"
"v_add_f32 v95, 1.0, v95\n"
"v_add_f32 v96, 1.0, v96\n"
"v_rcp_f32 v97, v97\n"
"v_rcp_f32 v95, v95\n"
"v_rcp_f32 v96, v96\n"
"v_mul_f32 v97, v97, v94\n"
"v_mul_f32 v95, v95, v92\n"
"v_mul_f32 v96, v96, v93\n"
"v_cvt_pk_bf16_f32 v97, v97, v38\n"
"v_cvt_pk_bf16_f32 v95, v95, v38\n"
"v_cvt_pk_bf16_f32 v96, v96, v38\n"
"v_lshlrev_b32 v100, 16, v97\n"
"v_lshlrev_b32 v98, 16, v95\n"
"v_lshlrev_b32 v99, 16, v96\n"
"v_readfirstlane_b32 s16, v17\n"
"s_add_u32 s16, s16, 0x9e3779b9\n"
"s_waitcnt vmcnt(8)\n"
"v_dot2_f32_bf16 v0, v88, v97, 0\n"
"v_dot2_f32_bf16 v1, v88, v100, 0\n"
"v_dot2_f32_bf16 v2, v89, v97, 0\n"
"v_dot2_f32_bf16 v3, v89, v100, 0\n"
"v_dot2_f32_bf16 v4, v90, v97, 0\n"
"v_dot2_f32_bf16 v5, v90, v100, 0\n"
"v_dot2_f32_bf16 v6, v91, v97, 0\n"
"v_dot2_f32_bf16 v7, v91, v100, 0\n"
"buffer_load_dwordx4 v8, s[64:67], s12 offen offset:0 lds\n"
"s_waitcnt vmcnt(8)\n"
"v_dot2c_f32_bf16_dpp v0, v95, v56 quad_perm:[0,0,0,0] row_mask:0xf bank_mask:0xf\n"
"v_dot2c_f32_bf16_dpp v1, v98, v56 quad_perm:[0,0,0,0] row_mask:0xf bank_mask:0xf\n"
"v_dot2c_f32_bf16_dpp v2, v95, v57 quad_perm:[0,0,0,0] row_mask:0xf bank_mask:0xf\n"
"v_dot2c_f32_bf16_dpp v3, v98, v57 quad_perm:[0,0,0,0] row_mask:0xf bank_mask:0xf\n"
"v_dot2c_f32_bf16_dpp v4, v95, v58 quad_perm:[0,0,0,0] row_mask:0xf bank_mask:0xf\n"
"v_dot2c_f32_bf16_dpp v5, v98, v58 quad_perm:[0,0,0,0] row_mask:0xf bank_mask:0xf\n"
"v_dot2c_f32_bf16_dpp v6, v95, v59 quad_perm:[0,0,0,0] row_mask:0xf bank_mask:0xf\n"
"v_dot2c_f32_bf16_dpp v7, v98, v59 quad_perm:[0,0,0,0] row_mask:0xf bank_mask:0xf\n"
"buffer_load_dwordx4 v9, s[64:67], s12 offen offset:1024 lds\n"
"s_waitcnt vmcnt(8)\n"
"v_dot2c_f32_bf16_dpp v0, v95, v60 quad_perm:[1,1,1,1] row_mask:0xf bank_mask:0xf\n"
"v_dot2c_f32_bf16_dpp v1, v98, v60 quad_perm:[1,1,1,1] row_mask:0xf bank_mask:0xf\n"
"v_dot2c_f32_bf16_dpp v2, v95, v61 quad_perm:[1,1,1,1] row_mask:0xf bank_mask:0xf\n"
"v_dot2c_f32_bf16_dpp v3, v98, v61 quad_perm:[1,1,1,1] row_mask:0xf bank_mask:0xf\n"
"v_dot2c_f32_bf16_dpp v4, v95, v62 quad_perm:[1,1,1,1] row_mask:0xf bank_mask:0xf\n"
"v_dot2c_f32_bf16_dpp v5, v98, v62 quad_perm:[1,1,1,1] row_mask:0xf bank_mask:0xf\n"
"v_dot2c_f32_bf16_dpp v6, v95, v63 quad_perm:[1,1,1,1] row_mask:0xf bank_mask:0xf\n"
"v_dot2c_f32_bf16_dpp v7, v98, v63 quad_perm:[1,1,1,1] row_mask:0xf bank_mask:0xf\n"
"buffer_load_dwordx4 v10, s[64:67], s12 offen offset:2048 lds\n"
"s_waitcnt vmcnt(8)\n"
"v_dot2c_f32_bf16_dpp v0, v95, v64 quad_perm:[2,2,2,2] row_mask:0xf bank_mask:0xf\n"
"v_dot2c_f32_bf16_dpp v1, v98, v64 quad_perm:[2,2,2,2] row_mask:0xf bank_mask:0xf\n"
"v_dot2c_f32_bf16_dpp v2, v95, v65 quad_perm:[2,2,2,2] row_mask:0xf bank_mask:0xf\n"
"v_dot2c_f32_bf16_dpp v3, v98, v65 quad_perm:[2,2,2,2] row_mask:0xf bank_mask:0xf\n"
"v_dot2c_f32_bf16_dpp v4, v95, v66 quad_perm:[2,2,2,2] row_mask:0xf bank_mask:0xf\n"
"v_dot2c_f32_bf16_dpp v5, v98, v66 quad_perm:[2,2,2,2] row_mask:0xf bank_mask:0xf\n"
"v_dot2c_f32_bf16_dpp v6, v95, v67 quad_perm:[2,2,2,2] row_mask:0xf bank_mask:0xf\n"
"v_dot2c_f32_bf16_dpp v7, v98, v67 quad_perm:[2,2,2,2] row_mask:0xf bank_mask:0xf\n"
"buffer_load_dwordx4 v11, s[64:67], s12 offen offset:3072 lds\n"
"s_waitcnt vmcnt(8)\n"
"v_dot2c_f32_bf16_dpp v0, v95, v68 quad_perm:[3,3,3,3] row_mask:0xf bank_mask:0xf\n"
"v_dot2c_f32_bf16_dpp v1, v98, v68 quad_perm:[3,3,3,3] row_mask:0xf bank_mask:0xf\n"
"v_dot2c_f32_bf16_dpp v2, v95, v69 quad_perm:[3,3,3,3] row_mask:0xf bank_mask:0xf\n"
"v_dot2c_f32_bf16_dpp v3, v98, v69 quad_perm:[3,3,3,3] row_mask:0xf bank_mask:0xf\n"
"v_dot2c_f32_bf16_dpp v4, v95, v70 quad_perm:[3,3,3,3] row_mask:0xf bank_mask:0xf\n"
"v_dot2c_f32_bf16_dpp v5, v98, v70 quad_perm:[3,3,3,3] row_mask:0xf bank_mask:0xf\n"
"v_dot2c_f32_bf16_dpp v6, v95, v71 quad_perm:[3,3,3,3] row_mask:0xf bank_mask:0xf\n"
"v_dot2c_f32_bf16_dpp v7, v98, v71 quad_perm:[3,3,3,3] row_mask:0xf bank_mask:0xf\n"
"s_add_u32 m0, m0, 4096\n"
"s_add_u32 s12, s12, 128\n"
"buffer_load_dwordx4 v8, s[64:67], s12 offen offset:0 lds\n"
"s_waitcnt vmcnt(8)\n"
"v_dot2c_f32_bf16_dpp v0, v96, v72 quad_perm:[0,0,0,0] row_mask:0xf bank_mask:0xf\n"
"v_dot2c_f32_bf16_dpp v1, v99, v72 quad_perm:[0,0,0,0] row_mask:0xf bank_mask:0xf\n"
"v_dot2c_f32_bf16_dpp v2, v96, v73 quad_perm:[0,0,0,0] row_mask:0xf bank_mask:0xf\n"
"v_dot2c_f32_bf16_dpp v3, v99, v73 quad_perm:[0,0,0,0] row_mask:0xf bank_mask:0xf\n"
"v_dot2c_f32_bf16_dpp v4, v96, v74 quad_perm:[0,0,0,0] row_mask:0xf bank_mask:0xf\n"
"v_dot2c_f32_bf16_dpp v5, v99, v74 quad_perm:[0,0,0,0] row_mask:0xf bank_mask:0xf\n"
"v_dot2c_f32_bf16_dpp v6, v96, v75 quad_perm:[0,0,0,0] row_mask:0xf bank_mask:0xf\n"
"v_dot2c_f32_bf16_dpp v7, v99, v75 quad_perm:[0,0,0,0] row_mask:0xf bank_mask:0xf\n"
"buffer_load_dwordx4 v9, s[64:67], s12 offen offset:1024 lds\n"
"s_waitcnt vmcnt(8)\n"
"v_dot2c_f32_bf16_dpp v0, v96, v76 quad_perm:[1,1,1,1] row_mask:0xf bank_mask:0xf\n"
"v_dot2c_f32_bf16_dpp v1, v99, v76 quad_perm:[1,1,1,1] row_mask:0xf bank_mask:0xf\n"
"v_dot2c_f32_bf16_dpp v2, v96, v77 quad_perm:[1,1,1,1] row_mask:0xf bank_mask:0xf\n"
"v_dot2c_f32_bf16_dpp v3, v99, v77 quad_perm:[1,1,1,1] row_mask:0xf bank_mask:0xf\n"
"v_dot2c_f32_bf16_dpp v4, v96, v78 quad_perm:[1,1,1,1] row_mask:0xf bank_mask:0xf\n"
"v_dot2c_f32_bf16_dpp v5, v99, v78 quad_perm:[1,1,1,1] row_mask:0xf bank_mask:0xf\n"
"v_dot2c_f32_bf16_dpp v6, v96, v79 quad_perm:[1,1,1,1] row_mask:0xf bank_mask:0xf\n"
"v_dot2c_f32_bf16_dpp v7, v99, v79 quad_perm:[1,1,1,1] row_mask:0xf bank_mask:0xf\n"
"buffer_load_dwordx4 v10, s[64:67], s12 offen offset:2048 lds\n"
"s_waitcnt vmcnt(8)\n"
"v_dot2c_f32_bf16_dpp v0, v96, v80 quad_perm:[2,2,2,2] row_mask:0xf bank_mask:0xf\n"
"v_dot2c_f32_bf16_dpp v1, v99, v80 quad_perm:[2,2,2,2] row_mask:0xf bank_mask:0xf\n"
"v_dot2c_f32_bf16_dpp v2, v96, v81 quad_perm:[2,2,2,2] row_mask:0xf bank_mask:0xf\n"
"v_dot2c_f32_bf16_dpp v3, v99, v81 quad_perm:[2,2,2,2] row_mask:0xf bank_mask:0xf\n"
"v_dot2c_f32_bf16_dpp v4, v96, v82 quad_perm:[2,2,2,2] row_mask:0xf bank_mask:0xf\n"
"v_dot2c_f32_bf16_dpp v5, v99, v82 quad_perm:[2,2,2,2] row_mask:0xf bank_mask:0xf\n"
"v_dot2c_f32_bf16_dpp v6, v96, v83 quad_perm:[2,2,2,2] row_mask:0xf bank_mask:0xf\n"
"v_dot2c_f32_bf16_dpp v7, v99, v83 quad_perm:[2,2,2,2] row_mask:0xf bank_mask:0xf\n"
"buffer_load_dwordx4 v11, s[64:67], s12 offen offset:3072 lds\n"
"s_waitcnt vmcnt(8)\n"
"v_dot2c_f32_bf16_dpp v0, v96, v84 quad_perm:[3,3,3,3] row_mask:0xf bank_mask:0xf\n"
"v_dot2c_f32_bf16_dpp v1, v99, v84 quad_perm:[3,3,3,3] row_mask:0xf bank_mask:0xf\n"
"v_dot2c_f32_bf16_dpp v2, v96, v85 quad_perm:[3,3,3,3] row_mask:0xf bank_mask:0xf\n"
"v_dot2c_f32_bf16_dpp v3, v99, v85 quad_perm:[3,3,3,3] row_mask:0xf bank_mask:0xf\n"
"v_dot2c_f32_bf16_dpp v4, v96, v86 quad_perm:[3,3,3,3] row_mask:0xf bank_mask:0xf\n"
"v_dot2c_f32_bf16_dpp v5, v99, v86 quad_perm:[3,3,3,3] row_mask:0xf bank_mask:0xf\n"
"v_dot2c_f32_bf16_dpp v6, v96, v87 quad_perm:[3,3,3,3] row_mask:0xf bank_mask:0xf\n"
"v_dot2c_f32_bf16_dpp v7, v99, v87 quad_perm:[3,3,3,3] row_mask:0xf bank_mask:0xf\n"
"s_add_u32 m0, m0, 4096\n"
"s_add_u32 s12, s12, 128\n"
"buffer_load_dwordx4 v8, s[64:67], s12 offen offset:0 lds\n"
"v_mov_b32 v13, s16\n"
"v_lshrrev_b32 v14, 1, v25\n"
"v_permlane32_swap_b32 v0, v4\n"
"v_permlane32_swap_b32 v1, v5\n"
"v_permlane32_swap_b32 v2, v6\n"
"v_permlane32_swap_b32 v3, v7\n"
"v_add_f32 v0, v0, v4\n"
"v_add_f32 v1, v1, v5\n"
"buffer_load_dwordx4 v9, s[64:67], s12 offen offset:1024 lds\n"
"v_add_f32 v2, v2, v6\n"
"v_add_f32 v3, v3, v7\n"
"s_nop 0\n"
"v_permlane16_swap_b32 v0, v2\n"
"v_permlane16_swap_b32 v1, v3\n"
"v_add_f32 v0, v0, v2\n"
"v_add_f32 v1, v1, v3\n"
"s_nop 0\n"
"v_add_f32_dpp v0, v0, v0 row_ror:8 row_mask:0xf bank_mask:0xf\n"
"v_add_f32_dpp v1, v1, v1 row_ror:8 row_mask:0xf bank_mask:0xf\n"
"buffer_load_dwordx4 v10, s[64:67], s12 offen offset:2048 lds\n"
"v_add_f32_dpp v0, v0, v0 row_ror:4 row_mask:0xf bank_mask:0xf\n"
"v_add_f32_dpp v1, v1, v1 row_ror:4 row_mask:0xf bank_mask:0xf\n"
"ds_write_b64 v22, v[0:1]\n"
"s_waitcnt lgkmcnt(0)\n"
"s_barrier\n"
"ds_read2_b64 v[0:3], v23 offset0:0 offset1:16\n"
"ds_read2_b64 v[4:7], v23 offset0:32 offset1:48\n"
"s_waitcnt lgkmcnt(1)\n"
"v_add_f32 v0, v0, v2\n"
"v_add_f32 v1, v1, v3\n"
"buffer_load_dwordx4 v11, s[64:67], s12 offen offset:3072 lds\n"
"s_waitcnt lgkmcnt(0)\n"
"v_add_f32 v4, v4, v6\n"
"v_add_f32 v5, v5, v7\n"
"v_add_f32 v0, v0, v4\n"
"v_add_f32 v1, v1, v5\n"
"v_cvt_pk_bf16_f32 v2, v0, v1\n"
"v_lshlrev_b32 v0, 16, v2\n"
"v_and_b32 v1, 0xffff0000, v2\n"
"v_add_f32 v2, s26, v0\n"
"s_add_u32 m0, m0, 4096\n"
"s_add_u32 s12, s12, 128\n"
"buffer_load_dwordx4 v8, s[64:67], s12 offen offset:0 lds\n"
"v_add_f32 v3, s26, v1\n"
"v_cvt_pk_bf16_f32 v12, v2, v3\n"
"buffer_store_dwordx2 v[12:13], v25, s[80:83], s18 offen sc1\n"
"buffer_store_dword v12, v14, s[80:83], s19 offen\n"
"buffer_store_dwordx2 v[0:1], v25, s[80:83], s20 offen\n"
"buffer_load_dwordx4 v9, s[64:67], s12 offen offset:1024 lds\n"
"buffer_load_dwordx4 v10, s[64:67], s12 offen offset:2048 lds\n"
"buffer_load_dwordx4 v11, s[64:67], s12 offen offset:3072 lds\n"
"s_add_u32 m0, m0, 4096\n"
"s_add_u32 s12, s12, 128\n"
"buffer_load_dwordx4 v8, s[64:67], s12 offen offset:0 lds\n"
"buffer_load_dwordx4 v9, s[64:67], s12 offen offset:1024 lds\n"
"buffer_load_dwordx4 v10, s[64:67], s12 offen offset:2048 lds\n"
"buffer_load_dwordx4 v11, s[64:67], s12 offen offset:3072 lds\n"
"s_add_u32 m0, m0, 4096\n"
"s_add_u32 s12, s12, 128\n"
"buffer_load_dwordx4 v8, s[64:67], s12 offen offset:0 lds\n"
"buffer_load_dwordx4 v9, s[64:67], s12 offen offset:1024 lds\n"
"buffer_load_dwordx4 v10, s[64:67], s12 offen offset:2048 lds\n"
"buffer_load_dwordx4 v11, s[64:67], s12 offen offset:3072 lds\n"
"s_add_u32 m0, m0, 4096\n"
"s_add_u32 s12, s12, 128\n"
"buffer_load_dwordx4 v8, s[64:67], s12 offen offset:0 lds\n"
"buffer_load_dwordx4 v9, s[64:67], s12 offen offset:1024 lds\n"
"buffer_load_dwordx4 v10, s[64:67], s12 offen offset:2048 lds\n"
"buffer_load_dwordx4 v11, s[64:67], s12 offen offset:3072 lds\n"
"s_add_u32 m0, m0, 4096\n"
"s_add_u32 s12, s12, 128\n"
"buffer_load_dwordx4 v8, s[64:67], s12 offen offset:0 lds\n"
"buffer_load_dwordx4 v9, s[64:67], s12 offen offset:1024 lds\n"
"buffer_load_dwordx4 v10, s[64:67], s12 offen offset:2048 lds\n"
"buffer_load_dwordx4 v11, s[64:67], s12 offen offset:3072 lds\n"
"s_add_u32 m0, m0, 4096\n"
"s_add_u32 s12, s12, 128\n"
"buffer_load_dwordx4 v8, s[64:67], s12 offen offset:0 lds\n"
"buffer_load_dwordx4 v9, s[64:67], s12 offen offset:1024 lds\n"
"buffer_load_dwordx4 v10, s[64:67], s12 offen offset:2048 lds\n"
"buffer_load_dwordx4 v11, s[64:67], s12 offen offset:3072 lds\n"
"s_branch .Lj2_%=\n"
".Ln2_%=:\n"
"s_add_u32 m0, m0, 4096\n"
"s_add_u32 s12, s12, 128\n"
"buffer_load_dwordx4 v8, s[64:67], s12 offen offset:0 lds\n"
"buffer_load_dwordx4 v9, s[64:67], s12 offen offset:1024 lds\n"
"buffer_load_dwordx4 v10, s[64:67], s12 offen offset:2048 lds\n"
"buffer_load_dwordx4 v11, s[64:67], s12 offen offset:3072 lds\n"
"s_add_u32 m0, m0, 4096\n"
"s_add_u32 s12, s12, 128\n"
"buffer_load_dwordx4 v8, s[64:67], s12 offen offset:0 lds\n"
"buffer_load_dwordx4 v9, s[64:67], s12 offen offset:1024 lds\n"
"buffer_load_dwordx4 v10, s[64:67], s12 offen offset:2048 lds\n"
"buffer_load_dwordx4 v11, s[64:67], s12 offen offset:3072 lds\n"
"s_add_u32 m0, m0, 4096\n"
"s_add_u32 s12, s12, 128\n"
"buffer_load_dwordx4 v8, s[64:67], s12 offen offset:0 lds\n"
"buffer_load_dwordx4 v9, s[64:67], s12 offen offset:1024 lds\n"
"buffer_load_dwordx4 v10, s[64:67], s12 offen offset:2048 lds\n"
"buffer_load_dwordx4 v11, s[64:67], s12 offen offset:3072 lds\n"
"s_add_u32 m0, m0, 4096\n"
"s_add_u32 s12, s12, 128\n"
"buffer_load_dwordx4 v8, s[64:67], s12 offen offset:0 lds\n"
"buffer_load_dwordx4 v9, s[64:67], s12 offen offset:1024 lds\n"
"buffer_load_dwordx4 v10, s[64:67], s12 offen offset:2048 lds\n"
"buffer_load_dwordx4 v11, s[64:67], s12 offen offset:3072 lds\n"
"s_add_u32 m0, m0, 4096\n"
"s_add_u32 s12, s12, 128\n"
"buffer_load_dwordx4 v8, s[64:67], s12 offen offset:0 lds\n"
"buffer_load_dwordx4 v9, s[64:67], s12 offen offset:1024 lds\n"
"buffer_load_dwordx4 v10, s[64:67], s12 offen offset:2048 lds\n"
"buffer_load_dwordx4 v11, s[64:67], s12 offen offset:3072 lds\n"
"s_add_u32 m0, m0, 4096\n"
"s_add_u32 s12, s12, 128\n"
"buffer_load_dwordx4 v8, s[64:67], s12 offen offset:0 lds\n"
"buffer_load_dwordx4 v9, s[64:67], s12 offen offset:1024 lds\n"
"buffer_load_dwordx4 v10, s[64:67], s12 offen offset:2048 lds\n"
"buffer_load_dwordx4 v11, s[64:67], s12 offen offset:3072 lds\n"
".Lj2_%=:\n"
"s_waitcnt vmcnt(36)\n"
"v_readfirstlane_b32 s16, v17\n"
"s_add_u32 s16, s16, 0x9e3779b9\n"
"s_nop 0\n"
"v_mov_b32 v13, s16\n"
"buffer_load_dword a201, v31, s[68:71], s98 offen\n"
"buffer_store_dword v13, off, s[80:83], s21 sc1\n"
"s_waitcnt vmcnt(34)\n"
"ds_read_b128 a[0:3], v20 offset:0\n"
"ds_read_b128 a[4:7], v20 offset:2048\n"
"ds_read_b128 a[8:11], v21 offset:0\n"
"ds_read_b128 a[12:15], v21 offset:2048\n"
"s_waitcnt vmcnt(30)\n"
"ds_read_b128 a[16:19], v20 offset:4096\n"
"ds_read_b128 a[20:23], v20 offset:6144\n"
"ds_read_b128 a[24:27], v21 offset:4096\n"
"ds_read_b128 a[28:31], v21 offset:6144\n"
"s_waitcnt lgkmcnt(4)\n"
"v_mfma_f32_16x16x32_bf16 v[40:43], v[32:35], a[0:3], 0\n"
"v_mfma_f32_16x16x32_bf16 v[48:51], a[0:3], a[0:3], 0\n"
"s_add_u32 m0, s11, 0\n"
"s_mov_b32 s14, s13\n"
"buffer_load_dwordx4 v188, s[68:71], s14 offen offset:0 lds\n"
"v_mfma_f32_16x16x32_bf16 v[44:47], v[32:35], a[4:7], 0\n"
"v_mfma_f32_16x16x32_bf16 v[52:55], a[4:7], a[4:7], 0\n"
"buffer_load_dwordx4 v189, s[68:71], s14 offen offset:1024 lds\n"
"v_mfma_f32_16x16x32_bf16 v[40:43], v[32:35], a[8:11], v[40:43]\n"
"v_mfma_f32_16x16x32_bf16 v[48:51], a[8:11], a[8:11], v[48:51]\n"
"buffer_load_dwordx4 v190, s[68:71], s14 offen offset:2048 lds\n"
"v_mfma_f32_16x16x32_bf16 v[44:47], v[32:35], a[12:15], v[44:47]\n"
"v_mfma_f32_16x16x32_bf16 v[52:55], a[12:15], a[12:15], v[52:55]\n"
"buffer_load_dwordx4 v191, s[68:71], s14 offen offset:3072 lds\n"
"s_waitcnt vmcnt(30)\n"
"ds_read_b128 a[32:35], v20 offset:8192\n"
"ds_read_b128 a[36:39], v20 offset:10240\n"
"ds_read_b128 a[40:43], v21 offset:8192\n"
"ds_read_b128 a[44:47], v21 offset:10240\n"
"s_waitcnt lgkmcnt(4)\n"
"v_mfma_f32_16x16x32_bf16 v[40:43], v[32:35], a[16:19], v[40:43]\n"
"v_mfma_f32_16x16x32_bf16 v[48:51], a[16:19], a[16:19], v[48:51]\n"
"s_add_u32 m0, m0, 4096\n"
"s_add_u32 s14, s14, 4096\n"
"buffer_load_dwordx4 v192, s[68:71], s14 offen offset:0 lds\n"
"v_mfma_f32_16x16x32_bf16 v[44:47], v[32:35], a[20:23], v[44:47]\n"
"v_mfma_f32_16x16x32_bf16 v[52:55], a[20:23], a[20:23], v[52:55]\n"
"buffer_load_dwordx4 v193, s[68:71], s14 offen offset:1024 lds\n"
"v_mfma_f32_16x16x32_bf16 v[40:43], v[32:35], a[24:27], v[40:43]\n"
"v_mfma_f32_16x16x32_bf16 v[48:51], a[24:27], a[24:27], v[48:51]\n"
"v_mfma_f32_16x16x32_bf16 v[44:47], v[32:35], a[28:31], v[44:47]\n"
"v_mfma_f32_16x16x32_bf16 v[52:55], a[28:31], a[28:31], v[52:55]\n"
"s_waitcnt vmcnt(28)\n"
"ds_read_b128 a[48:51], v20 offset:12288\n"
"ds_read_b128 a[52:55], v20 offset:14336\n"
"ds_read_b128 a[56:59], v21 offset:12288\n"
"ds_read_b128 a[60:63], v21 offset:14336\n"
"s_waitcnt lgkmcnt(4)\n"
"v_mfma_f32_16x16x32_bf16 v[40:43], v[32:35], a[32:35], v[40:43]\n"
"v_mfma_f32_16x16x32_bf16 v[48:51], a[32:35], a[32:35], v[48:51]\n"
"v_mfma_f32_16x16x32_bf16 v[44:47], v[32:35], a[36:39], v[44:47]\n"
"v_mfma_f32_16x16x32_bf16 v[52:55], a[36:39], a[36:39], v[52:55]\n"
"v_mfma_f32_16x16x32_bf16 v[40:43], v[32:35], a[40:43], v[40:43]\n"
"v_mfma_f32_16x16x32_bf16 v[48:51], a[40:43], a[40:43], v[48:51]\n"
"v_mfma_f32_16x16x32_bf16 v[44:47], v[32:35], a[44:47], v[44:47]\n"
"v_mfma_f32_16x16x32_bf16 v[52:55], a[44:47], a[44:47], v[52:55]\n"
"s_waitcnt vmcnt(24)\n"
"ds_read_b128 a[64:67], v20 offset:16384\n"
"ds_read_b128 a[68:71], v20 offset:18432\n"
"ds_read_b128 a[72:75], v21 offset:16384\n"
"ds_read_b128 a[76:79], v21 offset:18432\n"
"s_waitcnt lgkmcnt(4)\n"
"v_mfma_f32_16x16x32_bf16 v[40:43], v[32:35], a[48:51], v[40:43]\n"
"v_mfma_f32_16x16x32_bf16 v[48:51], a[48:51], a[48:51], v[48:51]\n"
"v_mfma_f32_16x16x32_bf16 v[44:47], v[32:35], a[52:55], v[44:47]\n"
"v_mfma_f32_16x16x32_bf16 v[52:55], a[52:55], a[52:55], v[52:55]\n"
"v_mfma_f32_16x16x32_bf16 v[40:43], v[32:35], a[56:59], v[40:43]\n"
"v_mfma_f32_16x16x32_bf16 v[48:51], a[56:59], a[56:59], v[48:51]\n"
"v_mfma_f32_16x16x32_bf16 v[44:47], v[32:35], a[60:63], v[44:47]\n"
"v_mfma_f32_16x16x32_bf16 v[52:55], a[60:63], a[60:63], v[52:55]\n"
"s_waitcnt vmcnt(20)\n"
"ds_read_b128 a[80:83], v20 offset:20480\n"
"ds_read_b128 a[84:87], v20 offset:22528\n"
"ds_read_b128 a[88:91], v21 offset:20480\n"
"ds_read_b128 a[92:95], v21 offset:22528\n"
"s_waitcnt lgkmcnt(4)\n"
"v_mfma_f32_16x16x32_bf16 v[40:43], v[32:35], a[64:67], v[40:43]\n"
"v_mfma_f32_16x16x32_bf16 v[48:51], a[64:67], a[64:67], v[48:51]\n"
"v_mfma_f32_16x16x32_bf16 v[44:47], v[32:35], a[68:71], v[44:47]\n"
"v_mfma_f32_16x16x32_bf16 v[52:55], a[68:71], a[68:71], v[52:55]\n"
"v_mfma_f32_16x16x32_bf16 v[40:43], v[32:35], a[72:75], v[40:43]\n"
"v_mfma_f32_16x16x32_bf16 v[48:51], a[72:75], a[72:75], v[48:51]\n"
"v_mfma_f32_16x16x32_bf16 v[44:47], v[32:35], a[76:79], v[44:47]\n"
"v_mfma_f32_16x16x32_bf16 v[52:55], a[76:79], a[76:79], v[52:55]\n"
"s_waitcnt vmcnt(16)\n"
"ds_read_b128 a[96:99], v20 offset:24576\n"
"ds_read_b128 a[100:103], v20 offset:26624\n"
"ds_read_b128 a[104:107], v21 offset:24576\n"
"ds_read_b128 a[108:111], v21 offset:26624\n"
"s_waitcnt lgkmcnt(4)\n"
"v_mfma_f32_16x16x32_bf16 v[40:43], v[32:35], a[80:83], v[40:43]\n"
"v_mfma_f32_16x16x32_bf16 v[48:51], a[80:83], a[80:83], v[48:51]\n"
"v_mfma_f32_16x16x32_bf16 v[44:47], v[32:35], a[84:87], v[44:47]\n"
"v_mfma_f32_16x16x32_bf16 v[52:55], a[84:87], a[84:87], v[52:55]\n"
"v_mfma_f32_16x16x32_bf16 v[40:43], v[32:35], a[88:91], v[40:43]\n"
"v_mfma_f32_16x16x32_bf16 v[48:51], a[88:91], a[88:91], v[48:51]\n"
"v_mfma_f32_16x16x32_bf16 v[44:47], v[32:35], a[92:95], v[44:47]\n"
"v_mfma_f32_16x16x32_bf16 v[52:55], a[92:95], a[92:95], v[52:55]\n"
"s_waitcnt vmcnt(12)\n"
"ds_read_b128 a[112:115], v20 offset:28672\n"
"ds_read_b128 a[116:119], v20 offset:30720\n"
"ds_read_b128 a[120:123], v21 offset:28672\n"
"ds_read_b128 a[124:127], v21 offset:30720\n"
"s_waitcnt lgkmcnt(4)\n"
"v_mfma_f32_16x16x32_bf16 v[40:43], v[32:35], a[96:99], v[40:43]\n"
"v_mfma_f32_16x16x32_bf16 v[48:51], a[96:99], a[96:99], v[48:51]\n"
"v_mfma_f32_16x16x32_bf16 v[44:47], v[32:35], a[100:103], v[44:47]\n"
"v_mfma_f32_16x16x32_bf16 v[52:55], a[100:103], a[100:103], v[52:55]\n"
"v_mfma_f32_16x16x32_bf16 v[40:43], v[32:35], a[104:107], v[40:43]\n"
"v_mfma_f32_16x16x32_bf16 v[48:51], a[104:107], a[104:107], v[48:51]\n"
"v_mfma_f32_16x16x32_bf16 v[44:47], v[32:35], a[108:111], v[44:47]\n"
"v_mfma_f32_16x16x32_bf16 v[52:55], a[108:111], a[108:111], v[52:55]\n"
"buffer_load_dwordx4 v[72:75], v24, s[80:83], s28 offen sc1\n"
"buffer_load_dwordx4 v[76:79], v24, s[80:83], s28 offen offset:2304 sc1\n"
"s_waitcnt vmcnt(10)\n"
"ds_read_b128 a[128:131], v20 offset:32768\n"
"ds_read_b128 a[132:135], v20 offset:34816\n"
"ds_read_b128 a[136:139], v21 offset:32768\n"
"ds_read_b128 a[140:143], v21 offset:34816\n"
"s_waitcnt lgkmcnt(4)\n"
"v_mfma_f32_16x16x32_bf16 v[40:43], v[32:35], a[112:115], v[40:43]\n"
"v_mfma_f32_16x16x32_bf16 v[48:51], a[112:115], a[112:115], v[48:51]\n"
"buffer_load_dwordx4 v194, s[68:71], s14 offen offset:2048 lds\n"
"v_mfma_f32_16x16x32_bf16 v[44:47], v[32:35], a[116:119], v[44:47]\n"
"v_mfma_f32_16x16x32_bf16 v[52:55], a[116:119], a[116:119], v[52:55]\n"
"buffer_load_dwordx4 v195, s[68:71], s14 offen offset:3072 lds\n"
"v_mfma_f32_16x16x32_bf16 v[40:43], v[32:35], a[120:123], v[40:43]\n"
"v_mfma_f32_16x16x32_bf16 v[48:51], a[120:123], a[120:123], v[48:51]\n"
"s_add_u32 m0, m0, 4096\n"
"s_add_u32 s14, s14, 4096\n"
"buffer_load_dwordx4 v196, s[68:71], s14 offen offset:0 lds\n"
"v_mfma_f32_16x16x32_bf16 v[44:47], v[32:35], a[124:127], v[44:47]\n"
"v_mfma_f32_16x16x32_bf16 v[52:55], a[124:127], a[124:127], v[52:55]\n"
"buffer_load_dwordx4 v197, s[68:71], s14 offen offset:1024 lds\n"
"s_waitcnt lgkmcnt(0)\n"
"v_mfma_f32_16x16x32_bf16 v[40:43], v[32:35], a[128:131], v[40:43]\n"
"v_mfma_f32_16x16x32_bf16 v[48:51], a[128:131], a[128:131], v[48:51]\n"
"buffer_load_dwordx4 v198, s[68:71], s14 offen offset:2048 lds\n"
"v_mfma_f32_16x16x32_bf16 v[44:47], v[32:35], a[132:135], v[44:47]\n"
"v_mfma_f32_16x16x32_bf16 v[52:55], a[132:135], a[132:135], v[52:55]\n"
"buffer_load_dwordx4 v199, s[68:71], s14 offen offset:3072 lds\n"
"v_mfma_f32_16x16x32_bf16 v[40:43], v[32:35], a[136:139], v[40:43]\n"
"v_mfma_f32_16x16x32_bf16 v[48:51], a[136:139], a[136:139], v[48:51]\n"
"s_add_u32 m0, m0, 4096\n"
"s_add_u32 s14, s14, 4096\n"
"buffer_load_dwordx4 v200, s[68:71], s14 offen offset:0 lds\n"
"v_mfma_f32_16x16x32_bf16 v[44:47], v[32:35], a[140:143], v[44:47]\n"
"v_mfma_f32_16x16x32_bf16 v[52:55], a[140:143], a[140:143], v[52:55]\n"
"buffer_load_dwordx4 v201, s[68:71], s14 offen offset:1024 lds\n"
"s_mov_b32 s94, 0x00100001\n"
"s_mov_b32 s95, 0x10000100\n"
"v_cndmask_b32 v0, 0, v48, s[94:95]\n"
"s_nop 3\n"
"v_cndmask_b32 v1, 0, v52, s[94:95]\n"
"s_lshl_b64 s[96:97], s[94:95], 1\n"
"v_cndmask_b32 v0, v0, v49, s[96:97]\n"
"v_cndmask_b32 v1, v1, v53, s[96:97]\n"
"s_lshl_b64 s[94:95], s[96:97], 1\n"
"v_cndmask_b32 v0, v0, v50, s[94:95]\n"
"v_cndmask_b32 v1, v1, v54, s[94:95]\n"
"s_lshl_b64 s[96:97], s[94:95], 1\n"
"v_cndmask_b32 v0, v0, v51, s[96:97]\n"
"v_cndmask_b32 v1, v1, v55, s[96:97]\n"
"v_mov_b32 v2, v0\n"
"v_mov_b32 v3, v1\n"
"v_mul_f32 v4, 0xbae38e39, v40\n"
"v_mul_f32 v5, 0xbae38e39, v44\n"
"v_permlane32_swap_b32 v0, v2\n"
"v_permlane32_swap_b32 v1, v3\n"
"v_add_f32 v0, v0, v2\n"
"v_add_f32 v1, v1, v3\n"
"v_mov_b32 v2, v0\n"
"v_mov_b32 v3, v1\n"
"v_mul_f32 v6, v4, v4\n"
"v_mul_f32 v7, v5, v5\n"
"v_permlane16_swap_b32 v0, v2\n"
"v_permlane16_swap_b32 v1, v3\n"
"v_add_f32 v0, v0, v2\n"
"v_add_f32 v1, v1, v3\n"
"s_mov_b32 s92, 0x3ae38e39\n"
"v_fma_f32 v0, v0, s92, -v6\n"
"v_fma_f32 v1, v1, s92, -v7\n"
"v_max_f32 v0, 0, v0\n"
"v_max_f32 v1, 0, v1\n"
"v_add_f32 v0, 0x3727c5ac, v0\n"
"v_add_f32 v1, 0x3727c5ac, v1\n"
"v_rsq_f32 v140, v0\n"
"v_rsq_f32 v141, v1\n"
"v_cmp_gt_u32 vcc, 16, v18\n"
"v_mul_f32 v0, v0, v140\n"
"v_mul_f32 v1, v1, v141\n"
"v_mul_f32 v142, 0x3eae1a93, v140\n"
"v_mul_f32 v143, 0x3eae1a93, v141\n"
"v_cvt_pk_bf16_f32 v136, v4, v0\n"
"v_cvt_pk_bf16_f32 v138, v5, v1\n"
"v_cndmask_b32 v152, v140, v142, vcc\n"
"v_cndmask_b32 v153, v141, v143, vcc\n"
"v_lshlrev_b32 v2, 16, v136\n"
"v_and_b32 v6, 0xffff0000, v136\n"
"v_lshlrev_b32 v3, 16, v138\n"
"v_and_b32 v7, 0xffff0000, v138\n"
"v_sub_f32 v2, v4, v2\n"
"v_sub_f32 v0, v0, v6\n"
"v_sub_f32 v3, v5, v3\n"
"v_sub_f32 v1, v1, v7\n"
"v_cvt_pk_bf16_f32 v137, v2, v0\n"
"v_cvt_pk_bf16_f32 v139, v3, v1\n"
"ds_bpermute_b32 v144, v39, v140 offset:0\n"
"ds_bpermute_b32 v145, v39, v140 offset:4\n"
"ds_bpermute_b32 v146, v39, v140 offset:8\n"
"ds_bpermute_b32 v147, v39, v140 offset:12\n"
"ds_bpermute_b32 v148, v39, v141 offset:0\n"
"ds_bpermute_b32 v149, v39, v141 offset:4\n"
"ds_bpermute_b32 v150, v39, v141 offset:8\n"
"ds_bpermute_b32 v151, v39, v141 offset:12\n"
"v_mov_b32 v57, 0\n"
"v_mov_b32 v61, 0\n"
"v_mov_b32 v64, 0\n"
"v_mov_b32 v68, 0\n"
"v_lshrrev_b32 v30, 2, v18\n"
"v_lshrrev_b32 v29, 5, v18\n"
"s_lshl3_add_u32 s6, s30, %[lb]\n"
"s_and_b32 s7, s99, 1\n"
"s_lshl3_add_u32 s7, s7, %[lb]\n"
"v_mul_u32_u24 v80, 480, v30\n"
"v_lshl_add_u32 v81, v19, 3, v80\n"
"v_add_u32 v203, s6, v81\n"
"v_add_u32 v207, s7, v81\n"
"v_min_u32 v81, 2, v19\n"
"buffer_load_dwordx4 v202, s[68:71], s14 offen offset:2048 lds\n"
"v_mad_u32_u24 v80, v81, 40, v80\n"
"v_mad_u32_u24 v80, v81, 40, v80\n"
"v_mov_b32 v81, s6\n"
"v_mov_b32 v82, s7\n"
"v_cmp_eq_u32 vcc, 0, v19\n"
"v_mul_u32_u24 v83, 33024, v29\n"
"v_add_u32 v204, 73728, v203\n"
"v_cndmask_b32 v81, v81, v82, vcc\n"
"v_add3_u32 v211, v80, v81, 32\n"
"v_add_u32 v208, 73728, v207\n"
"v_add_u32 v212, 73728, v211\n"
"v_add_u32 v205, v83, v203\n"
"v_add_u32 v209, v83, v207\n"
"v_add_u32 v213, v83, v211\n"
"v_add_u32 v206, v83, v204\n"
"v_add_u32 v210, v83, v208\n"
"v_add_u32 v214, v83, v212\n"
"s_add_u32 s6, %[lb], 147456\n"
"v_lshl_add_u32 v215, v27, 7, s6\n"
"v_lshl_add_u32 v215, v19, 4, v215\n"
"s_lshl2_add_u32 s6, %[b], s4\n"
"s_cmp_eq_u32 s6, 896\n"
"s_cselect_b32 s22, 34560, 1073741824\n"
"s_cselect_b32 s23, 35584, 1073741824\n"
"s_cselect_b32 s17, -1, 0\n"
"s_cmp_eq_u32 s6, 897\n"
"s_cselect_b32 s27, 36864, 1073741824\n"
"s_mov_b64 s[84:85], s[80:81]\n"
"s_mov_b32 s86, 7917568\n"
"s_mov_b32 s87, 0x20000\n"
"s_lshl_b32 s24, s99, 7\n"
"s_add_u32 s24, s24, s29\n"
"s_lshl_b32 s9, s24, 7\n"
"s_add_u32 s9, s9, 2805760\n"
"s_lshl_b32 s24, s24, 10\n"
"s_add_u32 s24, s24, 708608\n"
"s_mul_i32 s6, s5, 303104\n"
"s_lshr_b32 s7, s29, 5\n"
"s_mul_i32 s7, s7, 75776\n"
"s_add_u32 s6, s6, s7\n"
"s_add_u32 s6, s6, 3067904\n"
"s_and_b32 s7, s29, 31\n"
"s_mul_i32 s25, s7, 2240\n"
"s_add_u32 s25, s25, s6\n"
"s_add_u32 s25, s25, 4096\n"
"s_lshr_b32 s8, s7, 3\n"
"s_lshl_b32 s8, s8, 10\n"
"s_add_u32 s6, s6, s8\n"
"s_bfe_u32 s8, s7, 0x10001\n"
"s_lshl_b32 s8, s8, 9\n"
"s_add_u32 s6, s6, s8\n"
"s_bfe_u32 s8, s7, 0x10002\n"
"s_lshl3_add_u32 s6, s8, s6\n"
"s_and_b32 s8, s7, 1\n"
"s_lshl2_add_u32 s6, s8, s6\n"
"v_and_b32 v80, 31, v18\n"
"v_lshl_add_u32 v219, v80, 2, s9\n"
"v_lshl_add_u32 v81, v80, 4, s6\n"
"v_cmp_gt_u32 vcc, 32, v18\n"
"v_add_u32 v220, 8192, v219\n"
"v_add_u32 v82, 151552, v81\n"
"v_cndmask_b32 v219, v81, v219, vcc\n"
"v_cndmask_b32 v220, v82, v220, vcc\n"
"v_and_b32 v83, 1, v27\n"
"v_lshl_add_u32 v81, v83, 1, v29\n"
"v_mad_u32_u24 v81, v81, 19, v28\n"
"v_lshlrev_b32 v217, 4, v81\n"
"v_and_b32 v81, 1, v30\n"
"v_mul_u32_u24 v81, 151552, v81\n"
"v_mul_u32_u24 v218, 304, v19\n"
"v_add_u32 v81, 1312, v81\n"
"v_add_u32 v218, v218, v81\n"
"v_lshrrev_b32 v81, 3, v28\n"
"v_lshl_add_u32 v81, v83, 1, v81\n"
"v_mul_u32_u24 v81, 304, v81\n"
"v_mul_u32_u24 v221, 151552, v29\n"
"v_and_b32 v82, 7, v18\n"
"v_lshl_add_u32 v81, v82, 1, v81\n"
"v_add_u32 v81, 1280, v81\n"
"v_add_u32 v221, v221, v81\n"
"s_and_b32 s17, s17, s16\n"
"s_mov_b32 s93, 1048576\n"
"s_waitcnt vmcnt(10)\n"
"v_cmp_ne_u32 vcc, s16, v73\n"
"v_cmp_ne_u32 s[94:95], s16, v75\n"
"s_or_b64 vcc, vcc, s[94:95]\n"
"s_waitcnt vmcnt(9)\n"
"v_cmp_ne_u32 s[96:97], s16, v77\n"
"v_cmp_ne_u32 s[94:95], s16, v79\n"
"s_or_b64 vcc, vcc, s[96:97]\n"
"s_or_b64 vcc, vcc, s[94:95]\n"
"s_cbranch_scc0 .Lg1_%=\n"
".Lg0_%=:\n"
"s_sleep 2\n"
"buffer_load_dwordx4 v[72:75], v24, s[80:83], s28 offen sc1\n"
"buffer_load_dwordx4 v[76:79], v24, s[80:83], s28 offen offset:2304 sc1\n"
"s_waitcnt vmcnt(0)\n"
"v_cmp_ne_u32 vcc, s16, v73\n"
"v_cmp_ne_u32 s[94:95], s16, v75\n"
"s_or_b64 vcc, vcc, s[94:95]\n"
"v_cmp_ne_u32 s[96:97], s16, v77\n"
"v_cmp_ne_u32 s[94:95], s16, v79\n"
"s_or_b64 vcc, vcc, s[96:97]\n"
"s_or_b64 vcc, vcc, s[94:95]\n"
"s_cselect_b32 s92, s93, 0\n"
"s_sub_u32 s93, s93, 1\n"
"s_cmp_lg_u32 s92, 0\n"
"s_cbranch_scc1 .Lg0_%=\n"
".Lg1_%=:\n"
"s_mov_b32 s92, 0x05040100\n"
"s_mov_b32 s93, 0x07060302\n"
"v_and_b32 v56, 0xffff, v76\n"
"v_perm_b32 v58, v72, v76, s92\n"
"v_perm_b32 v59, v72, v76, s92\n"
"ds_write_b128 v26, v[56:59] offset:0\n"
"v_and_b32 v60, 0xffff0000, v76\n"
"v_perm_b32 v62, v72, v76, s93\n"
"v_perm_b32 v63, v72, v76, s93\n"
"ds_write_b128 v26, v[60:63] offset:16\n"
"v_and_b32 v65, 0xffff, v78\n"
"v_perm_b32 v66, v74, v78, s92\n"
"v_perm_b32 v67, v74, v78, s92\n"
"ds_write_b128 v26, v[64:67] offset:32\n"
"v_and_b32 v69, 0xffff0000, v78\n"
"v_perm_b32 v70, v74, v78, s93\n"
"v_perm_b32 v71, v74, v78, s93\n"
"ds_write_b128 v26, v[68:71] offset:48\n"
"s_waitcnt lgkmcnt(0)\n"
"s_waitcnt vmcnt(7)\n"
"s_barrier\n"
"ds_read_b128 v[120:123], v215 offset:0\n"
"ds_read_b128 v[124:127], v215 offset:64\n"
"ds_read_b64_tr_b16 a[144:145], v207 offset:0\n"
"ds_read_b64_tr_b16 a[146:147], v207 offset:240\n"
"ds_read_b64_tr_b16 a[148:149], v207 offset:40\n"
"ds_read_b64_tr_b16 a[150:151], v207 offset:280\n"
"ds_read_b64_tr_b16 a[152:153], v203 offset:80\n"
"ds_read_b64_tr_b16 a[154:155], v203 offset:320\n"
"ds_read_b64_tr_b16 a[156:157], v203 offset:120\n"
"ds_read_b64_tr_b16 a[158:159], v203 offset:360\n"
"ds_read_b64_tr_b16 a[160:161], v203 offset:160\n"
"ds_read_b64_tr_b16 a[162:163], v203 offset:400\n"
"ds_read_b64_tr_b16 a[164:165], v203 offset:200\n"
"ds_read_b64_tr_b16 a[166:167], v203 offset:440\n"
"ds_read_b64_tr_b16 a[168:169], v211 offset:0\n"
"ds_read_b64_tr_b16 a[170:171], v211 offset:240\n"
"ds_read_b128 v[128:131], v215 offset:1024\n"
"ds_read_b128 v[132:135], v215 offset:1088\n"
"s_waitcnt lgkmcnt(15)\n"
"v_mfma_f32_4x4x4_16b_bf16 v[104:107], v[122:123], v[136:137], 0\n"
"v_mfma_f32_4x4x4_16b_bf16 v[108:111], v[126:127], v[136:137], 0\n"
"v_mfma_f32_4x4x4_16b_bf16 v[112:115], v[122:123], v[138:139], 0\n"
"v_mfma_f32_4x4x4_16b_bf16 v[116:119], v[126:127], v[138:139], 0\n"
"v_mfma_f32_4x4x4_16b_bf16 v[104:107], v[120:121], a[0:1], v[104:107]\n"
"v_mfma_f32_4x4x4_16b_bf16 v[108:111], v[124:125], a[2:3], v[108:111]\n"
"v_mfma_f32_4x4x4_16b_bf16 v[112:115], v[120:121], a[4:5], v[112:115]\n"
"v_mfma_f32_4x4x4_16b_bf16 v[116:119], v[124:125], a[6:7], v[116:119]\n"
"s_nop 1\n"
"v_cvt_pk_bf16_f32 v56, v104, v106\n"
"v_cvt_pk_bf16_f32 v58, v105, v107\n"
"v_cvt_pk_bf16_f32 v57, v108, v110\n"
"v_cvt_pk_bf16_f32 v59, v109, v111\n"
"v_cvt_pk_bf16_f32 v60, v112, v114\n"
"v_cvt_pk_bf16_f32 v62, v113, v115\n"
"v_cvt_pk_bf16_f32 v61, v116, v118\n"
"v_cvt_pk_bf16_f32 v63, v117, v119\n"
"s_waitcnt lgkmcnt(2)\n"
"v_mfma_f32_16x16x32_bf16 v[0:3], a[144:147], v[56:59], 0\n"
"ds_read_b128 v[120:123], v215 offset:2048\n"
"ds_read_b128 v[124:127], v215 offset:2112\n"
"s_waitcnt lgkmcnt(3)\n"
"v_mfma_f32_4x4x4_16b_bf16 v[104:107], v[130:131], v[136:137], 0\n"
"v_mfma_f32_16x16x32_bf16 v[4:7], a[144:147], v[60:63], 0\n"
"ds_read_b64_tr_b16 a[172:173], v207 offset:36864\n"
"ds_read_b64_tr_b16 a[174:175], v207 offset:37104\n"
"s_waitcnt lgkmcnt(4)\n"
"v_mfma_f32_4x4x4_16b_bf16 v[108:111], v[134:135], v[136:137], 0\n"
"v_mfma_f32_16x16x32_bf16 v[8:11], a[148:151], v[56:59], 0\n"
"ds_read_b64_tr_b16 a[176:177], v207 offset:36904\n"
"ds_read_b64_tr_b16 a[178:179], v207 offset:37144\n"
"v_mfma_f32_4x4x4_16b_bf16 v[104:107], v[128:129], a[16:17], v[104:107]\n"
"v_mfma_f32_16x16x32_bf16 v[12:15], a[148:151], v[60:63], 0\n"
"ds_read_b64_tr_b16 a[180:181], v203 offset:36944\n"
"ds_read_b64_tr_b16 a[182:183], v203 offset:37184\n"
"v_mfma_f32_4x4x4_16b_bf16 v[108:111], v[132:133], a[18:19], v[108:111]\n"
"v_mfma_f32_16x16x32_bf16 v[16:19], a[152:155], v[56:59], 0\n"
"ds_read_b64_tr_b16 a[184:185], v203 offset:36984\n"
"ds_read_b64_tr_b16 a[186:187], v203 offset:37224\n"
"v_mfma_f32_4x4x4_16b_bf16 v[112:115], v[130:131], v[138:139], 0\n"
"v_mfma_f32_16x16x32_bf16 v[20:23], a[152:155], v[60:63], 0\n"
"ds_read_b64_tr_b16 a[188:189], v203 offset:37024\n"
"ds_read_b64_tr_b16 a[190:191], v203 offset:37264\n"
"v_mfma_f32_4x4x4_16b_bf16 v[116:119], v[134:135], v[138:139], 0\n"
"v_mfma_f32_16x16x32_bf16 v[24:27], a[156:159], v[56:59], 0\n"
"ds_read_b64_tr_b16 a[192:193], v203 offset:37064\n"
"ds_read_b64_tr_b16 a[194:195], v203 offset:37304\n"
"v_mfma_f32_4x4x4_16b_bf16 v[112:115], v[128:129], a[20:21], v[112:115]\n"
"v_mfma_f32_16x16x32_bf16 v[28:31], a[156:159], v[60:63], 0\n"
"ds_read_b64_tr_b16 a[196:197], v211 offset:36864\n"
"ds_read_b64_tr_b16 a[198:199], v211 offset:37104\n"
"v_mfma_f32_4x4x4_16b_bf16 v[116:119], v[132:133], a[22:23], v[116:119]\n"
"v_mfma_f32_16x16x32_bf16 v[32:35], v[56:59], a[160:163], 0\n"
"s_add_u32 m0, s11, 15360\n"
"s_add_u32 s14, s13, 884736\n"
"buffer_load_dwordx4 v188, s[68:71], s14 offen offset:0 lds\n"
"v_cvt_pk_bf16_f32 v64, v104, v106\n"
"v_mfma_f32_16x16x32_bf16 v[36:39], v[60:63], a[160:163], 0\n"
"v_cvt_pk_bf16_f32 v66, v105, v107\n"
"v_cvt_pk_bf16_f32 v65, v108, v110\n"
"v_mfma_f32_16x16x32_bf16 v[40:43], v[56:59], a[164:167], 0\n"
"buffer_load_dwordx4 v189, s[68:71], s14 offen offset:1024 lds\n"
"v_cvt_pk_bf16_f32 v67, v109, v111\n"
"v_mfma_f32_16x16x32_bf16 v[44:47], v[60:63], a[164:167], 0\n"
"v_cvt_pk_bf16_f32 v68, v112, v114\n"
"v_cvt_pk_bf16_f32 v70, v113, v115\n"
"v_mfma_f32_16x16x32_bf16 v[48:51], a[168:171], v[56:59], 0\n"
"buffer_load_dwordx4 v190, s[68:71], s14 offen offset:2048 lds\n"
"v_cvt_pk_bf16_f32 v69, v116, v118\n"
"v_mfma_f32_16x16x32_bf16 v[52:55], a[168:171], v[60:63], 0\n"
"v_cvt_pk_bf16_f32 v71, v117, v119\n"
"s_waitcnt lgkmcnt(0)\n"
"v_mfma_f32_16x16x32_bf16 v[0:3], a[172:175], v[64:67], v[0:3]\n"
"ds_read_b128 v[128:131], v215 offset:3072\n"
"ds_read_b128 v[132:135], v215 offset:3136\n"
"v_mfma_f32_4x4x4_16b_bf16 v[104:107], v[122:123], v[136:137], 0\n"
"v_mfma_f32_16x16x32_bf16 v[4:7], a[172:175], v[68:71], v[4:7]\n"
"ds_read_b64_tr_b16 a[144:145], v208 offset:0\n"
"ds_read_b64_tr_b16 a[146:147], v208 offset:240\n"
"v_mfma_f32_4x4x4_16b_bf16 v[108:111], v[126:127], v[136:137], 0\n"
"v_mfma_f32_16x16x32_bf16 v[8:11], a[176:179], v[64:67], v[8:11]\n"
"ds_read_b64_tr_b16 a[148:149], v208 offset:40\n"
"ds_read_b64_tr_b16 a[150:151], v208 offset:280\n"
"v_mfma_f32_4x4x4_16b_bf16 v[104:107], v[120:121], a[32:33], v[104:107]\n"
"v_mfma_f32_16x16x32_bf16 v[12:15], a[176:179], v[68:71], v[12:15]\n"
"ds_read_b64_tr_b16 a[152:153], v204 offset:80\n"
"ds_read_b64_tr_b16 a[154:155], v204 offset:320\n"
"v_mfma_f32_4x4x4_16b_bf16 v[108:111], v[124:125], a[34:35], v[108:111]\n"
"v_mfma_f32_16x16x32_bf16 v[16:19], a[180:183], v[64:67], v[16:19]\n"
"ds_read_b64_tr_b16 a[156:157], v204 offset:120\n"
"ds_read_b64_tr_b16 a[158:159], v204 offset:360\n"
"v_mfma_f32_4x4x4_16b_bf16 v[112:115], v[122:123], v[138:139], 0\n"
"v_mfma_f32_16x16x32_bf16 v[20:23], a[180:183], v[68:71], v[20:23]\n"
"ds_read_b64_tr_b16 a[160:161], v204 offset:160\n"
"ds_read_b64_tr_b16 a[162:163], v204 offset:400\n"
"v_mfma_f32_4x4x4_16b_bf16 v[116:119], v[126:127], v[138:139], 0\n"
"v_mfma_f32_16x16x32_bf16 v[24:27], a[184:187], v[64:67], v[24:27]\n"
"ds_read_b64_tr_b16 a[164:165], v204 offset:200\n"
"ds_read_b64_tr_b16 a[166:167], v204 offset:440\n"
"v_mfma_f32_4x4x4_16b_bf16 v[112:115], v[120:121], a[36:37], v[112:115]\n"
"v_mfma_f32_16x16x32_bf16 v[28:31], a[184:187], v[68:71], v[28:31]\n"
"ds_read_b64_tr_b16 a[168:169], v212 offset:0\n"
"ds_read_b64_tr_b16 a[170:171], v212 offset:240\n"
"v_mfma_f32_4x4x4_16b_bf16 v[116:119], v[124:125], a[38:39], v[116:119]\n"
"v_mfma_f32_16x16x32_bf16 v[32:35], v[64:67], a[188:191], v[32:35]\n"
"buffer_load_dwordx4 v191, s[68:71], s14 offen offset:3072 lds\n"
"v_cvt_pk_bf16_f32 v56, v104, v106\n"
"v_mfma_f32_16x16x32_bf16 v[36:39], v[68:71], a[188:191], v[36:39]\n"
"v_cvt_pk_bf16_f32 v58, v105, v107\n"
"v_cvt_pk_bf16_f32 v57, v108, v110\n"
"v_mfma_f32_16x16x32_bf16 v[40:43], v[64:67], a[192:195], v[40:43]\n"
"s_add_u32 m0, m0, 4096\n"
"s_add_u32 s14, s14, 4096\n"
"buffer_load_dwordx4 v192, s[68:71], s14 offen offset:0 lds\n"
"v_cvt_pk_bf16_f32 v59, v109, v111\n"
"v_mfma_f32_16x16x32_bf16 v[44:47], v[68:71], a[192:195], v[44:47]\n"
"v_cvt_pk_bf16_f32 v60, v112, v114\n"
"v_cvt_pk_bf16_f32 v62, v113, v115\n"
"v_mfma_f32_16x16x32_bf16 v[48:51], a[196:199], v[64:67], v[48:51]\n"
"buffer_load_dwordx4 v193, s[68:71], s14 offen offset:1024 lds\n"
"v_cvt_pk_bf16_f32 v61, v116, v118\n"
"v_mfma_f32_16x16x32_bf16 v[52:55], a[196:199], v[68:71], v[52:55]\n"
"v_cvt_pk_bf16_f32 v63, v117, v119\n"
"s_waitcnt lgkmcnt(0)\n"
"v_mfma_f32_16x16x32_bf16 v[0:3], a[144:147], v[56:59], v[0:3]\n"
"ds_read_b128 v[120:123], v215 offset:512\n"
"ds_read_b128 v[124:127], v215 offset:576\n"
"v_mfma_f32_4x4x4_16b_bf16 v[104:107], v[130:131], v[136:137], 0\n"
"v_mfma_f32_16x16x32_bf16 v[4:7], a[144:147], v[60:63], v[4:7]\n"
"ds_read_b64_tr_b16 a[172:173], v208 offset:36864\n"
"ds_read_b64_tr_b16 a[174:175], v208 offset:37104\n"
"v_mfma_f32_4x4x4_16b_bf16 v[108:111], v[134:135], v[136:137], 0\n"
"v_mfma_f32_16x16x32_bf16 v[8:11], a[148:151], v[56:59], v[8:11]\n"
"ds_read_b64_tr_b16 a[176:177], v208 offset:36904\n"
"ds_read_b64_tr_b16 a[178:179], v208 offset:37144\n"
"v_mfma_f32_4x4x4_16b_bf16 v[104:107], v[128:129], a[48:49], v[104:107]\n"
"v_mfma_f32_16x16x32_bf16 v[12:15], a[148:151], v[60:63], v[12:15]\n"
"ds_read_b64_tr_b16 a[180:181], v204 offset:36944\n"
"ds_read_b64_tr_b16 a[182:183], v204 offset:37184\n"
"v_mfma_f32_4x4x4_16b_bf16 v[108:111], v[132:133], a[50:51], v[108:111]\n"
"v_mfma_f32_16x16x32_bf16 v[16:19], a[152:155], v[56:59], v[16:19]\n"
"ds_read_b64_tr_b16 a[184:185], v204 offset:36984\n"
"ds_read_b64_tr_b16 a[186:187], v204 offset:37224\n"
"v_mfma_f32_4x4x4_16b_bf16 v[112:115], v[130:131], v[138:139], 0\n"
"v_mfma_f32_16x16x32_bf16 v[20:23], a[152:155], v[60:63], v[20:23]\n"
"ds_read_b64_tr_b16 a[188:189], v204 offset:37024\n"
"ds_read_b64_tr_b16 a[190:191], v204 offset:37264\n"
"v_mfma_f32_4x4x4_16b_bf16 v[116:119], v[134:135], v[138:139], 0\n"
"v_mfma_f32_16x16x32_bf16 v[24:27], a[156:159], v[56:59], v[24:27]\n"
"ds_read_b64_tr_b16 a[192:193], v204 offset:37064\n"
"ds_read_b64_tr_b16 a[194:195], v204 offset:37304\n"
"v_mfma_f32_4x4x4_16b_bf16 v[112:115], v[128:129], a[52:53], v[112:115]\n"
"v_mfma_f32_16x16x32_bf16 v[28:31], a[156:159], v[60:63], v[28:31]\n"
"ds_read_b64_tr_b16 a[196:197], v212 offset:36864\n"
"ds_read_b64_tr_b16 a[198:199], v212 offset:37104\n"
"v_mfma_f32_4x4x4_16b_bf16 v[116:119], v[132:133], a[54:55], v[116:119]\n"
"v_mfma_f32_16x16x32_bf16 v[32:35], v[56:59], a[160:163], v[32:35]\n"
"buffer_load_dwordx4 v194, s[68:71], s14 offen offset:2048 lds\n"
"v_cvt_pk_bf16_f32 v64, v104, v106\n"
"v_mfma_f32_16x16x32_bf16 v[36:39], v[60:63], a[160:163], v[36:39]\n"
"v_cvt_pk_bf16_f32 v66, v105, v107\n"
"v_cvt_pk_bf16_f32 v65, v108, v110\n"
"v_mfma_f32_16x16x32_bf16 v[40:43], v[56:59], a[164:167], v[40:43]\n"
"buffer_load_dwordx4 v195, s[68:71], s14 offen offset:3072 lds\n"
"v_cvt_pk_bf16_f32 v67, v109, v111\n"
"v_mfma_f32_16x16x32_bf16 v[44:47], v[60:63], a[164:167], v[44:47]\n"
"v_cvt_pk_bf16_f32 v68, v112, v114\n"
"v_cvt_pk_bf16_f32 v70, v113, v115\n"
"v_mfma_f32_16x16x32_bf16 v[48:51], a[168:171], v[56:59], v[48:51]\n"
"s_add_u32 m0, m0, 4096\n"
"s_add_u32 s14, s14, 4096\n"
"buffer_load_dwordx4 v196, s[68:71], s14 offen offset:0 lds\n"
"v_cvt_pk_bf16_f32 v69, v116, v118\n"
"v_mfma_f32_16x16x32_bf16 v[52:55], a[168:171], v[60:63], v[52:55]\n"
"v_cvt_pk_bf16_f32 v71, v117, v119\n"
"s_waitcnt lgkmcnt(0)\n"
"v_mfma_f32_16x16x32_bf16 v[0:3], a[172:175], v[64:67], v[0:3]\n"
"ds_read_b128 v[128:131], v215 offset:1536\n"
"ds_read_b128 v[132:135], v215 offset:1600\n"
"v_mfma_f32_4x4x4_16b_bf16 v[104:107], v[122:123], v[136:137], 0\n"
"v_mfma_f32_16x16x32_bf16 v[4:7], a[172:175], v[68:71], v[4:7]\n"
"s_waitcnt vmcnt(9)\n"
"s_barrier\n"
"ds_read_b64_tr_b16 a[144:145], v207 offset:7680\n"
"ds_read_b64_tr_b16 a[146:147], v207 offset:7920\n"
"v_mfma_f32_4x4x4_16b_bf16 v[108:111], v[126:127], v[136:137], 0\n"
"v_mfma_f32_16x16x32_bf16 v[8:11], a[176:179], v[64:67], v[8:11]\n"
"ds_read_b64_tr_b16 a[148:149], v207 offset:7720\n"
"ds_read_b64_tr_b16 a[150:151], v207 offset:7960\n"
"v_mfma_f32_4x4x4_16b_bf16 v[104:107], v[120:121], a[8:9], v[104:107]\n"
"v_mfma_f32_16x16x32_bf16 v[12:15], a[176:179], v[68:71], v[12:15]\n"
"ds_read_b64_tr_b16 a[152:153], v203 offset:7760\n"
"ds_read_b64_tr_b16 a[154:155], v203 offset:8000\n"
"v_mfma_f32_4x4x4_16b_bf16 v[108:111], v[124:125], a[10:11], v[108:111]\n"
"v_mfma_f32_16x16x32_bf16 v[16:19], a[180:183], v[64:67], v[16:19]\n"
"ds_read_b64_tr_b16 a[156:157], v203 offset:7800\n"
"ds_read_b64_tr_b16 a[158:159], v203 offset:8040\n"
"v_mfma_f32_4x4x4_16b_bf16 v[112:115], v[122:123], v[138:139], 0\n"
"v_mfma_f32_16x16x32_bf16 v[20:23], a[180:183], v[68:71], v[20:23]\n"
"ds_read_b64_tr_b16 a[160:161], v203 offset:7840\n"
"ds_read_b64_tr_b16 a[162:163], v203 offset:8080\n"
"v_mfma_f32_4x4x4_16b_bf16 v[116:119], v[126:127], v[138:139], 0\n"
"v_mfma_f32_16x16x32_bf16 v[24:27], a[184:187], v[64:67], v[24:27]\n"
"ds_read_b64_tr_b16 a[164:165], v203 offset:7880\n"
"ds_read_b64_tr_b16 a[166:167], v203 offset:8120\n"
"v_mfma_f32_4x4x4_16b_bf16 v[112:115], v[120:121], a[12:13], v[112:115]\n"
"v_mfma_f32_16x16x32_bf16 v[28:31], a[184:187], v[68:71], v[28:31]\n"
"ds_read_b64_tr_b16 a[168:169], v211 offset:7680\n"
"ds_read_b64_tr_b16 a[170:171], v211 offset:7920\n"
"v_mfma_f32_4x4x4_16b_bf16 v[116:119], v[124:125], a[14:15], v[116:119]\n"
"v_mfma_f32_16x16x32_bf16 v[32:35], v[64:67], a[188:191], v[32:35]\n"
"buffer_load_dwordx4 v197, s[68:71], s14 offen offset:1024 lds\n"
"v_cvt_pk_bf16_f32 v56, v104, v106\n"
"v_mfma_f32_16x16x32_bf16 v[36:39], v[68:71], a[188:191], v[36:39]\n"
"v_cvt_pk_bf16_f32 v58, v105, v107\n"
"v_cvt_pk_bf16_f32 v57, v108, v110\n"
"v_mfma_f32_16x16x32_bf16 v[40:43], v[64:67], a[192:195], v[40:43]\n"
"buffer_load_dwordx4 v198, s[68:71], s14 offen offset:2048 lds\n"
"v_cvt_pk_bf16_f32 v59, v109, v111\n"
"v_mfma_f32_16x16x32_bf16 v[44:47], v[68:71], a[192:195], v[44:47]\n"
"v_cvt_pk_bf16_f32 v60, v112, v114\n"
"v_cvt_pk_bf16_f32 v62, v113, v115\n"
"v_mfma_f32_16x16x32_bf16 v[48:51], a[196:199], v[64:67], v[48:51]\n"
"buffer_load_dwordx4 v199, s[68:71], s14 offen offset:3072 lds\n"
"v_cvt_pk_bf16_f32 v61, v116, v118\n"
"v_mfma_f32_16x16x32_bf16 v[52:55], a[196:199], v[68:71], v[52:55]\n"
"buffer_load_dwordx4 v[224:227], v216, s[80:83], s22 offen sc1\n"
"v_cvt_pk_bf16_f32 v63, v117, v119\n"
"s_waitcnt lgkmcnt(0)\n"
"v_mfma_f32_16x16x32_bf16 v[0:3], a[144:147], v[56:59], v[0:3]\n"
"ds_read_b128 v[120:123], v215 offset:2560\n"
"ds_read_b128 v[124:127], v215 offset:2624\n"
"v_mfma_f32_4x4x4_16b_bf16 v[104:107], v[130:131], v[136:137], 0\n"
"v_mfma_f32_16x16x32_bf16 v[4:7], a[144:147], v[60:63], v[4:7]\n"
"ds_read_b64_tr_b16 a[172:173], v207 offset:44544\n"
"ds_read_b64_tr_b16 a[174:175], v207 offset:44784\n"
"v_mfma_f32_4x4x4_16b_bf16 v[108:111], v[134:135], v[136:137], 0\n"
"v_mfma_f32_16x16x32_bf16 v[8:11], a[148:151], v[56:59], v[8:11]\n"
"ds_read_b64_tr_b16 a[176:177], v207 offset:44584\n"
"ds_read_b64_tr_b16 a[178:179], v207 offset:44824\n"
"v_mfma_f32_4x4x4_16b_bf16 v[104:107], v[128:129], a[24:25], v[104:107]\n"
"v_mfma_f32_16x16x32_bf16 v[12:15], a[148:151], v[60:63], v[12:15]\n"
"ds_read_b64_tr_b16 a[180:181], v203 offset:44624\n"
"ds_read_b64_tr_b16 a[182:183], v203 offset:44864\n"
"v_mfma_f32_4x4x4_16b_bf16 v[108:111], v[132:133], a[26:27], v[108:111]\n"
"v_mfma_f32_16x16x32_bf16 v[16:19], a[152:155], v[56:59], v[16:19]\n"
"ds_read_b64_tr_b16 a[184:185], v203 offset:44664\n"
"ds_read_b64_tr_b16 a[186:187], v203 offset:44904\n"
"v_mfma_f32_4x4x4_16b_bf16 v[112:115], v[130:131], v[138:139], 0\n"
"v_mfma_f32_16x16x32_bf16 v[20:23], a[152:155], v[60:63], v[20:23]\n"
"ds_read_b64_tr_b16 a[188:189], v203 offset:44704\n"
"ds_read_b64_tr_b16 a[190:191], v203 offset:44944\n"
"v_mfma_f32_4x4x4_16b_bf16 v[116:119], v[134:135], v[138:139], 0\n"
"v_mfma_f32_16x16x32_bf16 v[24:27], a[156:159], v[56:59], v[24:27]\n"
"ds_read_b64_tr_b16 a[192:193], v203 offset:44744\n"
"ds_read_b64_tr_b16 a[194:195], v203 offset:44984\n"
"v_mfma_f32_4x4x4_16b_bf16 v[112:115], v[128:129], a[28:29], v[112:115]\n"
"v_mfma_f32_16x16x32_bf16 v[28:31], a[156:159], v[60:63], v[28:31]\n"
"ds_read_b64_tr_b16 a[196:197], v211 offset:44544\n"
"ds_read_b64_tr_b16 a[198:199], v211 offset:44784\n"
"v_mfma_f32_4x4x4_16b_bf16 v[116:119], v[132:133], a[30:31], v[116:119]\n"
"v_mfma_f32_16x16x32_bf16 v[32:35], v[56:59], a[160:163], v[32:35]\n"
"s_add_u32 m0, m0, 4096\n"
"s_add_u32 s14, s14, 4096\n"
"buffer_load_dwordx4 v200, s[68:71], s14 offen offset:0 lds\n"
"v_cvt_pk_bf16_f32 v64, v104, v106\n"
"v_mfma_f32_16x16x32_bf16 v[36:39], v[60:63], a[160:163], v[36:39]\n"
"v_cvt_pk_bf16_f32 v66, v105, v107\n"
"v_cvt_pk_bf16_f32 v65, v108, v110\n"
"v_mfma_f32_16x16x32_bf16 v[40:43], v[56:59], a[164:167], v[40:43]\n"
"buffer_load_dwordx4 v201, s[68:71], s14 offen offset:1024 lds\n"
"v_cvt_pk_bf16_f32 v67, v109, v111\n"
"v_mfma_f32_16x16x32_bf16 v[44:47], v[60:63], a[164:167], v[44:47]\n"
"v_cvt_pk_bf16_f32 v68, v112, v114\n"
"v_cvt_pk_bf16_f32 v70, v113, v115\n"
"v_mfma_f32_16x16x32_bf16 v[48:51], a[168:171], v[56:59], v[48:51]\n"
"buffer_load_dwordx4 v202, s[68:71], s14 offen offset:2048 lds\n"
"v_cvt_pk_bf16_f32 v69, v116, v118\n"
"v_mfma_f32_16x16x32_bf16 v[52:55], a[168:171], v[60:63], v[52:55]\n"
"v_cvt_pk_bf16_f32 v71, v117, v119\n"
"s_waitcnt lgkmcnt(0)\n"
"v_mfma_f32_16x16x32_bf16 v[0:3], a[172:175], v[64:67], v[0:3]\n"
"ds_read_b128 v[128:131], v215 offset:3584\n"
"ds_read_b128 v[132:135], v215 offset:3648\n"
"v_mfma_f32_4x4x4_16b_bf16 v[104:107], v[122:123], v[136:137], 0\n"
"v_mfma_f32_16x16x32_bf16 v[4:7], a[172:175], v[68:71], v[4:7]\n"
"ds_read_b64_tr_b16 a[144:145], v208 offset:7680\n"
"ds_read_b64_tr_b16 a[146:147], v208 offset:7920\n"
"v_mfma_f32_4x4x4_16b_bf16 v[108:111], v[126:127], v[136:137], 0\n"
"v_mfma_f32_16x16x32_bf16 v[8:11], a[176:179], v[64:67], v[8:11]\n"
"ds_read_b64_tr_b16 a[148:149], v208 offset:7720\n"
"ds_read_b64_tr_b16 a[150:151], v208 offset:7960\n"
"v_mfma_f32_4x4x4_16b_bf16 v[104:107], v[120:121], a[40:41], v[104:107]\n"
"v_mfma_f32_16x16x32_bf16 v[12:15], a[176:179], v[68:71], v[12:15]\n"
"ds_read_b64_tr_b16 a[152:153], v204 offset:7760\n"
"ds_read_b64_tr_b16 a[154:155], v204 offset:8000\n"
"v_mfma_f32_4x4x4_16b_bf16 v[108:111], v[124:125], a[42:43], v[108:111]\n"
"v_mfma_f32_16x16x32_bf16 v[16:19], a[180:183], v[64:67], v[16:19]\n"
"ds_read_b64_tr_b16 a[156:157], v204 offset:7800\n"
"ds_read_b64_tr_b16 a[158:159], v204 offset:8040\n"
"v_mfma_f32_4x4x4_16b_bf16 v[112:115], v[122:123], v[138:139], 0\n"
"v_mfma_f32_16x16x32_bf16 v[20:23], a[180:183], v[68:71], v[20:23]\n"
"ds_read_b64_tr_b16 a[160:161], v204 offset:7840\n"
"ds_read_b64_tr_b16 a[162:163], v204 offset:8080\n"
"v_mfma_f32_4x4x4_16b_bf16 v[116:119], v[126:127], v[138:139], 0\n"
"v_mfma_f32_16x16x32_bf16 v[24:27], a[184:187], v[64:67], v[24:27]\n"
"ds_read_b64_tr_b16 a[164:165], v204 offset:7880\n"
"ds_read_b64_tr_b16 a[166:167], v204 offset:8120\n"
"v_mfma_f32_4x4x4_16b_bf16 v[112:115], v[120:121], a[44:45], v[112:115]\n"
"v_mfma_f32_16x16x32_bf16 v[28:31], a[184:187], v[68:71], v[28:31]\n"
"ds_read_b64_tr_b16 a[168:169], v212 offset:7680\n"
"ds_read_b64_tr_b16 a[170:171], v212 offset:7920\n"
"v_mfma_f32_4x4x4_16b_bf16 v[116:119], v[124:125], a[46:47], v[116:119]\n"
"v_mfma_f32_16x16x32_bf16 v[32:35], v[64:67], a[188:191], v[32:35]\n"
"s_add_u32 m0, s11, 30720\n"
"s_mov_b32 s14, s15\n"
"buffer_load_dwordx4 v188, s[68:71], s14 offen offset:0 lds\n"
"v_cvt_pk_bf16_f32 v56, v104, v106\n"
"v_mfma_f32_16x16x32_bf16 v[36:39], v[68:71], a[188:191], v[36:39]\n"
"v_cvt_pk_bf16_f32 v58, v105, v107\n"
"v_cvt_pk_bf16_f32 v57, v108, v110\n"
"v_mfma_f32_16x16x32_bf16 v[40:43], v[64:67], a[192:195], v[40:43]\n"
"buffer_load_dwordx4 v189, s[68:71], s14 offen offset:1024 lds\n"
"v_cvt_pk_bf16_f32 v59, v109, v111\n"
"v_mfma_f32_16x16x32_bf16 v[44:47], v[68:71], a[192:195], v[44:47]\n"
"v_cvt_pk_bf16_f32 v60, v112, v114\n"
"v_cvt_pk_bf16_f32 v62, v113, v115\n"
"v_mfma_f32_16x16x32_bf16 v[48:51], a[196:199], v[64:67], v[48:51]\n"
"buffer_load_dwordx4 v190, s[68:71], s14 offen offset:2048 lds\n"
"v_cvt_pk_bf16_f32 v61, v116, v118\n"
"v_mfma_f32_16x16x32_bf16 v[52:55], a[196:199], v[68:71], v[52:55]\n"
"v_cvt_pk_bf16_f32 v63, v117, v119\n"
"s_waitcnt lgkmcnt(0)\n"
"v_mfma_f32_16x16x32_bf16 v[0:3], a[144:147], v[56:59], v[0:3]\n"
"ds_read_b128 v[120:123], v215 offset:4096\n"
"ds_read_b128 v[124:127], v215 offset:4160\n"
"v_mfma_f32_4x4x4_16b_bf16 v[104:107], v[130:131], v[136:137], 0\n"
"v_mfma_f32_16x16x32_bf16 v[4:7], a[144:147], v[60:63], v[4:7]\n"
"ds_read_b64_tr_b16 a[172:173], v208 offset:44544\n"
"ds_read_b64_tr_b16 a[174:175], v208 offset:44784\n"
"v_mfma_f32_4x4x4_16b_bf16 v[108:111], v[134:135], v[136:137], 0\n"
"v_mfma_f32_16x16x32_bf16 v[8:11], a[148:151], v[56:59], v[8:11]\n"
"ds_read_b64_tr_b16 a[176:177], v208 offset:44584\n"
"ds_read_b64_tr_b16 a[178:179], v208 offset:44824\n"
"v_mfma_f32_4x4x4_16b_bf16 v[104:107], v[128:129], a[56:57], v[104:107]\n"
"v_mfma_f32_16x16x32_bf16 v[12:15], a[148:151], v[60:63], v[12:15]\n"
"ds_read_b64_tr_b16 a[180:181], v204 offset:44624\n"
"ds_read_b64_tr_b16 a[182:183], v204 offset:44864\n"
"v_mfma_f32_4x4x4_16b_bf16 v[108:111], v[132:133], a[58:59], v[108:111]\n"
"v_mfma_f32_16x16x32_bf16 v[16:19], a[152:155], v[56:59], v[16:19]\n"
"ds_read_b64_tr_b16 a[184:185], v204 offset:44664\n"
"ds_read_b64_tr_b16 a[186:187], v204 offset:44904\n"
"v_mfma_f32_4x4x4_16b_bf16 v[112:115], v[130:131], v[138:139], 0\n"
"v_mfma_f32_16x16x32_bf16 v[20:23], a[152:155], v[60:63], v[20:23]\n"
"ds_read_b64_tr_b16 a[188:189], v204 offset:44704\n"
"ds_read_b64_tr_b16 a[190:191], v204 offset:44944\n"
"v_mfma_f32_4x4x4_16b_bf16 v[116:119], v[134:135], v[138:139], 0\n"
"v_mfma_f32_16x16x32_bf16 v[24:27], a[156:159], v[56:59], v[24:27]\n"
"ds_read_b64_tr_b16 a[192:193], v204 offset:44744\n"
"ds_read_b64_tr_b16 a[194:195], v204 offset:44984\n"
"v_mfma_f32_4x4x4_16b_bf16 v[112:115], v[128:129], a[60:61], v[112:115]\n"
"v_mfma_f32_16x16x32_bf16 v[28:31], a[156:159], v[60:63], v[28:31]\n"
"ds_read_b64_tr_b16 a[196:197], v212 offset:44544\n"
"ds_read_b64_tr_b16 a[198:199], v212 offset:44784\n"
"v_mfma_f32_4x4x4_16b_bf16 v[116:119], v[132:133], a[62:63], v[116:119]\n"
"v_mfma_f32_16x16x32_bf16 v[32:35], v[56:59], a[160:163], v[32:35]\n"
"buffer_load_dwordx4 v191, s[68:71], s14 offen offset:3072 lds\n"
"v_cvt_pk_bf16_f32 v64, v104, v106\n"
"v_mfma_f32_16x16x32_bf16 v[36:39], v[60:63], a[160:163], v[36:39]\n"
"v_cvt_pk_bf16_f32 v66, v105, v107\n"
"v_cvt_pk_bf16_f32 v65, v108, v110\n"
"v_mfma_f32_16x16x32_bf16 v[40:43], v[56:59], a[164:167], v[40:43]\n"
"v_cvt_pk_bf16_f32 v67, v109, v111\n"
"v_cvt_pk_bf16_f32 v68, v112, v114\n"
"v_mfma_f32_16x16x32_bf16 v[44:47], v[60:63], a[164:167], v[44:47]\n"
"v_cvt_pk_bf16_f32 v70, v113, v115\n"
"v_cvt_pk_bf16_f32 v69, v116, v118\n"
"v_mfma_f32_16x16x32_bf16 v[48:51], a[168:171], v[56:59], v[48:51]\n"
"v_cvt_pk_bf16_f32 v71, v117, v119\n"
"v_mfma_f32_16x16x32_bf16 v[52:55], a[168:171], v[60:63], v[52:55]\n"
"s_waitcnt lgkmcnt(0)\n"
"v_mfma_f32_16x16x32_bf16 v[0:3], a[172:175], v[64:67], v[0:3]\n"
"ds_read_b128 v[128:131], v215 offset:5120\n"
"ds_read_b128 v[132:135], v215 offset:5184\n"
"v_mfma_f32_4x4x4_16b_bf16 v[104:107], v[122:123], v[136:137], 0\n"
"v_mfma_f32_16x16x32_bf16 v[4:7], a[172:175], v[68:71], v[4:7]\n"
"s_waitcnt vmcnt(12)\n"
"s_barrier\n"
"ds_read_b64_tr_b16 a[144:145], v207 offset:15360\n"
"ds_read_b64_tr_b16 a[146:147], v207 offset:15600\n"
"v_mfma_f32_4x4x4_16b_bf16 v[108:111], v[126:127], v[136:137], 0\n"
"v_mfma_f32_16x16x32_bf16 v[8:11], a[176:179], v[64:67], v[8:11]\n"
"ds_read_b64_tr_b16 a[148:149], v207 offset:15400\n"
"ds_read_b64_tr_b16 a[150:151], v207 offset:15640\n"
"v_mfma_f32_4x4x4_16b_bf16 v[104:107], v[120:121], a[64:65], v[104:107]\n"
"v_mfma_f32_16x16x32_bf16 v[12:15], a[176:179], v[68:71], v[12:15]\n"
"ds_read_b64_tr_b16 a[152:153], v203 offset:15440\n"
"ds_read_b64_tr_b16 a[154:155], v203 offset:15680\n"
"v_mfma_f32_4x4x4_16b_bf16 v[108:111], v[124:125], a[66:67], v[108:111]\n"
"v_mfma_f32_16x16x32_bf16 v[16:19], a[180:183], v[64:67], v[16:19]\n"
"ds_read_b64_tr_b16 a[156:157], v203 offset:15480\n"
"ds_read_b64_tr_b16 a[158:159], v203 offset:15720\n"
"v_mfma_f32_4x4x4_16b_bf16 v[112:115], v[122:123], v[138:139], 0\n"
"v_mfma_f32_16x16x32_bf16 v[20:23], a[180:183], v[68:71], v[20:23]\n"
"ds_read_b64_tr_b16 a[160:161], v203 offset:15520\n"
"ds_read_b64_tr_b16 a[162:163], v203 offset:15760\n"
"v_mfma_f32_4x4x4_16b_bf16 v[116:119], v[126:127], v[138:139], 0\n"
"v_mfma_f32_16x16x32_bf16 v[24:27], a[184:187], v[64:67], v[24:27]\n"
"ds_read_b64_tr_b16 a[164:165], v203 offset:15560\n"
"ds_read_b64_tr_b16 a[166:167], v203 offset:15800\n"
"v_mfma_f32_4x4x4_16b_bf16 v[112:115], v[120:121], a[68:69], v[112:115]\n"
"v_mfma_f32_16x16x32_bf16 v[28:31], a[184:187], v[68:71], v[28:31]\n"
"ds_read_b64_tr_b16 a[168:169], v211 offset:15360\n"
"ds_read_b64_tr_b16 a[170:171], v211 offset:15600\n"
"v_mfma_f32_4x4x4_16b_bf16 v[116:119], v[124:125], a[70:71], v[116:119]\n"
"v_mfma_f32_16x16x32_bf16 v[32:35], v[64:67], a[188:191], v[32:35]\n"
"v_cvt_pk_bf16_f32 v56, v104, v106\n"
"v_cvt_pk_bf16_f32 v58, v105, v107\n"
"v_mfma_f32_16x16x32_bf16 v[36:39], v[68:71], a[188:191], v[36:39]\n"
"v_cvt_pk_bf16_f32 v57, v108, v110\n"
"v_cvt_pk_bf16_f32 v59, v109, v111\n"
"v_mfma_f32_16x16x32_bf16 v[40:43], v[64:67], a[192:195], v[40:43]\n"
"v_cvt_pk_bf16_f32 v60, v112, v114\n"
"v_cvt_pk_bf16_f32 v62, v113, v115\n"
"v_mfma_f32_16x16x32_bf16 v[44:47], v[68:71], a[192:195], v[44:47]\n"
"v_cvt_pk_bf16_f32 v61, v116, v118\n"
"v_cvt_pk_bf16_f32 v63, v117, v119\n"
"v_mfma_f32_16x16x32_bf16 v[48:51], a[196:199], v[64:67], v[48:51]\n"
"v_mfma_f32_16x16x32_bf16 v[52:55], a[196:199], v[68:71], v[52:55]\n"
"s_waitcnt lgkmcnt(0)\n"
"v_mfma_f32_16x16x32_bf16 v[0:3], a[144:147], v[56:59], v[0:3]\n"
"ds_read_b128 v[120:123], v215 offset:6144\n"
"ds_read_b128 v[124:127], v215 offset:6208\n"
"v_mfma_f32_4x4x4_16b_bf16 v[104:107], v[130:131], v[136:137], 0\n"
"v_mfma_f32_16x16x32_bf16 v[4:7], a[144:147], v[60:63], v[4:7]\n"
"ds_read_b64_tr_b16 a[172:173], v207 offset:52224\n"
"ds_read_b64_tr_b16 a[174:175], v207 offset:52464\n"
"v_mfma_f32_4x4x4_16b_bf16 v[108:111], v[134:135], v[136:137], 0\n"
"v_mfma_f32_16x16x32_bf16 v[8:11], a[148:151], v[56:59], v[8:11]\n"
"ds_read_b64_tr_b16 a[176:177], v207 offset:52264\n"
"ds_read_b64_tr_b16 a[178:179], v207 offset:52504\n"
"v_mfma_f32_4x4x4_16b_bf16 v[104:107], v[128:129], a[80:81], v[104:107]\n"
"v_mfma_f32_16x16x32_bf16 v[12:15], a[148:151], v[60:63], v[12:15]\n"
"ds_read_b64_tr_b16 a[180:181], v203 offset:52304\n"
"ds_read_b64_tr_b16 a[182:183], v203 offset:52544\n"
"v_mfma_f32_4x4x4_16b_bf16 v[108:111], v[132:133], a[82:83], v[108:111]\n"
"v_mfma_f32_16x16x32_bf16 v[16:19], a[152:155], v[56:59], v[16:19]\n"
"ds_read_b64_tr_b16 a[184:185], v203 offset:52344\n"
"ds_read_b64_tr_b16 a[186:187], v203 offset:52584\n"
"v_mfma_f32_4x4x4_16b_bf16 v[112:115], v[130:131], v[138:139], 0\n"
"v_mfma_f32_16x16x32_bf16 v[20:23], a[152:155], v[60:63], v[20:23]\n"
"ds_read_b64_tr_b16 a[188:189], v203 offset:52384\n"
"ds_read_b64_tr_b16 a[190:191], v203 offset:52624\n"
"v_mfma_f32_4x4x4_16b_bf16 v[116:119], v[134:135], v[138:139], 0\n"
"v_mfma_f32_16x16x32_bf16 v[24:27], a[156:159], v[56:59], v[24:27]\n"
"ds_read_b64_tr_b16 a[192:193], v203 offset:52424\n"
"ds_read_b64_tr_b16 a[194:195], v203 offset:52664\n"
"v_mfma_f32_4x4x4_16b_bf16 v[112:115], v[128:129], a[84:85], v[112:115]\n"
"v_mfma_f32_16x16x32_bf16 v[28:31], a[156:159], v[60:63], v[28:31]\n"
"ds_read_b64_tr_b16 a[196:197], v211 offset:52224\n"
"ds_read_b64_tr_b16 a[198:199], v211 offset:52464\n"
"v_mfma_f32_4x4x4_16b_bf16 v[116:119], v[132:133], a[86:87], v[116:119]\n"
"v_mfma_f32_16x16x32_bf16 v[32:35], v[56:59], a[160:163], v[32:35]\n"
"v_cvt_pk_bf16_f32 v64, v104, v106\n"
"v_cvt_pk_bf16_f32 v66, v105, v107\n"
"v_mfma_f32_16x16x32_bf16 v[36:39], v[60:63], a[160:163], v[36:39]\n"
"v_cvt_pk_bf16_f32 v65, v108, v110\n"
"v_cvt_pk_bf16_f32 v67, v109, v111\n"
"v_mfma_f32_16x16x32_bf16 v[40:43], v[56:59], a[164:167], v[40:43]\n"
"v_cvt_pk_bf16_f32 v68, v112, v114\n"
"v_cvt_pk_bf16_f32 v70, v113, v115\n"
"v_mfma_f32_16x16x32_bf16 v[44:47], v[60:63], a[164:167], v[44:47]\n"
"v_cvt_pk_bf16_f32 v69, v116, v118\n"
"v_cvt_pk_bf16_f32 v71, v117, v119\n"
"v_mfma_f32_16x16x32_bf16 v[48:51], a[168:171], v[56:59], v[48:51]\n"
"v_mfma_f32_16x16x32_bf16 v[52:55], a[168:171], v[60:63], v[52:55]\n"
"s_waitcnt lgkmcnt(0)\n"
"v_mfma_f32_16x16x32_bf16 v[0:3], a[172:175], v[64:67], v[0:3]\n"
"ds_read_b128 v[128:131], v215 offset:7168\n"
"ds_read_b128 v[132:135], v215 offset:7232\n"
"v_mfma_f32_4x4x4_16b_bf16 v[104:107], v[122:123], v[136:137], 0\n"
"v_mfma_f32_16x16x32_bf16 v[4:7], a[172:175], v[68:71], v[4:7]\n"
"ds_read_b64_tr_b16 a[144:145], v208 offset:15360\n"
"ds_read_b64_tr_b16 a[146:147], v208 offset:15600\n"
"v_mfma_f32_4x4x4_16b_bf16 v[108:111], v[126:127], v[136:137], 0\n"
"v_mfma_f32_16x16x32_bf16 v[8:11], a[176:179], v[64:67], v[8:11]\n"
"ds_read_b64_tr_b16 a[148:149], v208 offset:15400\n"
"ds_read_b64_tr_b16 a[150:151], v208 offset:15640\n"
"v_mfma_f32_4x4x4_16b_bf16 v[104:107], v[120:121], a[96:97], v[104:107]\n"
"v_mfma_f32_16x16x32_bf16 v[12:15], a[176:179], v[68:71], v[12:15]\n"
"ds_read_b64_tr_b16 a[152:153], v204 offset:15440\n"
"ds_read_b64_tr_b16 a[154:155], v204 offset:15680\n"
"v_mfma_f32_4x4x4_16b_bf16 v[108:111], v[124:125], a[98:99], v[108:111]\n"
"v_mfma_f32_16x16x32_bf16 v[16:19], a[180:183], v[64:67], v[16:19]\n"
"ds_read_b64_tr_b16 a[156:157], v204 offset:15480\n"
"ds_read_b64_tr_b16 a[158:159], v204 offset:15720\n"
"v_mfma_f32_4x4x4_16b_bf16 v[112:115], v[122:123], v[138:139], 0\n"
"v_mfma_f32_16x16x32_bf16 v[20:23], a[180:183], v[68:71], v[20:23]\n"
"ds_read_b64_tr_b16 a[160:161], v204 offset:15520\n"
"ds_read_b64_tr_b16 a[162:163], v204 offset:15760\n"
"v_mfma_f32_4x4x4_16b_bf16 v[116:119], v[126:127], v[138:139], 0\n"
"v_mfma_f32_16x16x32_bf16 v[24:27], a[184:187], v[64:67], v[24:27]\n"
"ds_read_b64_tr_b16 a[164:165], v204 offset:15560\n"
"ds_read_b64_tr_b16 a[166:167], v204 offset:15800\n"
"v_mfma_f32_4x4x4_16b_bf16 v[112:115], v[120:121], a[100:101], v[112:115]\n"
"v_mfma_f32_16x16x32_bf16 v[28:31], a[184:187], v[68:71], v[28:31]\n"
"ds_read_b64_tr_b16 a[168:169], v212 offset:15360\n"
"ds_read_b64_tr_b16 a[170:171], v212 offset:15600\n"
"v_mfma_f32_4x4x4_16b_bf16 v[116:119], v[124:125], a[102:103], v[116:119]\n"
"v_mfma_f32_16x16x32_bf16 v[32:35], v[64:67], a[188:191], v[32:35]\n"
"v_cvt_pk_bf16_f32 v56, v104, v106\n"
"v_cvt_pk_bf16_f32 v58, v105, v107\n"
"v_mfma_f32_16x16x32_bf16 v[36:39], v[68:71], a[188:191], v[36:39]\n"
"v_cvt_pk_bf16_f32 v57, v108, v110\n"
"v_cvt_pk_bf16_f32 v59, v109, v111\n"
"v_mfma_f32_16x16x32_bf16 v[40:43], v[64:67], a[192:195], v[40:43]\n"
"v_cvt_pk_bf16_f32 v60, v112, v114\n"
"v_cvt_pk_bf16_f32 v62, v113, v115\n"
"v_mfma_f32_16x16x32_bf16 v[44:47], v[68:71], a[192:195], v[44:47]\n"
"v_cvt_pk_bf16_f32 v61, v116, v118\n"
"v_cvt_pk_bf16_f32 v63, v117, v119\n"
"v_mfma_f32_16x16x32_bf16 v[48:51], a[196:199], v[64:67], v[48:51]\n"
"v_mfma_f32_16x16x32_bf16 v[52:55], a[196:199], v[68:71], v[52:55]\n"
"s_waitcnt lgkmcnt(0)\n"
"v_mfma_f32_16x16x32_bf16 v[0:3], a[144:147], v[56:59], v[0:3]\n"
"ds_read_b128 v[120:123], v215 offset:4608\n"
"ds_read_b128 v[124:127], v215 offset:4672\n"
"v_mfma_f32_4x4x4_16b_bf16 v[104:107], v[130:131], v[136:137], 0\n"
"v_mfma_f32_16x16x32_bf16 v[4:7], a[144:147], v[60:63], v[4:7]\n"
"ds_read_b64_tr_b16 a[172:173], v208 offset:52224\n"
"ds_read_b64_tr_b16 a[174:175], v208 offset:52464\n"
"v_mfma_f32_4x4x4_16b_bf16 v[108:111], v[134:135], v[136:137], 0\n"
"v_mfma_f32_16x16x32_bf16 v[8:11], a[148:151], v[56:59], v[8:11]\n"
"ds_read_b64_tr_b16 a[176:177], v208 offset:52264\n"
"ds_read_b64_tr_b16 a[178:179], v208 offset:52504\n"
"v_mfma_f32_4x4x4_16b_bf16 v[104:107], v[128:129], a[112:113], v[104:107]\n"
"v_mfma_f32_16x16x32_bf16 v[12:15], a[148:151], v[60:63], v[12:15]\n"
"ds_read_b64_tr_b16 a[180:181], v204 offset:52304\n"
"ds_read_b64_tr_b16 a[182:183], v204 offset:52544\n"
"v_mfma_f32_4x4x4_16b_bf16 v[108:111], v[132:133], a[114:115], v[108:111]\n"
"v_mfma_f32_16x16x32_bf16 v[16:19], a[152:155], v[56:59], v[16:19]\n"
"ds_read_b64_tr_b16 a[184:185], v204 offset:52344\n"
"ds_read_b64_tr_b16 a[186:187], v204 offset:52584\n"
"v_mfma_f32_4x4x4_16b_bf16 v[112:115], v[130:131], v[138:139], 0\n"
"v_mfma_f32_16x16x32_bf16 v[20:23], a[152:155], v[60:63], v[20:23]\n"
"ds_read_b64_tr_b16 a[188:189], v204 offset:52384\n"
"ds_read_b64_tr_b16 a[190:191], v204 offset:52624\n"
"v_mfma_f32_4x4x4_16b_bf16 v[116:119], v[134:135], v[138:139], 0\n"
"v_mfma_f32_16x16x32_bf16 v[24:27], a[156:159], v[56:59], v[24:27]\n"
"ds_read_b64_tr_b16 a[192:193], v204 offset:52424\n"
"ds_read_b64_tr_b16 a[194:195], v204 offset:52664\n"
"v_mfma_f32_4x4x4_16b_bf16 v[112:115], v[128:129], a[116:117], v[112:115]\n"
"v_mfma_f32_16x16x32_bf16 v[28:31], a[156:159], v[60:63], v[28:31]\n"
"ds_read_b64_tr_b16 a[196:197], v212 offset:52224\n"
"ds_read_b64_tr_b16 a[198:199], v212 offset:52464\n"
"v_mfma_f32_4x4x4_16b_bf16 v[116:119], v[132:133], a[118:119], v[116:119]\n"
"v_mfma_f32_16x16x32_bf16 v[32:35], v[56:59], a[160:163], v[32:35]\n"
"v_cvt_pk_bf16_f32 v64, v104, v106\n"
"v_cvt_pk_bf16_f32 v66, v105, v107\n"
"v_mfma_f32_16x16x32_bf16 v[36:39], v[60:63], a[160:163], v[36:39]\n"
"v_cvt_pk_bf16_f32 v65, v108, v110\n"
"v_cvt_pk_bf16_f32 v67, v109, v111\n"
"v_mfma_f32_16x16x32_bf16 v[40:43], v[56:59], a[164:167], v[40:43]\n"
"v_cvt_pk_bf16_f32 v68, v112, v114\n"
"v_cvt_pk_bf16_f32 v70, v113, v115\n"
"v_mfma_f32_16x16x32_bf16 v[44:47], v[60:63], a[164:167], v[44:47]\n"
"v_cvt_pk_bf16_f32 v69, v116, v118\n"
"v_cvt_pk_bf16_f32 v71, v117, v119\n"
"v_mfma_f32_16x16x32_bf16 v[48:51], a[168:171], v[56:59], v[48:51]\n"
"v_mfma_f32_16x16x32_bf16 v[52:55], a[168:171], v[60:63], v[52:55]\n"
"s_waitcnt lgkmcnt(0)\n"
"v_mfma_f32_16x16x32_bf16 v[0:3], a[172:175], v[64:67], v[0:3]\n"
"ds_read_b128 v[128:131], v215 offset:5632\n"
"ds_read_b128 v[132:135], v215 offset:5696\n"
"v_mfma_f32_4x4x4_16b_bf16 v[104:107], v[122:123], v[136:137], 0\n"
"v_mfma_f32_16x16x32_bf16 v[4:7], a[172:175], v[68:71], v[4:7]\n"
"s_waitcnt vmcnt(4)\n"
"s_barrier\n"
"ds_read_b64_tr_b16 a[144:145], v207 offset:23040\n"
"ds_read_b64_tr_b16 a[146:147], v207 offset:23280\n"
"v_mfma_f32_4x4x4_16b_bf16 v[108:111], v[126:127], v[136:137], 0\n"
"v_mfma_f32_16x16x32_bf16 v[8:11], a[176:179], v[64:67], v[8:11]\n"
"ds_read_b64_tr_b16 a[148:149], v207 offset:23080\n"
"ds_read_b64_tr_b16 a[150:151], v207 offset:23320\n"
"v_mfma_f32_4x4x4_16b_bf16 v[104:107], v[120:121], a[72:73], v[104:107]\n"
"v_mfma_f32_16x16x32_bf16 v[12:15], a[176:179], v[68:71], v[12:15]\n"
"ds_read_b64_tr_b16 a[152:153], v203 offset:23120\n"
"ds_read_b64_tr_b16 a[154:155], v203 offset:23360\n"
"v_mfma_f32_4x4x4_16b_bf16 v[108:111], v[124:125], a[74:75], v[108:111]\n"
"v_mfma_f32_16x16x32_bf16 v[16:19], a[180:183], v[64:67], v[16:19]\n"
"ds_read_b64_tr_b16 a[156:157], v203 offset:23160\n"
"ds_read_b64_tr_b16 a[158:159], v203 offset:23400\n"
"v_mfma_f32_4x4x4_16b_bf16 v[112:115], v[122:123], v[138:139], 0\n"
"v_mfma_f32_16x16x32_bf16 v[20:23], a[180:183], v[68:71], v[20:23]\n"
"ds_read_b64_tr_b16 a[160:161], v203 offset:23200\n"
"ds_read_b64_tr_b16 a[162:163], v203 offset:23440\n"
"v_mfma_f32_4x4x4_16b_bf16 v[116:119], v[126:127], v[138:139], 0\n"
"v_mfma_f32_16x16x32_bf16 v[24:27], a[184:187], v[64:67], v[24:27]\n"
"ds_read_b64_tr_b16 a[164:165], v203 offset:23240\n"
"ds_read_b64_tr_b16 a[166:167], v203 offset:23480\n"
"v_mfma_f32_4x4x4_16b_bf16 v[112:115], v[120:121], a[76:77], v[112:115]\n"
"v_mfma_f32_16x16x32_bf16 v[28:31], a[184:187], v[68:71], v[28:31]\n"
"ds_read_b64_tr_b16 a[168:169], v211 offset:23040\n"
"ds_read_b64_tr_b16 a[170:171], v211 offset:23280\n"
"v_mfma_f32_4x4x4_16b_bf16 v[116:119], v[124:125], a[78:79], v[116:119]\n"
"v_mfma_f32_16x16x32_bf16 v[32:35], v[64:67], a[188:191], v[32:35]\n"
"v_cvt_pk_bf16_f32 v56, v104, v106\n"
"v_cvt_pk_bf16_f32 v58, v105, v107\n"
"v_mfma_f32_16x16x32_bf16 v[36:39], v[68:71], a[188:191], v[36:39]\n"
"v_cvt_pk_bf16_f32 v57, v108, v110\n"
"v_cvt_pk_bf16_f32 v59, v109, v111\n"
"v_mfma_f32_16x16x32_bf16 v[40:43], v[64:67], a[192:195], v[40:43]\n"
"v_cvt_pk_bf16_f32 v60, v112, v114\n"
"v_cvt_pk_bf16_f32 v62, v113, v115\n"
"v_mfma_f32_16x16x32_bf16 v[44:47], v[68:71], a[192:195], v[44:47]\n"
"v_cvt_pk_bf16_f32 v61, v116, v118\n"
"v_cvt_pk_bf16_f32 v63, v117, v119\n"
"v_mfma_f32_16x16x32_bf16 v[48:51], a[196:199], v[64:67], v[48:51]\n"
"v_mfma_f32_16x16x32_bf16 v[52:55], a[196:199], v[68:71], v[52:55]\n"
"s_waitcnt lgkmcnt(0)\n"
"v_mfma_f32_16x16x32_bf16 v[0:3], a[144:147], v[56:59], v[0:3]\n"
"ds_read_b128 v[120:123], v215 offset:6656\n"
"ds_read_b128 v[124:127], v215 offset:6720\n"
"v_mfma_f32_4x4x4_16b_bf16 v[104:107], v[130:131], v[136:137], 0\n"
"v_mfma_f32_16x16x32_bf16 v[4:7], a[144:147], v[60:63], v[4:7]\n"
"ds_read_b64_tr_b16 a[172:173], v207 offset:59904\n"
"ds_read_b64_tr_b16 a[174:175], v207 offset:60144\n"
"v_mfma_f32_4x4x4_16b_bf16 v[108:111], v[134:135], v[136:137], 0\n"
"v_mfma_f32_16x16x32_bf16 v[8:11], a[148:151], v[56:59], v[8:11]\n"
"ds_read_b64_tr_b16 a[176:177], v207 offset:59944\n"
"ds_read_b64_tr_b16 a[178:179], v207 offset:60184\n"
"v_mfma_f32_4x4x4_16b_bf16 v[104:107], v[128:129], a[88:89], v[104:107]\n"
"v_mfma_f32_16x16x32_bf16 v[12:15], a[148:151], v[60:63], v[12:15]\n"
"ds_read_b64_tr_b16 a[180:181], v203 offset:59984\n"
"ds_read_b64_tr_b16 a[182:183], v203 offset:60224\n"
"v_mfma_f32_4x4x4_16b_bf16 v[108:111], v[132:133], a[90:91], v[108:111]\n"
"v_mfma_f32_16x16x32_bf16 v[16:19], a[152:155], v[56:59], v[16:19]\n"
"ds_read_b64_tr_b16 a[184:185], v203 offset:60024\n"
"ds_read_b64_tr_b16 a[186:187], v203 offset:60264\n"
"v_mfma_f32_4x4x4_16b_bf16 v[112:115], v[130:131], v[138:139], 0\n"
"v_mfma_f32_16x16x32_bf16 v[20:23], a[152:155], v[60:63], v[20:23]\n"
"ds_read_b64_tr_b16 a[188:189], v203 offset:60064\n"
"ds_read_b64_tr_b16 a[190:191], v203 offset:60304\n"
"v_mfma_f32_4x4x4_16b_bf16 v[116:119], v[134:135], v[138:139], 0\n"
"v_mfma_f32_16x16x32_bf16 v[24:27], a[156:159], v[56:59], v[24:27]\n"
"ds_read_b64_tr_b16 a[192:193], v203 offset:60104\n"
"ds_read_b64_tr_b16 a[194:195], v203 offset:60344\n"
"v_mfma_f32_4x4x4_16b_bf16 v[112:115], v[128:129], a[92:93], v[112:115]\n"
"v_mfma_f32_16x16x32_bf16 v[28:31], a[156:159], v[60:63], v[28:31]\n"
"ds_read_b64_tr_b16 a[196:197], v211 offset:59904\n"
"ds_read_b64_tr_b16 a[198:199], v211 offset:60144\n"
"v_mfma_f32_4x4x4_16b_bf16 v[116:119], v[132:133], a[94:95], v[116:119]\n"
"v_mfma_f32_16x16x32_bf16 v[32:35], v[56:59], a[160:163], v[32:35]\n"
"v_cvt_pk_bf16_f32 v64, v104, v106\n"
"v_cvt_pk_bf16_f32 v66, v105, v107\n"
"v_mfma_f32_16x16x32_bf16 v[36:39], v[60:63], a[160:163], v[36:39]\n"
"v_cvt_pk_bf16_f32 v65, v108, v110\n"
"v_cvt_pk_bf16_f32 v67, v109, v111\n"
"v_mfma_f32_16x16x32_bf16 v[40:43], v[56:59], a[164:167], v[40:43]\n"
"v_cvt_pk_bf16_f32 v68, v112, v114\n"
"v_cvt_pk_bf16_f32 v70, v113, v115\n"
"v_mfma_f32_16x16x32_bf16 v[44:47], v[60:63], a[164:167], v[44:47]\n"
"v_cvt_pk_bf16_f32 v69, v116, v118\n"
"v_cvt_pk_bf16_f32 v71, v117, v119\n"
"v_mfma_f32_16x16x32_bf16 v[48:51], a[168:171], v[56:59], v[48:51]\n"
"v_mfma_f32_16x16x32_bf16 v[52:55], a[168:171], v[60:63], v[52:55]\n"
"s_waitcnt lgkmcnt(0)\n"
"v_mfma_f32_16x16x32_bf16 v[0:3], a[172:175], v[64:67], v[0:3]\n"
"ds_read_b128 v[128:131], v215 offset:7680\n"
"ds_read_b128 v[132:135], v215 offset:7744\n"
"v_mfma_f32_4x4x4_16b_bf16 v[104:107], v[122:123], v[136:137], 0\n"
"v_mfma_f32_16x16x32_bf16 v[4:7], a[172:175], v[68:71], v[4:7]\n"
"ds_read_b64_tr_b16 a[144:145], v208 offset:23040\n"
"ds_read_b64_tr_b16 a[146:147], v208 offset:23280\n"
"v_mfma_f32_4x4x4_16b_bf16 v[108:111], v[126:127], v[136:137], 0\n"
"v_mfma_f32_16x16x32_bf16 v[8:11], a[176:179], v[64:67], v[8:11]\n"
"ds_read_b64_tr_b16 a[148:149], v208 offset:23080\n"
"ds_read_b64_tr_b16 a[150:151], v208 offset:23320\n"
"v_mfma_f32_4x4x4_16b_bf16 v[104:107], v[120:121], a[104:105], v[104:107]\n"
"v_mfma_f32_16x16x32_bf16 v[12:15], a[176:179], v[68:71], v[12:15]\n"
"ds_read_b64_tr_b16 a[152:153], v204 offset:23120\n"
"ds_read_b64_tr_b16 a[154:155], v204 offset:23360\n"
"v_mfma_f32_4x4x4_16b_bf16 v[108:111], v[124:125], a[106:107], v[108:111]\n"
"v_mfma_f32_16x16x32_bf16 v[16:19], a[180:183], v[64:67], v[16:19]\n"
"ds_read_b64_tr_b16 a[156:157], v204 offset:23160\n"
"ds_read_b64_tr_b16 a[158:159], v204 offset:23400\n"
"v_mfma_f32_4x4x4_16b_bf16 v[112:115], v[122:123], v[138:139], 0\n"
"v_mfma_f32_16x16x32_bf16 v[20:23], a[180:183], v[68:71], v[20:23]\n"
"ds_read_b64_tr_b16 a[160:161], v204 offset:23200\n"
"ds_read_b64_tr_b16 a[162:163], v204 offset:23440\n"
"v_mfma_f32_4x4x4_16b_bf16 v[116:119], v[126:127], v[138:139], 0\n"
"v_mfma_f32_16x16x32_bf16 v[24:27], a[184:187], v[64:67], v[24:27]\n"
"ds_read_b64_tr_b16 a[164:165], v204 offset:23240\n"
"ds_read_b64_tr_b16 a[166:167], v204 offset:23480\n"
"v_mfma_f32_4x4x4_16b_bf16 v[112:115], v[120:121], a[108:109], v[112:115]\n"
"v_mfma_f32_16x16x32_bf16 v[28:31], a[184:187], v[68:71], v[28:31]\n"
"ds_read_b64_tr_b16 a[168:169], v212 offset:23040\n"
"ds_read_b64_tr_b16 a[170:171], v212 offset:23280\n"
"v_mfma_f32_4x4x4_16b_bf16 v[116:119], v[124:125], a[110:111], v[116:119]\n"
"v_mfma_f32_16x16x32_bf16 v[32:35], v[64:67], a[188:191], v[32:35]\n"
"v_cvt_pk_bf16_f32 v56, v104, v106\n"
"v_cvt_pk_bf16_f32 v58, v105, v107\n"
"v_mfma_f32_16x16x32_bf16 v[36:39], v[68:71], a[188:191], v[36:39]\n"
"v_cvt_pk_bf16_f32 v57, v108, v110\n"
"v_cvt_pk_bf16_f32 v59, v109, v111\n"
"v_mfma_f32_16x16x32_bf16 v[40:43], v[64:67], a[192:195], v[40:43]\n"
"v_cvt_pk_bf16_f32 v60, v112, v114\n"
"v_cvt_pk_bf16_f32 v62, v113, v115\n"
"v_mfma_f32_16x16x32_bf16 v[44:47], v[68:71], a[192:195], v[44:47]\n"
"v_cvt_pk_bf16_f32 v61, v116, v118\n"
"v_cvt_pk_bf16_f32 v63, v117, v119\n"
"v_mfma_f32_16x16x32_bf16 v[48:51], a[196:199], v[64:67], v[48:51]\n"
"v_mfma_f32_16x16x32_bf16 v[52:55], a[196:199], v[68:71], v[52:55]\n"
"s_waitcnt lgkmcnt(0)\n"
"v_mfma_f32_16x16x32_bf16 v[0:3], a[144:147], v[56:59], v[0:3]\n"
"ds_read_b128 v[120:123], v215 offset:8192\n"
"ds_read_b128 v[124:127], v215 offset:8256\n"
"v_mfma_f32_4x4x4_16b_bf16 v[104:107], v[130:131], v[136:137], 0\n"
"v_mfma_f32_16x16x32_bf16 v[4:7], a[144:147], v[60:63], v[4:7]\n"
"ds_read_b64_tr_b16 a[172:173], v208 offset:59904\n"
"ds_read_b64_tr_b16 a[174:175], v208 offset:60144\n"
"v_mfma_f32_4x4x4_16b_bf16 v[108:111], v[134:135], v[136:137], 0\n"
"v_mfma_f32_16x16x32_bf16 v[8:11], a[148:151], v[56:59], v[8:11]\n"
"ds_read_b64_tr_b16 a[176:177], v208 offset:59944\n"
"ds_read_b64_tr_b16 a[178:179], v208 offset:60184\n"
"v_mfma_f32_4x4x4_16b_bf16 v[104:107], v[128:129], a[120:121], v[104:107]\n"
"v_mfma_f32_16x16x32_bf16 v[12:15], a[148:151], v[60:63], v[12:15]\n"
"ds_read_b64_tr_b16 a[180:181], v204 offset:59984\n"
"ds_read_b64_tr_b16 a[182:183], v204 offset:60224\n"
"v_mfma_f32_4x4x4_16b_bf16 v[108:111], v[132:133], a[122:123], v[108:111]\n"
"v_mfma_f32_16x16x32_bf16 v[16:19], a[152:155], v[56:59], v[16:19]\n"
"ds_read_b64_tr_b16 a[184:185], v204 offset:60024\n"
"ds_read_b64_tr_b16 a[186:187], v204 offset:60264\n"
"v_mfma_f32_4x4x4_16b_bf16 v[112:115], v[130:131], v[138:139], 0\n"
"v_mfma_f32_16x16x32_bf16 v[20:23], a[152:155], v[60:63], v[20:23]\n"
"ds_read_b64_tr_b16 a[188:189], v204 offset:60064\n"
"ds_read_b64_tr_b16 a[190:191], v204 offset:60304\n"
"v_mfma_f32_4x4x4_16b_bf16 v[116:119], v[134:135], v[138:139], 0\n"
"v_mfma_f32_16x16x32_bf16 v[24:27], a[156:159], v[56:59], v[24:27]\n"
"ds_read_b64_tr_b16 a[192:193], v204 offset:60104\n"
"ds_read_b64_tr_b16 a[194:195], v204 offset:60344\n"
"v_mfma_f32_4x4x4_16b_bf16 v[112:115], v[128:129], a[124:125], v[112:115]\n"
"v_mfma_f32_16x16x32_bf16 v[28:31], a[156:159], v[60:63], v[28:31]\n"
"ds_read_b64_tr_b16 a[196:197], v212 offset:59904\n"
"ds_read_b64_tr_b16 a[198:199], v212 offset:60144\n"
"v_mfma_f32_4x4x4_16b_bf16 v[116:119], v[132:133], a[126:127], v[116:119]\n"
"v_mfma_f32_16x16x32_bf16 v[32:35], v[56:59], a[160:163], v[32:35]\n"
"v_cvt_pk_bf16_f32 v64, v104, v106\n"
"v_cvt_pk_bf16_f32 v66, v105, v107\n"
"v_mfma_f32_16x16x32_bf16 v[36:39], v[60:63], a[160:163], v[36:39]\n"
"v_cvt_pk_bf16_f32 v65, v108, v110\n"
"v_cvt_pk_bf16_f32 v67, v109, v111\n"
"v_mfma_f32_16x16x32_bf16 v[40:43], v[56:59], a[164:167], v[40:43]\n"
"v_cvt_pk_bf16_f32 v68, v112, v114\n"
"v_cvt_pk_bf16_f32 v70, v113, v115\n"
"v_mfma_f32_16x16x32_bf16 v[44:47], v[60:63], a[164:167], v[44:47]\n"
"v_cvt_pk_bf16_f32 v69, v116, v118\n"
"v_cvt_pk_bf16_f32 v71, v117, v119\n"
"v_mfma_f32_16x16x32_bf16 v[48:51], a[168:171], v[56:59], v[48:51]\n"
"v_mfma_f32_16x16x32_bf16 v[52:55], a[168:171], v[60:63], v[52:55]\n"
"s_waitcnt lgkmcnt(0)\n"
"v_mfma_f32_16x16x32_bf16 v[0:3], a[172:175], v[64:67], v[0:3]\n"
"ds_read_b128 v[128:131], v215 offset:8704\n"
"ds_read_b128 v[132:135], v215 offset:8768\n"
"v_mfma_f32_4x4x4_16b_bf16 v[104:107], v[122:123], v[136:137], 0\n"
"v_mfma_f32_16x16x32_bf16 v[4:7], a[172:175], v[68:71], v[4:7]\n"
"s_waitcnt vmcnt(0)\n"
"s_barrier\n"
"ds_read_b64_tr_b16 a[144:145], v209 offset:30720\n"
"ds_read_b64_tr_b16 a[146:147], v209 offset:30960\n"
"v_mfma_f32_4x4x4_16b_bf16 v[108:111], v[126:127], v[136:137], 0\n"
"v_mfma_f32_16x16x32_bf16 v[8:11], a[176:179], v[64:67], v[8:11]\n"
"ds_read_b64_tr_b16 a[148:149], v209 offset:30760\n"
"ds_read_b64_tr_b16 a[150:151], v209 offset:31000\n"
"v_mfma_f32_4x4x4_16b_bf16 v[104:107], v[120:121], a[128:129], v[104:107]\n"
"v_mfma_f32_16x16x32_bf16 v[12:15], a[176:179], v[68:71], v[12:15]\n"
"ds_read_b64_tr_b16 a[152:153], v205 offset:30800\n"
"ds_read_b64_tr_b16 a[154:155], v205 offset:31040\n"
"v_mfma_f32_4x4x4_16b_bf16 v[108:111], v[124:125], a[130:131], v[108:111]\n"
"v_mfma_f32_16x16x32_bf16 v[16:19], a[180:183], v[64:67], v[16:19]\n"
"ds_read_b64_tr_b16 a[156:157], v205 offset:30840\n"
"ds_read_b64_tr_b16 a[158:159], v205 offset:31080\n"
"v_mfma_f32_4x4x4_16b_bf16 v[112:115], v[122:123], v[138:139], 0\n"
"v_mfma_f32_16x16x32_bf16 v[20:23], a[180:183], v[68:71], v[20:23]\n"
"ds_read_b64_tr_b16 a[160:161], v205 offset:30880\n"
"ds_read_b64_tr_b16 a[162:163], v205 offset:31120\n"
"v_mfma_f32_4x4x4_16b_bf16 v[116:119], v[126:127], v[138:139], 0\n"
"v_mfma_f32_16x16x32_bf16 v[24:27], a[184:187], v[64:67], v[24:27]\n"
"ds_read_b64_tr_b16 a[164:165], v205 offset:30920\n"
"ds_read_b64_tr_b16 a[166:167], v205 offset:31160\n"
"v_mfma_f32_4x4x4_16b_bf16 v[112:115], v[120:121], a[132:133], v[112:115]\n"
"v_mfma_f32_16x16x32_bf16 v[28:31], a[184:187], v[68:71], v[28:31]\n"
"ds_read_b64_tr_b16 a[168:169], v213 offset:30720\n"
"ds_read_b64_tr_b16 a[170:171], v213 offset:30960\n"
"v_mfma_f32_4x4x4_16b_bf16 v[116:119], v[124:125], a[134:135], v[116:119]\n"
"v_mfma_f32_16x16x32_bf16 v[32:35], v[64:67], a[188:191], v[32:35]\n"
"v_cvt_pk_bf16_f32 v56, v104, v106\n"
"v_cvt_pk_bf16_f32 v58, v105, v107\n"
"v_mfma_f32_16x16x32_bf16 v[36:39], v[68:71], a[188:191], v[36:39]\n"
"v_cvt_pk_bf16_f32 v57, v108, v110\n"
"v_cvt_pk_bf16_f32 v59, v109, v111\n"
"v_mfma_f32_16x16x32_bf16 v[40:43], v[64:67], a[192:195], v[40:43]\n"
"v_cvt_pk_bf16_f32 v60, v112, v114\n"
"v_cvt_pk_bf16_f32 v62, v113, v115\n"
"v_mfma_f32_16x16x32_bf16 v[44:47], v[68:71], a[192:195], v[44:47]\n"
"v_cvt_pk_bf16_f32 v61, v116, v118\n"
"v_cvt_pk_bf16_f32 v63, v117, v119\n"
"v_mfma_f32_16x16x32_bf16 v[48:51], a[196:199], v[64:67], v[48:51]\n"
"v_mfma_f32_16x16x32_bf16 v[52:55], a[196:199], v[68:71], v[52:55]\n"
"s_waitcnt lgkmcnt(0)\n"
"v_mfma_f32_16x16x32_bf16 v[0:3], a[144:147], v[56:59], v[0:3]\n"
"ds_read_b64_tr_b16 a[172:173], v210 offset:30720\n"
"ds_read_b64_tr_b16 a[174:175], v210 offset:30960\n"
"v_mfma_f32_4x4x4_16b_bf16 v[104:107], v[130:131], v[136:137], 0\n"
"v_mfma_f32_16x16x32_bf16 v[4:7], a[144:147], v[60:63], v[4:7]\n"
"ds_read_b64_tr_b16 a[176:177], v210 offset:30760\n"
"ds_read_b64_tr_b16 a[178:179], v210 offset:31000\n"
"v_mfma_f32_4x4x4_16b_bf16 v[108:111], v[134:135], v[136:137], 0\n"
"v_mfma_f32_16x16x32_bf16 v[8:11], a[148:151], v[56:59], v[8:11]\n"
"ds_read_b64_tr_b16 a[180:181], v206 offset:30800\n"
"ds_read_b64_tr_b16 a[182:183], v206 offset:31040\n"
"v_mfma_f32_4x4x4_16b_bf16 v[104:107], v[128:129], a[136:137], v[104:107]\n"
"v_mfma_f32_16x16x32_bf16 v[12:15], a[148:151], v[60:63], v[12:15]\n"
"ds_read_b64_tr_b16 a[184:185], v206 offset:30840\n"
"ds_read_b64_tr_b16 a[186:187], v206 offset:31080\n"
"v_mfma_f32_4x4x4_16b_bf16 v[108:111], v[132:133], a[138:139], v[108:111]\n"
"v_mfma_f32_16x16x32_bf16 v[16:19], a[152:155], v[56:59], v[16:19]\n"
"ds_read_b64_tr_b16 a[188:189], v206 offset:30880\n"
"ds_read_b64_tr_b16 a[190:191], v206 offset:31120\n"
"v_mfma_f32_4x4x4_16b_bf16 v[112:115], v[130:131], v[138:139], 0\n"
"v_mfma_f32_16x16x32_bf16 v[20:23], a[152:155], v[60:63], v[20:23]\n"
"ds_read_b64_tr_b16 a[192:193], v206 offset:30920\n"
"ds_read_b64_tr_b16 a[194:195], v206 offset:31160\n"
"v_mfma_f32_4x4x4_16b_bf16 v[116:119], v[134:135], v[138:139], 0\n"
"v_mfma_f32_16x16x32_bf16 v[24:27], a[156:159], v[56:59], v[24:27]\n"
"ds_read_b64_tr_b16 a[196:197], v214 offset:30720\n"
"ds_read_b64_tr_b16 a[198:199], v214 offset:30960\n"
"v_mfma_f32_4x4x4_16b_bf16 v[112:115], v[128:129], a[140:141], v[112:115]\n"
"v_mfma_f32_16x16x32_bf16 v[28:31], a[156:159], v[60:63], v[28:31]\n"
"v_mfma_f32_4x4x4_16b_bf16 v[116:119], v[132:133], a[142:143], v[116:119]\n"
"v_mfma_f32_16x16x32_bf16 v[32:35], v[56:59], a[160:163], v[32:35]\n"
"v_cvt_pk_bf16_f32 v64, v104, v106\n"
"v_cvt_pk_bf16_f32 v66, v105, v107\n"
"v_mfma_f32_16x16x32_bf16 v[36:39], v[60:63], a[160:163], v[36:39]\n"
"v_cvt_pk_bf16_f32 v65, v108, v110\n"
"v_cvt_pk_bf16_f32 v67, v109, v111\n"
"v_mfma_f32_16x16x32_bf16 v[40:43], v[56:59], a[164:167], v[40:43]\n"
"v_cvt_pk_bf16_f32 v68, v112, v114\n"
"v_cvt_pk_bf16_f32 v70, v113, v115\n"
"v_mfma_f32_16x16x32_bf16 v[44:47], v[60:63], a[164:167], v[44:47]\n"
"v_cvt_pk_bf16_f32 v69, v116, v118\n"
"v_cvt_pk_bf16_f32 v71, v117, v119\n"
"v_mfma_f32_16x16x32_bf16 v[48:51], a[168:171], v[56:59], v[48:51]\n"
"v_mfma_f32_16x16x32_bf16 v[52:55], a[168:171], v[60:63], v[52:55]\n"
"s_waitcnt lgkmcnt(0)\n"
"v_mfma_f32_16x16x32_bf16 v[48:51], a[196:199], v[64:67], v[48:51]\n"
"v_mfma_f32_16x16x32_bf16 v[52:55], a[196:199], v[68:71], v[52:55]\n"
"v_mfma_f32_16x16x32_bf16 v[0:3], a[172:175], v[64:67], v[0:3]\n"
"v_mfma_f32_16x16x32_bf16 v[4:7], a[172:175], v[68:71], v[4:7]\n"
"v_mfma_f32_16x16x32_bf16 v[16:19], a[180:183], v[64:67], v[16:19]\n"
"v_mfma_f32_16x16x32_bf16 v[20:23], a[180:183], v[68:71], v[20:23]\n"
"v_mfma_f32_16x16x32_bf16 v[32:35], v[64:67], a[188:191], v[32:35]\n"
"v_mfma_f32_16x16x32_bf16 v[36:39], v[68:71], a[188:191], v[36:39]\n"
"v_mfma_f32_16x16x32_bf16 v[8:11], a[176:179], v[64:67], v[8:11]\n"
"v_mfma_f32_16x16x32_bf16 v[12:15], a[176:179], v[68:71], v[12:15]\n"
"v_mfma_f32_16x16x32_bf16 v[24:27], a[184:187], v[64:67], v[24:27]\n"
"v_mfma_f32_16x16x32_bf16 v[28:31], a[184:187], v[68:71], v[28:31]\n"
"v_mfma_f32_16x16x32_bf16 v[40:43], v[64:67], a[192:195], v[40:43]\n"
"v_mfma_f32_16x16x32_bf16 v[44:47], v[68:71], a[192:195], v[44:47]\n"
"v_mul_f32 v48, v152, v48\n"
"v_mul_f32 v49, v152, v49\n"
"v_mul_f32 v50, v152, v50\n"
"v_mul_f32 v51, v152, v51\n"
"v_mul_f32 v52, v153, v52\n"
"v_mul_f32 v53, v153, v53\n"
"v_mul_f32 v54, v153, v54\n"
"v_mul_f32 v55, v153, v55\n"
"v_cvt_pk_bf16_f32 v180, v48, v49\n"
"v_cvt_pk_bf16_f32 v181, v52, v53\n"
"v_cvt_pk_bf16_f32 v182, v50, v51\n"
"v_cvt_pk_bf16_f32 v183, v54, v55\n"
"v_mov_b32 v184, 0x3f803f80\n"
"v_mov_b32 v185, 0x3f803f80\n"
"v_permlane16_swap_b32 v180, v181\n"
"v_permlane16_swap_b32 v182, v183\n"
"v_mov_b32 v186, 0x3f803f80\n"
"v_mov_b32 v187, 0x3f803f80\n"
"v_permlane32_swap_b32 v180, v181\n"
"v_permlane32_swap_b32 v182, v183\n"
"buffer_store_dwordx4 v[184:187], v218, s[84:87], s25 offen\n"
"buffer_store_dword v180, v219, s[84:87], 0 offen\n"
"buffer_store_dword v182, v220, s[84:87], 0 offen\n"
"v_permlane32_swap_b32 v181, v183\n"
"s_nop 0\n"
"buffer_store_short v181, v221, s[84:87], s25 offen\n"
"buffer_store_short_d16_hi v181, v221, s[84:87], s25 offen offset:16\n"
"v_mul_f32 v0, v142, v0\n"
"v_mul_f32 v1, v142, v1\n"
"v_mul_f32 v2, v142, v2\n"
"v_mul_f32 v3, v142, v3\n"
"v_mul_f32 v4, v143, v4\n"
"v_mul_f32 v5, v143, v5\n"
"v_mul_f32 v6, v143, v6\n"
"v_mul_f32 v7, v143, v7\n"
"v_cvt_pk_bf16_f32 v156, v0, v1\n"
"v_cvt_pk_bf16_f32 v157, v2, v3\n"
"v_cvt_pk_bf16_f32 v158, v4, v5\n"
"v_cvt_pk_bf16_f32 v159, v6, v7\n"
"s_nop 0\n"
"v_permlane16_swap_b32 v156, v158\n"
"v_permlane16_swap_b32 v157, v159\n"
"buffer_store_dwordx4 v[156:159], v216, s[84:87], s24 offen\n"
"v_mul_f32 v16, v140, v16\n"
"v_mul_f32 v17, v140, v17\n"
"v_mul_f32 v18, v140, v18\n"
"v_mul_f32 v19, v140, v19\n"
"v_mul_f32 v20, v141, v20\n"
"v_mul_f32 v21, v141, v21\n"
"v_mul_f32 v22, v141, v22\n"
"v_mul_f32 v23, v141, v23\n"
"v_cvt_pk_bf16_f32 v164, v16, v17\n"
"v_cvt_pk_bf16_f32 v165, v18, v19\n"
"v_cvt_pk_bf16_f32 v166, v20, v21\n"
"v_cvt_pk_bf16_f32 v167, v22, v23\n"
"s_nop 0\n"
"v_permlane16_swap_b32 v164, v166\n"
"v_permlane16_swap_b32 v165, v167\n"
"buffer_store_dwordx4 v[164:167], v216, s[84:87], s25 offen\n"
"v_mul_f32 v32, v144, v32\n"
"v_mul_f32 v33, v145, v33\n"
"v_mul_f32 v34, v146, v34\n"
"v_mul_f32 v35, v147, v35\n"
"v_mul_f32 v36, v148, v36\n"
"v_mul_f32 v37, v149, v37\n"
"v_mul_f32 v38, v150, v38\n"
"v_mul_f32 v39, v151, v39\n"
"v_cvt_pk_bf16_f32 v172, v32, v33\n"
"v_cvt_pk_bf16_f32 v173, v34, v35\n"
"v_cvt_pk_bf16_f32 v174, v36, v37\n"
"v_cvt_pk_bf16_f32 v175, v38, v39\n"
"s_nop 0\n"
"v_permlane16_swap_b32 v172, v174\n"
"v_permlane16_swap_b32 v173, v175\n"
"buffer_store_dwordx4 v[172:175], v217, s[84:87], s25 offen offset:1024\n"
"v_mul_f32 v8, v142, v8\n"
"v_mul_f32 v9, v142, v9\n"
"v_mul_f32 v10, v142, v10\n"
"v_mul_f32 v11, v142, v11\n"
"v_mul_f32 v12, v143, v12\n"
"v_mul_f32 v13, v143, v13\n"
"v_mul_f32 v14, v143, v14\n"
"v_mul_f32 v15, v143, v15\n"
"v_cvt_pk_bf16_f32 v160, v8, v9\n"
"v_cvt_pk_bf16_f32 v161, v10, v11\n"
"v_cvt_pk_bf16_f32 v162, v12, v13\n"
"v_cvt_pk_bf16_f32 v163, v14, v15\n"
"s_add_u32 s24, s24, 65536\n"
"s_add_u32 s25, s25, 151552\n"
"v_permlane16_swap_b32 v160, v162\n"
"v_permlane16_swap_b32 v161, v163\n"
"buffer_store_dwordx4 v[160:163], v216, s[84:87], s24 offen\n"
"v_mul_f32 v24, v140, v24\n"
"v_mul_f32 v25, v140, v25\n"
"v_mul_f32 v26, v140, v26\n"
"v_mul_f32 v27, v140, v27\n"
"v_mul_f32 v28, v141, v28\n"
"v_mul_f32 v29, v141, v29\n"
"v_mul_f32 v30, v141, v30\n"
"v_mul_f32 v31, v141, v31\n"
"v_cvt_pk_bf16_f32 v168, v24, v25\n"
"v_cvt_pk_bf16_f32 v169, v26, v27\n"
"v_cvt_pk_bf16_f32 v170, v28, v29\n"
"v_cvt_pk_bf16_f32 v171, v30, v31\n"
"s_nop 0\n"
"v_permlane16_swap_b32 v168, v170\n"
"v_permlane16_swap_b32 v169, v171\n"
"buffer_store_dwordx4 v[168:171], v216, s[84:87], s25 offen\n"
"v_mul_f32 v40, v144, v40\n"
"v_mul_f32 v41, v145, v41\n"
"v_mul_f32 v42, v146, v42\n"
"v_mul_f32 v43, v147, v43\n"
"v_mul_f32 v44, v148, v44\n"
"v_mul_f32 v45, v149, v45\n"
"v_mul_f32 v46, v150, v46\n"
"v_mul_f32 v47, v151, v47\n"
"v_cvt_pk_bf16_f32 v176, v40, v41\n"
"v_cvt_pk_bf16_f32 v177, v42, v43\n"
"v_cvt_pk_bf16_f32 v178, v44, v45\n"
"v_cvt_pk_bf16_f32 v179, v46, v47\n"
"s_nop 0\n"
"v_permlane16_swap_b32 v176, v178\n"
"v_permlane16_swap_b32 v177, v179\n"
"buffer_store_dwordx4 v[176:179], v217, s[84:87], s25 offen offset:1024\n"
"s_mov_b32 s93, 1048576\n"
"v_cmp_ne_u32 vcc, s17, v224\n"
"v_cmp_ne_u32 s[94:95], s17, v225\n"
"s_or_b64 vcc, vcc, s[94:95]\n"
"v_cmp_ne_u32 s[96:97], s17, v226\n"
"v_cmp_ne_u32 s[94:95], s17, v227\n"
"s_or_b64 vcc, vcc, s[96:97]\n"
"s_or_b64 vcc, vcc, s[94:95]\n"
"s_cbranch_scc0 .Ls1_%=\n"
".Ls0_%=:\n"
"s_sleep 4\n"
"buffer_load_dwordx4 v[224:227], v216, s[80:83], s22 offen sc1\n"
"s_waitcnt vmcnt(0)\n"
"v_cmp_ne_u32 vcc, s17, v224\n"
"v_cmp_ne_u32 s[94:95], s17, v225\n"
"s_or_b64 vcc, vcc, s[94:95]\n"
"v_cmp_ne_u32 s[96:97], s17, v226\n"
"v_cmp_ne_u32 s[94:95], s17, v227\n"
"s_or_b64 vcc, vcc, s[96:97]\n"
"s_or_b64 vcc, vcc, s[94:95]\n"
"s_cselect_b32 s92, s93, 0\n"
"s_sub_u32 s93, s93, 1\n"
"s_cmp_lg_u32 s92, 0\n"
"s_cbranch_scc1 .Ls0_%=\n"
".Ls1_%=:\n"
"v_mov_b32 v224, s16\n"
"buffer_store_dword v224, off, s[80:83], s23 sc1\n"
    :
    : [ka] "s"(__builtin_amdgcn_kernarg_segment_ptr()), [b] "s"(b), [tid] "v"(tid), [lb] "s"((u32)(uintptr_t)sm)
    : "memory","scc","a0","a1","a2","a3","a4","a5","a6","a7","a8","a9","a10","a11","a12","a13","a14","a15","a16","a17","a18","a19","a20","a21","a22","a23","a24","a25","a26","a27","a28","a29","a30","a31","a32","a33","a34","a35","a36","a37","a38","a39","a40","a41","a42","a43","a44","a45","a46","a47","a48","a49","a50","a51","a52","a53","a54","a55","a56","a57","a58","a59","a60","a61","a62","a63","a64","a65","a66","a67","a68","a69","a70","a71","a72","a73","a74","a75","a76","a77","a78","a79","a80","a81","a82","a83","a84","a85","a86","a87","a88","a89","a90","a91","a92","a93","a94","a95","a96","a97","a98","a99","a100","a101","a102","a103","a104","a105","a106","a107","a108","a109","a110","a111","a112","a113","a114","a115","a116","a117","a118","a119","a120","a121","a122","a123","a124","a125","a126","a127","a128","a129","a130","a131","a132","a133","a134","a135","a136","a137","a138","a139","a140","a141","a142","a143","a144","a145","a146","a147","a148","a149","a150","a151","a152","a153","a154","a155","a156","a157","a158","a159","a160","a161","a162","a163","a164","a165","a166","a167","a168","a169","a170","a171","a172","a173","a174","a175","a176","a177","a178","a179","a180","a181","a182","a183","a184","a185","a186","a187","a188","a189","a190","a191","a192","a193","a194","a195","a196","a197","a198","a199","a200","a201","s4","s5","s6","s7","s8","s9","s10","s11","s12","s13","s14","s15","s16","s17","s18","s19","s20","s21","s22","s23","s24","s25","s26","s27","s28","s29","s30","s31","s64","s65","s66","s67","s68","s69","s70","s71","s72","s73","s74","s75","s76","s77","s78","s79","s80","s81","s82","s83","s84","s85","s86","s87","s88","s89","s90","s91","s92","s93","s94","s95","s96","s97","s98","s99","v0","vcc","v1","v2","v3","v4","v5","v6","v7","v8","v9","v10","v11","v12","v13","v14","v15","v16","v17","v18","v19","v20","v21","v22","v23","v24","v25","v26","v27","v28","v29","v30","v31","v32","v33","v34","v35","v36","v37","v38","v39","v40","v41","v42","v43","v44","v45","v46","v47","v48","v49","v50","v51","v52","v53","v54","v55","v56","v57","v58","v59","v60","v61","v62","v63","v64","v65","v66","v67","v68","v69","v70","v71","v72","v73","v74","v75","v76","v77","v78","v79","v80","v81","v82","v83","v84","v85","v86","v87","v88","v89","v90","v91","v92","v93","v94","v95","v96","v97","v98","v99","v100","v104","v105","v106","v107","v108","v109","v110","v111","v112","v113","v114","v115","v116","v117","v118","v119","v120","v121","v122","v123","v124","v125","v126","v127","v128","v129","v130","v131","v132","v133","v134","v135","v136","v137","v138","v139","v140","v141","v142","v143","v144","v145","v146","v147","v148","v149","v150","v151","v152","v153","v156","v157","v158","v159","v160","v161","v162","v163","v164","v165","v166","v167","v168","v169","v170","v171","v172","v173","v174","v175","v176","v177","v178","v179","v180","v181","v182","v183","v184","v185","v186","v187","v188","v189","v190","v191","v192","v193","v194","v195","v196","v197","v198","v199","v200","v201","v202","v203","v204","v205","v206","v207","v208","v209","v210","v211","v212","v213","v214","v215","v216","v217","v218","v219","v220","v221","v224","v225","v226","v227");
  if (b < 8 && tid == 0) {
    g_tab[8 * b] = u64x2{(uint64_t)ws, (uint64_t)x}; g_tab[8 * b + 1] = u64x2{(uint64_t)w1, (uint64_t)w2}; g_tab[8 * b + 2] = u64x2{(uint64_t)wo, (uint64_t)out};
  }
}

DEV void attn_slow(const char* ws, int h, int st, int l, float* o  ) {
  const int q = l & 31, g = l >> 5;
  float qv[18];
  {
    const char* qm = ws + O_QM + ((size_t)h * 64 + st) * 1024 + q * 16;
    u32x4 a = *(const u32x4*)qm, bq = *(const u32x4*)(qm + 512);
    u32 t = *(const u32*)(ws + O_QT + ((size_t)h * S_ + 32 * st + q) * 4);
#pragma unroll
    for (int j = 0; j < 4; j++) { qv[2 * j] = bflo(a[j]); qv[2 * j + 1] = bfhi(a[j]); qv[8 + 2 * j] = bflo(bq[j]); qv[9 + 2 * j] = bfhi(bq[j]); }
    qv[16] = bflo(t); qv[17] = bfhi(t);
  }
  float m = -__builtin_inff(), ls = 0;
#pragma unroll
  for (int j = 0; j < 10; j++) o[j] = 0;
#pragma unroll 1
  for (int k = 0; k < S_; k++) {
    const int Tt = (k >> 5) & 31, key = k & 31;
    const char* kv = ws + O_KV + (size_t)h * 151552 + (k >> 10) * KVH;
    const char* tile = kv + 4096 + Tt * KVT;
    u32x4 a = *(const u32x4*)(tile + key * 16), bk = *(const u32x4*)(tile + 512 + key * 16);
    u32 t = *(const u32*)(kv + (Tt >> 3) * 1024 + (((Tt >> 1) & 1) * 32 + key) * 16 + (2 * ((Tt >> 2) & 1) + (Tt & 1)) * 4);
    float s = qv[16] * bflo(t) + qv[17] * bfhi(t);
#pragma unroll
    for (int j = 0; j < 4; j++) s += qv[2 * j] * bflo(a[j]) + qv[2 * j + 1] * bfhi(a[j]) + qv[8 + 2 * j] * bflo(bk[j]) + qv[9 + 2 * j] * bfhi(bk[j]);
    const float mn = fmaxf(m, s);
    const float al = (m == mn) ? 1.f : exp2f(m - mn), pp = exp2f(s - mn);
    const char* vb = tile + 1024 + (key >> 3) * 19 * 16 + (key & 7) * 2;
    ls = ls * al + pp;
#pragma unroll
    for (int j = 0; j < 10; j++) { int d = j < 8 ? (j & 3) + 4 * g + 8 * (j >> 2) : 8 + j; o[j] = o[j] * al + pp * bf2f(*(const u16*)(vb + d * 16)); }
    m = mn;
  }
  const float inv = 1.f / ls;
#pragma unroll
  for (int j = 0; j < 10; j++) o[j] *= inv;
}

DEV u32 pick(u32 a, u32 b, int e) { return __builtin_amdgcn_perm(b, a, e ? 0x07060302u : 0x05040100u); }
DEV f32x4 mfma8(u32 a0, u32 a1, u32 b0, u32 b1) {
  return __builtin_amdgcn_mfma_f32_16x16x16bf16_1k(__builtin_bit_cast(s16x4, u32x2{a0, a1}), __builtin_bit_cast(s16x4, u32x2{b0, b1}), f32x4{0, 0, 0, 0}, 0, 0, 0);
}
#define WFPOL 16
DEV void st16(u32x4 o, __amdgpu_buffer_rsrc_t r, u32 vo, int so) {
  __builtin_amdgcn_raw_buffer_store_b128(o, r, vo, so, WFPOL);
  asm volatile("s_nop 1" :: "v"(o) : "memory");
}
struct W2u { u32x2 v[8]; };
DEV void w2_load(W2u& r, __amdgpu_buffer_rsrc_t rs, int j, u32 vo) {
  const int kap = j / 9, q = j % 9;
  int wa, Ta, wb, Tb; kap_tiles(kap, wa, Ta, wb, Tb);
  const int ga = 9 * wa + Ta, gb = 9 * wb + Tb;
  const int sa = (32 * (ga >> 1) + (ga & 1)) * 1152 + q * 128, sb = (32 * (gb >> 1) + (gb & 1)) * 1152 + q * 128;
#pragma unroll
  for (int i = 0; i < 8; i++) r.v[i] = __builtin_amdgcn_raw_buffer_load_b64(rs, vo, (i < 4 ? sa : sb) + (i & 3) * 2304, 0);
}
DEV void w2_put(const W2u& r, __amdgpu_buffer_rsrc_t rw, int j, int l) {
  const int kap = j / 9, q = j % 9, n = kap < 8 ? (kap >> 1) + 4 * (kap & 1) : (kap < 10 ? kap : 10 + ((kap - 10) >> 1) + 4 * (kap & 1));
  const int z = n - NA_, c0 = z < 0 ? 9 * n + 4 * (q & 1) : 9 * NA_ + ((q & 1) ? 3 * ND_ + 2 * z : 3 * z), c1 = z < 0 ? 2 : ((q & 1) ? 4 * ND_ : 5 * ND_ - z);
  const int f0 = 171 + (q < 8 ? (q >> 1) * 333 + c0 : (z < 0 ? 9 * n + 8 : 9 * NA_ + 3 * z + 2)), m1 = q < 8 ? 1 : 333, m2 = q < 8 ? c1 : 666;
#pragma unroll
  for (int e = 0; e < 4; e++) {
    const u32x4 o = {pick(r.v[0][e >> 1], r.v[1][e >> 1], e & 1), pick(r.v[2][e >> 1], r.v[3][e >> 1], e & 1),
                     pick(r.v[4][e >> 1], r.v[5][e >> 1], e & 1), pick(r.v[6][e >> 1], r.v[7][e >> 1], e & 1)};
    st16(o, rw, l * 16, (f0 + m2 * (e >> 1) + m1 * (e & 1)) * 1024);
  }
}
DEV u32 w2_touch(__amdgpu_buffer_rsrc_t rw, int j, int l) {
  const int kap = j / 9, q = j % 9, n = kap < 8 ? (kap >> 1) + 4 * (kap & 1) : (kap < 10 ? kap : 10 + ((kap - 10) >> 1) + 4 * (kap & 1));
  const int z = n - NA_, c0 = z < 0 ? 9 * n + 4 * (q & 1) : 9 * NA_ + ((q & 1) ? 3 * ND_ + 2 * z : 3 * z), c1 = z < 0 ? 2 : ((q & 1) ? 4 * ND_ : 5 * ND_ - z);
  const int f0 = 171 + (q < 8 ? (q >> 1) * 333 + c0 : (z < 0 ? 9 * n + 8 : 9 * NA_ + 3 * z + 2)), m1 = q < 8 ? 1 : 333, m2 = q < 8 ? c1 : 666;
  const int e = (l >> 3) & 3;
  return __builtin_amdgcn_raw_buffer_load_b32(rw, (m2 * (e >> 1) + m1 * (e & 1)) * 1024 + (l & 7) * 128, f0 * 1024, 0);
}
DEV void ffn_touch(char* __restrict__ ws, int s, int u, int l) {
  const __amdgpu_buffer_rsrc_t rw = __builtin_amdgcn_make_buffer_rsrc(ws + O_WF + (size_t)s * 4 * 333 * 1024, 0, 4 * 333 * 1024, 0x00020000);
  u32 t0 = 0, t1 = 0, t2 = 0;
  if (u < 108) {
    const int pp = u / 6, grp = u % 6, e = l >> 5, d = min((l >> 3) & 3, 2);
    const int G = 2 * pp + e, w = G / 9, T = G % 9, fb = w * 333 + (T < 5 ? T : 90 + T), fm = T < 5 ? 5 : 4;
    t0 = w2_touch(rw, u, l);
    t1 = __builtin_amdgcn_raw_buffer_load_b32(rw, (fb + fm * (3 * grp + d)) * 1024 + (l & 7) * 128, 0, 0);
  } else if (u < 126) { t0 = w2_touch(rw, 3 * u - 216, l); t1 = w2_touch(rw, 3 * u - 215, l); t2 = w2_touch(rw, 3 * u - 214, l); }
  if (u >= 126) t1 = __builtin_amdgcn_raw_buffer_load_b32(__builtin_amdgcn_make_buffer_rsrc((char*)g_tab, 0, 1024, 0x00020000), 0, 128 * s, 0);
  asm volatile("" :: "v"(t0), "v"(t1), "v"(t2));
}
DEV void ffn_tiles(const u16* __restrict__ w1, const u16* __restrict__ w2, char* __restrict__ ws, int s, int u, int l) {
  const int n = l & 15, kg = l >> 4;
  char* wf = ws + O_WF + (size_t)s * 4 * 333 * 1024;
  const __amdgpu_buffer_rsrc_t rw = __builtin_amdgcn_make_buffer_rsrc(wf, 0, 4 * 333 * 1024, 0x00020000);
  const __amdgpu_buffer_rsrc_t r2 = __builtin_amdgcn_make_buffer_rsrc((char*)w2 + (size_t)s * 576 * 1152, 0, 576 * 1152, 0x00020000);
  const u32 vo2 = kg * 8 * 1152 + n * 8;
  if (u < 108) {
    const int pp = u / 6, grp = u % 6;
    const __amdgpu_buffer_rsrc_t r1 = __builtin_amdgcn_make_buffer_rsrc((char*)w1, 0, D_ * F_ * 2, 0x00020000);
    const int so = 96 * grp * (F_ * 2) + (576 * s + 32 * pp) * 2;
    const u32 vo = kg * 8 * (F_ * 2) + n * 4;
    u32 v[3][8]; u32x4 sc[3], sb[3]; W2u r;
    w2_load(r, r2, u, vo2);
#pragma unroll
    for (int d = 0; d < 3; d++) {
      sb[d] = *(const u32x4*)(ws + O_MH + 3 * 1152 + (96 * grp + 32 * d) * 2 + kg * 16);
      sc[d] = *(const u32x4*)(ws + O_MH + 4 * 1152 + (96 * grp + 32 * d) * 2 + kg * 16);
#pragma unroll
      for (int j = 0; j < 8; j++) v[d][j] = __builtin_amdgcn_raw_buffer_load_b32(r1, vo, so + (32 * d + j) * (F_ * 2), 0);
    }
    __builtin_amdgcn_sched_barrier(0);
    w2_put(r, rw, u, l);
    const u32 mk = kg == (n >> 2) ? ((n & 1) ? 0xffff0000u : 0xffffu) : 0, m0 = (n & 2) ? 0 : mk, m1 = (n & 2) ? mk : 0;
    const u32x4 ones = {0x3f803f80u, 0x3f803f80u, 0x3f803f80u, 0x3f803f80u};
#pragma unroll
    for (int e = 0; e < 2; e++) {
      const int G = 2 * pp + e, w = G / 9, T = G % 9, fb = w * 333 + (T < 5 ? T : 90 + T), fm = T < 5 ? 5 : 4;
      f32x4 ua = {0, 0, 0, 0}, ta = {0, 0, 0, 0};
#pragma unroll
      for (int d = 0; d < 3; d++) {
        const u32x4 r = {pick(v[d][0], v[d][1], e), pick(v[d][2], v[d][3], e), pick(v[d][4], v[d][5], e), pick(v[d][6], v[d][7], e)};
        const f32x4 d0 = mfma8(sc[d][0] & m0, sc[d][1] & m1, r[0], r[1]), d1 = mfma8(sc[d][2] & m0, sc[d][3] & m1, r[2], r[3]);
        ta = mfma16(sb[d], r, ta);
        const u32x4 o = {pk2(d0[0], d0[1]), pk2(d0[2], d0[3]), pk2(d1[0], d1[1]), pk2(d1[2], d1[3])};
        st16(o, rw, l * 16, (fb + fm * (3 * grp + d)) * 1024);
        ua = mfma16(ones, o, ua);
      }
      const u16 uh = f2bf(ua[0]), ul = f2bf(ua[0] - bf2f(uh)), th = f2bf(ta[0]);
      const int xo = (fb + fm * 18) * 1024;
      __builtin_amdgcn_raw_buffer_store_b16(kg < 2 ? uh : (kg == 2 ? ul : th), rw, l * 16, xo + grp * 2, 0);
      if (kg == 0) __builtin_amdgcn_raw_buffer_store_b16(th, rw, l * 16, xo + (grp >> 1) * 256 + (6 + (grp & 1)) * 2, 0);
      if (kg == 3 && grp == 0) __builtin_amdgcn_raw_buffer_store_b32(0, rw, l * 16, xo + 12, 0);
    }
  } else if (u < 126) {
    W2u r[3];
#pragma unroll
    for (int i = 0; i < 3; i++) w2_load(r[i], r2, 3 * u - 216 + i, vo2);
    __builtin_amdgcn_sched_barrier(0);
#pragma unroll
    for (int i = 0; i < 3; i++) w2_put(r[i], rw, 3 * u - 216 + i, l);
  }
}

DEV void wo_tile(const u16* __restrict__ wo, char* __restrict__ ws, int f, int l) {
  const int n = l & 15, kg = l >> 4, ks = (f / 3) % 18, ct = 3 * (f / 54) + f % 3;
  u32 v[8];
#pragma unroll
  for (int j = 0; j < 8; j++) v[j] = wo[wo_row(ks, kg, j) * D_ + 16 * ct + n];
  st16(u32x4{v[0] | v[1] << 16, v[2] | v[3] << 16, v[4] | v[5] << 16, v[6] | v[7] << 16}, __builtin_amdgcn_make_buffer_rsrc(ws + O_WO, 0, 663552, 0x00020000), l * 16, f * 1024);
}

__global__ __launch_bounds__(768) void k3_attn(const u16*, const u16*, const u16*, char*) {
  __shared__ __attribute__((aligned(16))) char sm[151552];
  int b = blockIdx.x, tid = threadIdx.x;
  uint64_t pw = TAB(0);
  int w = __builtin_amdgcn_readfirstlane(tid >> 6);
  const u32 lb = (u32)(uintptr_t)sm, vl = (tid & 63) * 16;
  u32x4 q0, q1; u32 t0, t1;
  if (w >= 8) {
    const u32 ko = ((w >> 1) & 1) * KVH + (w & 1) * 1024;
    const uint64_t kb = pw + (u32)(O_KV + (4 * (b & 7) + ((b >> 3) & 3)) * 151552 + ko);
    asm volatile(
"s_nop 4\n"
"s_add_u32 m0, %[kl], 0\n"
"s_add_u32 s20, %[kb0], 0\n"
"s_addc_u32 s21, %[kb1], 0\n"
"global_load_lds_dwordx4 %[vl], s[20:21] offset:0\n"
"s_add_u32 m0, %[kl], 4096\n"
"s_add_u32 s20, %[kb0], 4096\n"
"s_addc_u32 s21, %[kb1], 0\n"
"global_load_lds_dwordx4 %[vl], s[20:21] offset:0\n"
"global_load_lds_dwordx4 %[vl], s[20:21] offset:2048\n"
"s_add_u32 m0, %[kl], 8192\n"
"s_add_u32 s20, %[kb0], 8192\n"
"s_addc_u32 s21, %[kb1], 0\n"
"global_load_lds_dwordx4 %[vl], s[20:21] offset:0\n"
"global_load_lds_dwordx4 %[vl], s[20:21] offset:2048\n"
"s_add_u32 m0, %[kl], 12288\n"
"s_add_u32 s20, %[kb0], 12288\n"
"s_addc_u32 s21, %[kb1], 0\n"
"global_load_lds_dwordx4 %[vl], s[20:21] offset:0\n"
"global_load_lds_dwordx4 %[vl], s[20:21] offset:2048\n"
"s_add_u32 m0, %[kl], 16384\n"
"s_add_u32 s20, %[kb0], 16384\n"
"s_addc_u32 s21, %[kb1], 0\n"
"global_load_lds_dwordx4 %[vl], s[20:21] offset:0\n"
"s_waitcnt vmcnt(4)\n"
"s_barrier\n"
"global_load_lds_dwordx4 %[vl], s[20:21] offset:2048\n"
"s_add_u32 m0, %[kl], 20480\n"
"s_add_u32 s20, %[kb0], 20480\n"
"s_addc_u32 s21, %[kb1], 0\n"
"global_load_lds_dwordx4 %[vl], s[20:21] offset:0\n"
"global_load_lds_dwordx4 %[vl], s[20:21] offset:2048\n"
"s_add_u32 m0, %[kl], 2048\n"
"s_add_u32 s20, %[kb0], 2048\n"
"s_addc_u32 s21, %[kb1], 0\n"
"global_load_lds_dwordx4 %[vl], s[20:21] offset:0\n"
"s_add_u32 m0, %[kl], 24576\n"
"s_add_u32 s20, %[kb0], 24576\n"
"s_addc_u32 s21, %[kb1], 0\n"
"global_load_lds_dwordx4 %[vl], s[20:21] offset:0\n"
"global_load_lds_dwordx4 %[vl], s[20:21] offset:2048\n"
"s_add_u32 m0, %[kl], 28672\n"
"s_add_u32 s20, %[kb0], 28672\n"
"s_addc_u32 s21, %[kb1], 0\n"
"global_load_lds_dwordx4 %[vl], s[20:21] offset:0\n"
"global_load_lds_dwordx4 %[vl], s[20:21] offset:2048\n"
"s_add_u32 m0, %[kl], 32768\n"
"s_add_u32 s20, %[kb0], 32768\n"
"s_addc_u32 s21, %[kb1], 0\n"
"global_load_lds_dwordx4 %[vl], s[20:21] offset:0\n"
"global_load_lds_dwordx4 %[vl], s[20:21] offset:2048\n"
"s_add_u32 m0, %[kl], 36864\n"
"s_add_u32 s20, %[kb0], 36864\n"
"s_addc_u32 s21, %[kb1], 0\n"
"global_load_lds_dwordx4 %[vl], s[20:21] offset:0\n"
"global_load_lds_dwordx4 %[vl], s[20:21] offset:2048\n"
"s_waitcnt vmcnt(9)\n"
"s_barrier\n"
"s_add_u32 m0, %[kl], 40960\n"
"s_add_u32 s20, %[kb0], 40960\n"
"s_addc_u32 s21, %[kb1], 0\n"
"global_load_lds_dwordx4 %[vl], s[20:21] offset:0\n"
"global_load_lds_dwordx4 %[vl], s[20:21] offset:2048\n"
"s_add_u32 m0, %[kl], 45056\n"
"s_add_u32 s20, %[kb0], 45056\n"
"s_addc_u32 s21, %[kb1], 0\n"
"global_load_lds_dwordx4 %[vl], s[20:21] offset:0\n"
"global_load_lds_dwordx4 %[vl], s[20:21] offset:2048\n"
"s_add_u32 m0, %[kl], 49152\n"
"s_add_u32 s20, %[kb0], 49152\n"
"s_addc_u32 s21, %[kb1], 0\n"
"global_load_lds_dwordx4 %[vl], s[20:21] offset:0\n"
"global_load_lds_dwordx4 %[vl], s[20:21] offset:2048\n"
"s_add_u32 m0, %[kl], 53248\n"
"s_add_u32 s20, %[kb0], 53248\n"
"s_addc_u32 s21, %[kb1], 0\n"
"global_load_lds_dwordx4 %[vl], s[20:21] offset:0\n"
"global_load_lds_dwordx4 %[vl], s[20:21] offset:2048\n"
"s_add_u32 m0, %[kl], 57344\n"
"s_add_u32 s20, %[kb0], 57344\n"
"s_addc_u32 s21, %[kb1], 0\n"
"global_load_lds_dwordx4 %[vl], s[20:21] offset:0\n"
"global_load_lds_dwordx4 %[vl], s[20:21] offset:2048\n"
"s_add_u32 m0, %[kl], 61440\n"
"s_add_u32 s20, %[kb0], 61440\n"
"s_addc_u32 s21, %[kb1], 0\n"
"global_load_lds_dwordx4 %[vl], s[20:21] offset:0\n"
"global_load_lds_dwordx4 %[vl], s[20:21] offset:2048\n"
"s_add_u32 m0, %[kl], 65536\n"
"s_add_u32 s20, %[kb0], 65536\n"
"s_addc_u32 s21, %[kb1], 0\n"
"global_load_lds_dwordx4 %[vl], s[20:21] offset:0\n"
"global_load_lds_dwordx4 %[vl], s[20:21] offset:2048\n"
"s_add_u32 m0, %[kl], 69632\n"
"s_add_u32 s20, %[kb0], 69632\n"
"s_addc_u32 s21, %[kb1], 0\n"
"global_load_lds_dwordx4 %[vl], s[20:21] offset:0\n"
"global_load_lds_dwordx4 %[vl], s[20:21] offset:2048\n"
"s_add_u32 m0, %[kl], 73728\n"
"s_add_u32 s20, %[kb0], 73728\n"
"s_addc_u32 s21, %[kb1], 0\n"
"global_load_lds_dwordx4 %[vl], s[20:21] offset:0\n"
"s_waitcnt vmcnt(0)\n"
      :
      : [kb0] "s"((u32)kb), [kb1] "s"((u32)(kb >> 32)), [kl] "s"(lb + ko), [vl] "v"(vl)
      : "memory","scc","s20","s21");
    asm volatile("s_barrier\n s_setprio 3" ::: "memory");
  } else {
    const u32 hs = (4 * (b & 7) + ((b >> 3) & 3)) * 64 + 8 * (b >> 5) + 2 * (w & 3) + (w >> 2), vq = (tid & 31) * 4 + (u32)O_QT, vm = vl + (u32)O_QM;
    asm volatile(
"s_nop 4\n"
"global_load_dword v107, %[qt0], %[ws]\n"
"global_load_dword v108, %[qt1], %[ws]\n"
"global_load_dwordx4 v[80:83], %[qm0], %[ws]\n"
"global_load_dwordx4 v[84:87], %[qm1], %[ws]\n"
      : "={v[80:83]}"(q0), "={v[84:87]}"(q1), "={v107}"(t0), "={v108}"(t1)
      : [ws] "s"(pw), [qm0] "v"(vm + (hs ^ 1) * 1024), [qm1] "v"(vm + hs * 1024), [qt0] "v"(vq + (hs ^ 1) * 128), [qt1] "v"(vq + hs * 128)
      : "memory");
  }
  asm volatile("" : "+v"(tid)); asm volatile("" : "+s"(b), "+s"(w), "+s"(pw));
  char* ws = GP(char, pw);
  const int l = tid & 63, q = l & 31, g = l >> 5;
  const int h = 4 * (b & 7) + ((b >> 3) & 3), qt = b >> 5;
  if (w >= 8) {
    const u16* w1 = GP(const u16, TAB(2)); const u16* w2 = GP(const u16, TAB(3)); const u16* wo = GP(const u16, TAB(4));
    if (w >= 10) __builtin_amdgcn_raw_buffer_store_b32((w & l & l >> 1) & 1, __builtin_amdgcn_make_buffer_rsrc(ws + O_FLG, 0, 131072, 0x00020000), l * 4, (b + 256 * (w & 1)) * 256, WFPOL);
    if (w == 9) st16(u32x4{0, 0, 0, 0}, __builtin_amdgcn_make_buffer_rsrc(ws + (u32)(O_ST + b * 768), 0, 768, 0x00020000), l * 16, 0);
    ffn_tiles(w1, w2, ws, b & 7, (b >> 3) + 32 * (w & 3), l);
    { const int f = 1023 - b - 256 * (w & 3); if (f < 648) wo_tile(wo, ws, f, l); }
    ffn_touch(ws, b & 7, (b >> 3) + 32 * (w & 3), l);
    return;
  }
  const int kh = w >> 2, st0 = 8 * qt + 2 * (w & 3);
  const int st = st0 + kh;
  const char* ob0 = ws + (u32)(O_OFR + st * 36864);
  const u32x4 ro = {(u32)(uintptr_t)ob0, (u32)((uintptr_t)ob0 >> 32) & 0xffff, 36864, 0x00020000};
  const u32 vo = (q >> 4) * 18432 + (q & 15) * 16, vo1_ = vo + g * 256 + (h >> 1) * 1024 + (h & 1) * 512;
  const u32 vo2_ = vo + (16 + (h >> 4)) * 1024 + ((h >> 2) & 3) * 256 + (h & 3) * 4 + g * 36864;
  uint64_t bad;
  {
    const u32 vk = lb + kh * KVH + vl, vv = lb + kh * KVH + (g * 19 + (q < 18 ? q : 18)) * 16;
    const u32 ex = lb + 4096 + (w & 3) * 3072 + vl;
    asm volatile(
"v_mov_b32 v110, 0\n"
"v_mov_b32 v111, 0\n"
"v_mov_b32 v112, 0\n"
"v_mov_b32 v113, 0\n"
"v_mov_b32 v114, 0\n"
"v_mov_b32 v115, 0\n"
"v_mov_b32 v116, 0\n"
"v_mov_b32 v117, 0\n"
"v_mov_b32 v118, 0\n"
"v_mov_b32 v119, 0\n"
"v_mov_b32 v120, 0\n"
"v_mov_b32 v121, 0\n"
"v_mov_b32 v122, 0\n"
"v_mov_b32 v123, 0\n"
"v_mov_b32 v124, 0\n"
"v_mov_b32 v125, 0\n"
"v_mov_b32 v126, 0\n"
"v_mov_b32 v127, 0\n"
"v_mov_b32 v128, 0\n"
"v_mov_b32 v129, 0\n"
"v_mov_b32 v130, 0\n"
"v_mov_b32 v131, 0\n"
"v_mov_b32 v132, 0\n"
"v_mov_b32 v133, 0\n"
"v_mov_b32 v134, 0\n"
"v_mov_b32 v135, 0\n"
"v_mov_b32 v136, 0\n"
"v_mov_b32 v137, 0\n"
"v_mov_b32 v138, 0\n"
"v_mov_b32 v139, 0\n"
"v_mov_b32 v140, 0\n"
"v_mov_b32 v141, 0\n"
"v_mov_b32 v104, 0xdddddddd\n"
"v_add_u32 v105, 40960, %[vk]\n"
"v_add_u32 v106, 40960, %[vv]\n"
"v_cmp_gt_u32 vcc, 512, %[vl]\n"
"v_mov_b32 v89, 0\n"
"v_mov_b32 v90, 0\n"
"v_mov_b32 v93, 0\n"
"v_mov_b32 v94, 0\n"
"v_mov_b32 v97, 0\n"
"v_mov_b32 v98, 0\n"
"v_mov_b32 v101, 0\n"
"v_mov_b32 v102, 0\n"
"s_waitcnt vmcnt(3)\n"
"v_cndmask_b32 v88, 0, v107, vcc\n"
"v_cndmask_b32 v92, v107, 0, vcc\n"
"v_cndmask_b32 v91, 0, v107, vcc\n"
"v_cndmask_b32 v95, v107, 0, vcc\n"
"s_waitcnt vmcnt(2)\n"
"v_cndmask_b32 v96, 0, v108, vcc\n"
"v_cndmask_b32 v100, v108, 0, vcc\n"
"v_cndmask_b32 v99, 0, v108, vcc\n"
"v_cndmask_b32 v103, v108, 0, vcc\n"
"s_waitcnt vmcnt(0)\n"
"s_barrier\n"
"ds_read_b128 v[72:75], %[vk] offset:0\n"
"ds_read_b128 v[48:51], %[vk] offset:4096\n"
"ds_read_b128 v[56:59], %[vv] offset:5120\n"
"ds_read_b128 v[52:55], %[vk] offset:6336\n"
"ds_read_b128 v[64:67], %[vv] offset:5728\n"
"s_waitcnt lgkmcnt(4)\n"
"v_mfma_f32_32x32x8bf16_1k v[0:15], v[72:73], v[88:89], 0\n"
"s_waitcnt lgkmcnt(3)\n"
"v_mfma_f32_32x32x16_bf16 v[0:15], v[48:51], v[80:83], v[0:15]\n"
"s_nop 7\n"
"s_nop 3\n"
"v_mfma_f32_32x32x8bf16_1k v[16:31], v[72:73], v[96:97], 0\n"
"ds_read_b128 v[60:63], %[vv] offset:7360\n"
"v_exp_f32 v0, v0\n"
"v_exp_f32 v1, v1\n"
"v_exp_f32 v2, v2\n"
"v_exp_f32 v3, v3\n"
"v_mfma_f32_32x32x16_bf16 v[16:31], v[48:51], v[84:87], v[16:31]\n"
"v_exp_f32 v4, v4\n"
"v_exp_f32 v5, v5\n"
"v_exp_f32 v6, v6\n"
"v_exp_f32 v7, v7\n"
"v_exp_f32 v8, v8\n"
"v_exp_f32 v9, v9\n"
"v_exp_f32 v10, v10\n"
"v_exp_f32 v11, v11\n"
"s_waitcnt lgkmcnt(3)\n"
"v_smfmac_f32_32x32x32_bf16 v[110:125], v[56:59], v[0:7], v104\n"
"s_waitcnt lgkmcnt(1)\n"
"v_exp_f32 v12, v12\n"
"v_exp_f32 v13, v13\n"
"v_exp_f32 v14, v14\n"
"v_exp_f32 v15, v15\n"
"v_mfma_f32_32x32x8bf16_1k v[32:47], v[72:73], v[90:91], 0\n"
"ds_read_b128 v[48:51], %[vk] offset:8576\n"
"v_exp_f32 v16, v16\n"
"v_exp_f32 v17, v17\n"
"v_exp_f32 v18, v18\n"
"v_exp_f32 v19, v19\n"
"v_mfma_f32_32x32x16_bf16 v[32:47], v[52:55], v[80:83], v[32:47]\n"
"ds_read_b128 v[68:71], %[vv] offset:7968\n"
"v_exp_f32 v20, v20\n"
"v_exp_f32 v21, v21\n"
"v_exp_f32 v22, v22\n"
"v_exp_f32 v23, v23\n"
"v_smfmac_f32_32x32x32_bf16 v[110:125], v[64:67], v[8:15], v104\n"
"v_exp_f32 v24, v24\n"
"v_exp_f32 v25, v25\n"
"v_exp_f32 v26, v26\n"
"v_exp_f32 v27, v27\n"
"v_smfmac_f32_32x32x32_bf16 v[126:141], v[56:59], v[16:23], v104\n"
"s_waitcnt lgkmcnt(0)\n"
"v_exp_f32 v28, v28\n"
"v_exp_f32 v29, v29\n"
"v_exp_f32 v30, v30\n"
"v_exp_f32 v31, v31\n"
"v_mfma_f32_32x32x8bf16_1k v[0:15], v[72:73], v[98:99], 0\n"
"ds_read_b128 v[56:59], %[vv] offset:9600\n"
"v_exp_f32 v32, v32\n"
"v_exp_f32 v33, v33\n"
"v_exp_f32 v34, v34\n"
"v_exp_f32 v35, v35\n"
"v_mfma_f32_32x32x16_bf16 v[0:15], v[52:55], v[84:87], v[0:15]\n"
"v_exp_f32 v36, v36\n"
"v_exp_f32 v37, v37\n"
"v_exp_f32 v38, v38\n"
"v_exp_f32 v39, v39\n"
"v_smfmac_f32_32x32x32_bf16 v[126:141], v[64:67], v[24:31], v104\n"
"s_barrier\n"
"v_exp_f32 v40, v40\n"
"v_exp_f32 v41, v41\n"
"v_exp_f32 v42, v42\n"
"v_exp_f32 v43, v43\n"
"v_smfmac_f32_32x32x32_bf16 v[110:125], v[60:63], v[32:39], v104\n"
"v_exp_f32 v44, v44\n"
"v_exp_f32 v45, v45\n"
"v_exp_f32 v46, v46\n"
"v_exp_f32 v47, v47\n"
"v_mfma_f32_32x32x8bf16_1k v[16:31], v[72:73], v[92:93], 0\n"
"ds_read_b128 v[52:55], %[vk] offset:10816\n"
"v_exp_f32 v0, v0\n"
"v_exp_f32 v1, v1\n"
"v_exp_f32 v2, v2\n"
"v_exp_f32 v3, v3\n"
"v_mfma_f32_32x32x16_bf16 v[16:31], v[48:51], v[80:83], v[16:31]\n"
"ds_read_b128 v[64:67], %[vv] offset:10208\n"
"v_exp_f32 v4, v4\n"
"v_exp_f32 v5, v5\n"
"v_exp_f32 v6, v6\n"
"v_exp_f32 v7, v7\n"
"v_smfmac_f32_32x32x32_bf16 v[110:125], v[68:71], v[40:47], v104\n"
"v_exp_f32 v8, v8\n"
"v_exp_f32 v9, v9\n"
"v_exp_f32 v10, v10\n"
"v_exp_f32 v11, v11\n"
"v_smfmac_f32_32x32x32_bf16 v[126:141], v[60:63], v[0:7], v104\n"
"s_waitcnt lgkmcnt(2)\n"
"v_exp_f32 v12, v12\n"
"v_exp_f32 v13, v13\n"
"v_exp_f32 v14, v14\n"
"v_exp_f32 v15, v15\n"
"v_mfma_f32_32x32x8bf16_1k v[32:47], v[72:73], v[100:101], 0\n"
"ds_read_b128 v[60:63], %[vv] offset:11840\n"
"v_exp_f32 v16, v16\n"
"v_exp_f32 v17, v17\n"
"v_exp_f32 v18, v18\n"
"v_exp_f32 v19, v19\n"
"v_mfma_f32_32x32x16_bf16 v[32:47], v[48:51], v[84:87], v[32:47]\n"
"v_exp_f32 v20, v20\n"
"v_exp_f32 v21, v21\n"
"v_exp_f32 v22, v22\n"
"v_exp_f32 v23, v23\n"
"v_smfmac_f32_32x32x32_bf16 v[126:141], v[68:71], v[8:15], v104\n"
"v_exp_f32 v24, v24\n"
"v_exp_f32 v25, v25\n"
"v_exp_f32 v26, v26\n"
"v_exp_f32 v27, v27\n"
"v_smfmac_f32_32x32x32_bf16 v[110:125], v[56:59], v[16:23], v104\n"
"s_waitcnt lgkmcnt(1)\n"
"v_exp_f32 v28, v28\n"
"v_exp_f32 v29, v29\n"
"v_exp_f32 v30, v30\n"
"v_exp_f32 v31, v31\n"
"v_mfma_f32_32x32x8bf16_1k v[0:15], v[72:73], v[94:95], 0\n"
"ds_read_b128 v[48:51], %[vk] offset:13056\n"
"v_exp_f32 v32, v32\n"
"v_exp_f32 v33, v33\n"
"v_exp_f32 v34, v34\n"
"v_exp_f32 v35, v35\n"
"v_mfma_f32_32x32x16_bf16 v[0:15], v[52:55], v[80:83], v[0:15]\n"
"ds_read_b128 v[68:71], %[vv] offset:12448\n"
"v_exp_f32 v36, v36\n"
"v_exp_f32 v37, v37\n"
"v_exp_f32 v38, v38\n"
"v_exp_f32 v39, v39\n"
"v_smfmac_f32_32x32x32_bf16 v[110:125], v[64:67], v[24:31], v104\n"
"v_exp_f32 v40, v40\n"
"v_exp_f32 v41, v41\n"
"v_exp_f32 v42, v42\n"
"v_exp_f32 v43, v43\n"
"v_smfmac_f32_32x32x32_bf16 v[126:141], v[56:59], v[32:39], v104\n"
"s_waitcnt lgkmcnt(2)\n"
"v_exp_f32 v44, v44\n"
"v_exp_f32 v45, v45\n"
"v_exp_f32 v46, v46\n"
"v_exp_f32 v47, v47\n"
"v_mfma_f32_32x32x8bf16_1k v[16:31], v[72:73], v[102:103], 0\n"
"ds_read_b128 v[56:59], %[vv] offset:14080\n"
"v_exp_f32 v0, v0\n"
"v_exp_f32 v1, v1\n"
"v_exp_f32 v2, v2\n"
"v_exp_f32 v3, v3\n"
"v_mfma_f32_32x32x16_bf16 v[16:31], v[52:55], v[84:87], v[16:31]\n"
"v_exp_f32 v4, v4\n"
"v_exp_f32 v5, v5\n"
"v_exp_f32 v6, v6\n"
"v_exp_f32 v7, v7\n"
"v_smfmac_f32_32x32x32_bf16 v[126:141], v[64:67], v[40:47], v104\n"
"v_exp_f32 v8, v8\n"
"v_exp_f32 v9, v9\n"
"v_exp_f32 v10, v10\n"
"v_exp_f32 v11, v11\n"
"v_smfmac_f32_32x32x32_bf16 v[110:125], v[60:63], v[0:7], v104\n"
"s_waitcnt lgkmcnt(1)\n"
"v_exp_f32 v12, v12\n"
"v_exp_f32 v13, v13\n"
"v_exp_f32 v14, v14\n"
"v_exp_f32 v15, v15\n"
"v_mfma_f32_32x32x8bf16_1k v[32:47], v[74:75], v[88:89], 0\n"
"ds_read_b128 v[52:55], %[vk] offset:15296\n"
"v_exp_f32 v16, v16\n"
"v_exp_f32 v17, v17\n"
"v_exp_f32 v18, v18\n"
"v_exp_f32 v19, v19\n"
"v_mfma_f32_32x32x16_bf16 v[32:47], v[48:51], v[80:83], v[32:47]\n"
"ds_read_b128 v[64:67], %[vv] offset:14688\n"
"v_exp_f32 v20, v20\n"
"v_exp_f32 v21, v21\n"
"v_exp_f32 v22, v22\n"
"v_exp_f32 v23, v23\n"
"v_smfmac_f32_32x32x32_bf16 v[110:125], v[68:71], v[8:15], v104\n"
"v_exp_f32 v24, v24\n"
"v_exp_f32 v25, v25\n"
"v_exp_f32 v26, v26\n"
"v_exp_f32 v27, v27\n"
"v_smfmac_f32_32x32x32_bf16 v[126:141], v[60:63], v[16:23], v104\n"
"s_waitcnt lgkmcnt(2)\n"
"v_exp_f32 v28, v28\n"
"v_exp_f32 v29, v29\n"
"v_exp_f32 v30, v30\n"
"v_exp_f32 v31, v31\n"
"v_mfma_f32_32x32x8bf16_1k v[0:15], v[74:75], v[96:97], 0\n"
"ds_read_b128 v[60:63], %[vv] offset:16320\n"
"v_exp_f32 v32, v32\n"
"v_exp_f32 v33, v33\n"
"v_exp_f32 v34, v34\n"
"v_exp_f32 v35, v35\n"
"v_mfma_f32_32x32x16_bf16 v[0:15], v[48:51], v[84:87], v[0:15]\n"
"v_exp_f32 v36, v36\n"
"v_exp_f32 v37, v37\n"
"v_exp_f32 v38, v38\n"
"v_exp_f32 v39, v39\n"
"v_smfmac_f32_32x32x32_bf16 v[126:141], v[68:71], v[24:31], v104\n"
"v_exp_f32 v40, v40\n"
"v_exp_f32 v41, v41\n"
"v_exp_f32 v42, v42\n"
"v_exp_f32 v43, v43\n"
"v_smfmac_f32_32x32x32_bf16 v[110:125], v[56:59], v[32:39], v104\n"
"s_waitcnt lgkmcnt(1)\n"
"v_exp_f32 v44, v44\n"
"v_exp_f32 v45, v45\n"
"v_exp_f32 v46, v46\n"
"v_exp_f32 v47, v47\n"
"v_mfma_f32_32x32x8bf16_1k v[16:31], v[74:75], v[90:91], 0\n"
"ds_read_b128 v[48:51], %[vk] offset:17536\n"
"v_exp_f32 v0, v0\n"
"v_exp_f32 v1, v1\n"
"v_exp_f32 v2, v2\n"
"v_exp_f32 v3, v3\n"
"v_mfma_f32_32x32x16_bf16 v[16:31], v[52:55], v[80:83], v[16:31]\n"
"ds_read_b128 v[68:71], %[vv] offset:16928\n"
"v_exp_f32 v4, v4\n"
"v_exp_f32 v5, v5\n"
"v_exp_f32 v6, v6\n"
"v_exp_f32 v7, v7\n"
"v_smfmac_f32_32x32x32_bf16 v[110:125], v[64:67], v[40:47], v104\n"
"v_exp_f32 v8, v8\n"
"v_exp_f32 v9, v9\n"
"v_exp_f32 v10, v10\n"
"v_exp_f32 v11, v11\n"
"v_smfmac_f32_32x32x32_bf16 v[126:141], v[56:59], v[0:7], v104\n"
"s_waitcnt lgkmcnt(2)\n"
"v_exp_f32 v12, v12\n"
"v_exp_f32 v13, v13\n"
"v_exp_f32 v14, v14\n"
"v_exp_f32 v15, v15\n"
"v_mfma_f32_32x32x8bf16_1k v[32:47], v[74:75], v[98:99], 0\n"
"ds_read_b128 v[56:59], %[vv] offset:18560\n"
"v_exp_f32 v16, v16\n"
"v_exp_f32 v17, v17\n"
"v_exp_f32 v18, v18\n"
"v_exp_f32 v19, v19\n"
"v_mfma_f32_32x32x16_bf16 v[32:47], v[52:55], v[84:87], v[32:47]\n"
"ds_read_b128 v[76:79], %[vk] offset:1024\n"
"v_exp_f32 v20, v20\n"
"v_exp_f32 v21, v21\n"
"v_exp_f32 v22, v22\n"
"v_exp_f32 v23, v23\n"
"v_smfmac_f32_32x32x32_bf16 v[126:141], v[64:67], v[8:15], v104\n"
"v_exp_f32 v24, v24\n"
"v_exp_f32 v25, v25\n"
"v_exp_f32 v26, v26\n"
"v_exp_f32 v27, v27\n"
"v_smfmac_f32_32x32x32_bf16 v[110:125], v[60:63], v[16:23], v104\n"
"s_waitcnt lgkmcnt(2)\n"
"v_exp_f32 v28, v28\n"
"v_exp_f32 v29, v29\n"
"v_exp_f32 v30, v30\n"
"v_exp_f32 v31, v31\n"
"v_mfma_f32_32x32x8bf16_1k v[0:15], v[74:75], v[92:93], 0\n"
"ds_read_b128 v[52:55], %[vk] offset:19776\n"
"v_exp_f32 v32, v32\n"
"v_exp_f32 v33, v33\n"
"v_exp_f32 v34, v34\n"
"v_exp_f32 v35, v35\n"
"v_mfma_f32_32x32x16_bf16 v[0:15], v[48:51], v[80:83], v[0:15]\n"
"ds_read_b128 v[64:67], %[vv] offset:19168\n"
"v_exp_f32 v36, v36\n"
"v_exp_f32 v37, v37\n"
"v_exp_f32 v38, v38\n"
"v_exp_f32 v39, v39\n"
"v_smfmac_f32_32x32x32_bf16 v[110:125], v[68:71], v[24:31], v104\n"
"v_exp_f32 v40, v40\n"
"v_exp_f32 v41, v41\n"
"v_exp_f32 v42, v42\n"
"v_exp_f32 v43, v43\n"
"v_smfmac_f32_32x32x32_bf16 v[126:141], v[60:63], v[32:39], v104\n"
"s_waitcnt lgkmcnt(3)\n"
"v_exp_f32 v44, v44\n"
"v_exp_f32 v45, v45\n"
"v_exp_f32 v46, v46\n"
"v_exp_f32 v47, v47\n"
"v_mfma_f32_32x32x8bf16_1k v[16:31], v[74:75], v[100:101], 0\n"
"ds_read_b128 v[60:63], %[vv] offset:20800\n"
"v_exp_f32 v0, v0\n"
"v_exp_f32 v1, v1\n"
"v_exp_f32 v2, v2\n"
"v_exp_f32 v3, v3\n"
"v_mfma_f32_32x32x16_bf16 v[16:31], v[48:51], v[84:87], v[16:31]\n"
"v_exp_f32 v4, v4\n"
"v_exp_f32 v5, v5\n"
"v_exp_f32 v6, v6\n"
"v_exp_f32 v7, v7\n"
"v_smfmac_f32_32x32x32_bf16 v[126:141], v[68:71], v[40:47], v104\n"
"v_exp_f32 v8, v8\n"
"v_exp_f32 v9, v9\n"
"v_exp_f32 v10, v10\n"
"v_exp_f32 v11, v11\n"
"v_smfmac_f32_32x32x32_bf16 v[110:125], v[56:59], v[0:7], v104\n"
"s_waitcnt lgkmcnt(1)\n"
"v_exp_f32 v12, v12\n"
"v_exp_f32 v13, v13\n"
"v_exp_f32 v14, v14\n"
"v_exp_f32 v15, v15\n"
"v_mfma_f32_32x32x8bf16_1k v[32:47], v[74:75], v[94:95], 0\n"
"ds_read_b128 v[48:51], %[vk] offset:22016\n"
"v_exp_f32 v16, v16\n"
"v_exp_f32 v17, v17\n"
"v_exp_f32 v18, v18\n"
"v_exp_f32 v19, v19\n"
"v_mfma_f32_32x32x16_bf16 v[32:47], v[52:55], v[80:83], v[32:47]\n"
"ds_read_b128 v[68:71], %[vv] offset:21408\n"
"v_exp_f32 v20, v20\n"
"v_exp_f32 v21, v21\n"
"v_exp_f32 v22, v22\n"
"v_exp_f32 v23, v23\n"
"v_smfmac_f32_32x32x32_bf16 v[110:125], v[64:67], v[8:15], v104\n"
"v_exp_f32 v24, v24\n"
"v_exp_f32 v25, v25\n"
"v_exp_f32 v26, v26\n"
"v_exp_f32 v27, v27\n"
"v_smfmac_f32_32x32x32_bf16 v[126:141], v[56:59], v[16:23], v104\n"
"s_waitcnt lgkmcnt(0)\n"
"v_exp_f32 v28, v28\n"
"v_exp_f32 v29, v29\n"
"v_exp_f32 v30, v30\n"
"v_exp_f32 v31, v31\n"
"v_mfma_f32_32x32x8bf16_1k v[0:15], v[74:75], v[102:103], 0\n"
"ds_read_b128 v[56:59], %[vv] offset:23040\n"
"v_exp_f32 v32, v32\n"
"v_exp_f32 v33, v33\n"
"v_exp_f32 v34, v34\n"
"v_exp_f32 v35, v35\n"
"v_mfma_f32_32x32x16_bf16 v[0:15], v[52:55], v[84:87], v[0:15]\n"
"v_exp_f32 v36, v36\n"
"v_exp_f32 v37, v37\n"
"v_exp_f32 v38, v38\n"
"v_exp_f32 v39, v39\n"
"v_smfmac_f32_32x32x32_bf16 v[126:141], v[64:67], v[24:31], v104\n"
"s_barrier\n"
"v_exp_f32 v40, v40\n"
"v_exp_f32 v41, v41\n"
"v_exp_f32 v42, v42\n"
"v_exp_f32 v43, v43\n"
"v_smfmac_f32_32x32x32_bf16 v[110:125], v[60:63], v[32:39], v104\n"
"v_exp_f32 v44, v44\n"
"v_exp_f32 v45, v45\n"
"v_exp_f32 v46, v46\n"
"v_exp_f32 v47, v47\n"
"v_mfma_f32_32x32x8bf16_1k v[16:31], v[76:77], v[88:89], 0\n"
"ds_read_b128 v[52:55], %[vk] offset:24256\n"
"v_exp_f32 v0, v0\n"
"v_exp_f32 v1, v1\n"
"v_exp_f32 v2, v2\n"
"v_exp_f32 v3, v3\n"
"v_mfma_f32_32x32x16_bf16 v[16:31], v[48:51], v[80:83], v[16:31]\n"
"ds_read_b128 v[64:67], %[vv] offset:23648\n"
"v_exp_f32 v4, v4\n"
"v_exp_f32 v5, v5\n"
"v_exp_f32 v6, v6\n"
"v_exp_f32 v7, v7\n"
"v_smfmac_f32_32x32x32_bf16 v[110:125], v[68:71], v[40:47], v104\n"
"v_exp_f32 v8, v8\n"
"v_exp_f32 v9, v9\n"
"v_exp_f32 v10, v10\n"
"v_exp_f32 v11, v11\n"
"v_smfmac_f32_32x32x32_bf16 v[126:141], v[60:63], v[0:7], v104\n"
"s_waitcnt lgkmcnt(2)\n"
"v_exp_f32 v12, v12\n"
"v_exp_f32 v13, v13\n"
"v_exp_f32 v14, v14\n"
"v_exp_f32 v15, v15\n"
"v_mfma_f32_32x32x8bf16_1k v[32:47], v[76:77], v[96:97], 0\n"
"ds_read_b128 v[60:63], %[vv] offset:25280\n"
"v_exp_f32 v16, v16\n"
"v_exp_f32 v17, v17\n"
"v_exp_f32 v18, v18\n"
"v_exp_f32 v19, v19\n"
"v_mfma_f32_32x32x16_bf16 v[32:47], v[48:51], v[84:87], v[32:47]\n"
"v_exp_f32 v20, v20\n"
"v_exp_f32 v21, v21\n"
"v_exp_f32 v22, v22\n"
"v_exp_f32 v23, v23\n"
"v_smfmac_f32_32x32x32_bf16 v[126:141], v[68:71], v[8:15], v104\n"
"v_exp_f32 v24, v24\n"
"v_exp_f32 v25, v25\n"
"v_exp_f32 v26, v26\n"
"v_exp_f32 v27, v27\n"
"v_smfmac_f32_32x32x32_bf16 v[110:125], v[56:59], v[16:23], v104\n"
"s_waitcnt lgkmcnt(1)\n"
"v_exp_f32 v28, v28\n"
"v_exp_f32 v29, v29\n"
"v_exp_f32 v30, v30\n"
"v_exp_f32 v31, v31\n"
"v_mfma_f32_32x32x8bf16_1k v[0:15], v[76:77], v[90:91], 0\n"
"ds_read_b128 v[48:51], %[vk] offset:26496\n"
"v_exp_f32 v32, v32\n"
"v_exp_f32 v33, v33\n"
"v_exp_f32 v34, v34\n"
"v_exp_f32 v35, v35\n"
"v_mfma_f32_32x32x16_bf16 v[0:15], v[52:55], v[80:83], v[0:15]\n"
"ds_read_b128 v[68:71], %[vv] offset:25888\n"
"v_exp_f32 v36, v36\n"
"v_exp_f32 v37, v37\n"
"v_exp_f32 v38, v38\n"
"v_exp_f32 v39, v39\n"
"v_smfmac_f32_32x32x32_bf16 v[110:125], v[64:67], v[24:31], v104\n"
"v_exp_f32 v40, v40\n"
"v_exp_f32 v41, v41\n"
"v_exp_f32 v42, v42\n"
"v_exp_f32 v43, v43\n"
"v_smfmac_f32_32x32x32_bf16 v[126:141], v[56:59], v[32:39], v104\n"
"s_waitcnt lgkmcnt(2)\n"
"v_exp_f32 v44, v44\n"
"v_exp_f32 v45, v45\n"
"v_exp_f32 v46, v46\n"
"v_exp_f32 v47, v47\n"
"v_mfma_f32_32x32x8bf16_1k v[16:31], v[76:77], v[98:99], 0\n"
"ds_read_b128 v[56:59], %[vv] offset:27520\n"
"v_exp_f32 v0, v0\n"
"v_exp_f32 v1, v1\n"
"v_exp_f32 v2, v2\n"
"v_exp_f32 v3, v3\n"
"v_mfma_f32_32x32x16_bf16 v[16:31], v[52:55], v[84:87], v[16:31]\n"
"v_exp_f32 v4, v4\n"
"v_exp_f32 v5, v5\n"
"v_exp_f32 v6, v6\n"
"v_exp_f32 v7, v7\n"
"v_smfmac_f32_32x32x32_bf16 v[126:141], v[64:67], v[40:47], v104\n"
"v_exp_f32 v8, v8\n"
"v_exp_f32 v9, v9\n"
"v_exp_f32 v10, v10\n"
"v_exp_f32 v11, v11\n"
"v_smfmac_f32_32x32x32_bf16 v[110:125], v[60:63], v[0:7], v104\n"
"s_waitcnt lgkmcnt(1)\n"
"v_exp_f32 v12, v12\n"
"v_exp_f32 v13, v13\n"
"v_exp_f32 v14, v14\n"
"v_exp_f32 v15, v15\n"
"v_mfma_f32_32x32x8bf16_1k v[32:47], v[76:77], v[92:93], 0\n"
"ds_read_b128 v[52:55], %[vk] offset:28736\n"
"v_exp_f32 v16, v16\n"
"v_exp_f32 v17, v17\n"
"v_exp_f32 v18, v18\n"
"v_exp_f32 v19, v19\n"
"v_mfma_f32_32x32x16_bf16 v[32:47], v[48:51], v[80:83], v[32:47]\n"
"ds_read_b128 v[64:67], %[vv] offset:28128\n"
"v_exp_f32 v20, v20\n"
"v_exp_f32 v21, v21\n"
"v_exp_f32 v22, v22\n"
"v_exp_f32 v23, v23\n"
"v_smfmac_f32_32x32x32_bf16 v[110:125], v[68:71], v[8:15], v104\n"
"v_exp_f32 v24, v24\n"
"v_exp_f32 v25, v25\n"
"v_exp_f32 v26, v26\n"
"v_exp_f32 v27, v27\n"
"v_smfmac_f32_32x32x32_bf16 v[126:141], v[60:63], v[16:23], v104\n"
"s_waitcnt lgkmcnt(2)\n"
"v_exp_f32 v28, v28\n"
"v_exp_f32 v29, v29\n"
"v_exp_f32 v30, v30\n"
"v_exp_f32 v31, v31\n"
"v_mfma_f32_32x32x8bf16_1k v[0:15], v[76:77], v[100:101], 0\n"
"ds_read_b128 v[60:63], %[vv] offset:29760\n"
"v_exp_f32 v32, v32\n"
"v_exp_f32 v33, v33\n"
"v_exp_f32 v34, v34\n"
"v_exp_f32 v35, v35\n"
"v_mfma_f32_32x32x16_bf16 v[0:15], v[48:51], v[84:87], v[0:15]\n"
"v_exp_f32 v36, v36\n"
"v_exp_f32 v37, v37\n"
"v_exp_f32 v38, v38\n"
"v_exp_f32 v39, v39\n"
"v_smfmac_f32_32x32x32_bf16 v[126:141], v[68:71], v[24:31], v104\n"
"v_exp_f32 v40, v40\n"
"v_exp_f32 v41, v41\n"
"v_exp_f32 v42, v42\n"
"v_exp_f32 v43, v43\n"
"v_smfmac_f32_32x32x32_bf16 v[110:125], v[56:59], v[32:39], v104\n"
"s_waitcnt lgkmcnt(1)\n"
"v_exp_f32 v44, v44\n"
"v_exp_f32 v45, v45\n"
"v_exp_f32 v46, v46\n"
"v_exp_f32 v47, v47\n"
"v_mfma_f32_32x32x8bf16_1k v[16:31], v[76:77], v[94:95], 0\n"
"ds_read_b128 v[48:51], %[vk] offset:30976\n"
"v_exp_f32 v0, v0\n"
"v_exp_f32 v1, v1\n"
"v_exp_f32 v2, v2\n"
"v_exp_f32 v3, v3\n"
"v_mfma_f32_32x32x16_bf16 v[16:31], v[52:55], v[80:83], v[16:31]\n"
"ds_read_b128 v[68:71], %[vv] offset:30368\n"
"v_exp_f32 v4, v4\n"
"v_exp_f32 v5, v5\n"
"v_exp_f32 v6, v6\n"
"v_exp_f32 v7, v7\n"
"v_smfmac_f32_32x32x32_bf16 v[110:125], v[64:67], v[40:47], v104\n"
"v_exp_f32 v8, v8\n"
"v_exp_f32 v9, v9\n"
"v_exp_f32 v10, v10\n"
"v_exp_f32 v11, v11\n"
"v_smfmac_f32_32x32x32_bf16 v[126:141], v[56:59], v[0:7], v104\n"
"s_waitcnt lgkmcnt(2)\n"
"v_exp_f32 v12, v12\n"
"v_exp_f32 v13, v13\n"
"v_exp_f32 v14, v14\n"
"v_exp_f32 v15, v15\n"
"v_mfma_f32_32x32x8bf16_1k v[32:47], v[76:77], v[102:103], 0\n"
"ds_read_b128 v[56:59], %[vv] offset:32000\n"
"v_exp_f32 v16, v16\n"
"v_exp_f32 v17, v17\n"
"v_exp_f32 v18, v18\n"
"v_exp_f32 v19, v19\n"
"v_mfma_f32_32x32x16_bf16 v[32:47], v[52:55], v[84:87], v[32:47]\n"
"v_exp_f32 v20, v20\n"
"v_exp_f32 v21, v21\n"
"v_exp_f32 v22, v22\n"
"v_exp_f32 v23, v23\n"
"v_smfmac_f32_32x32x32_bf16 v[126:141], v[64:67], v[8:15], v104\n"
"v_exp_f32 v24, v24\n"
"v_exp_f32 v25, v25\n"
"v_exp_f32 v26, v26\n"
"v_exp_f32 v27, v27\n"
"v_smfmac_f32_32x32x32_bf16 v[110:125], v[60:63], v[16:23], v104\n"
"s_waitcnt lgkmcnt(1)\n"
"v_exp_f32 v28, v28\n"
"v_exp_f32 v29, v29\n"
"v_exp_f32 v30, v30\n"
"v_exp_f32 v31, v31\n"
"v_mfma_f32_32x32x8bf16_1k v[0:15], v[78:79], v[88:89], 0\n"
"ds_read_b128 v[52:55], %[vk] offset:33216\n"
"v_exp_f32 v32, v32\n"
"v_exp_f32 v33, v33\n"
"v_exp_f32 v34, v34\n"
"v_exp_f32 v35, v35\n"
"v_mfma_f32_32x32x16_bf16 v[0:15], v[48:51], v[80:83], v[0:15]\n"
"ds_read_b128 v[64:67], %[vv] offset:32608\n"
"v_exp_f32 v36, v36\n"
"v_exp_f32 v37, v37\n"
"v_exp_f32 v38, v38\n"
"v_exp_f32 v39, v39\n"
"v_smfmac_f32_32x32x32_bf16 v[110:125], v[68:71], v[24:31], v104\n"
"v_exp_f32 v40, v40\n"
"v_exp_f32 v41, v41\n"
"v_exp_f32 v42, v42\n"
"v_exp_f32 v43, v43\n"
"v_smfmac_f32_32x32x32_bf16 v[126:141], v[60:63], v[32:39], v104\n"
"s_waitcnt lgkmcnt(2)\n"
"v_exp_f32 v44, v44\n"
"v_exp_f32 v45, v45\n"
"v_exp_f32 v46, v46\n"
"v_exp_f32 v47, v47\n"
"v_mfma_f32_32x32x8bf16_1k v[16:31], v[78:79], v[96:97], 0\n"
"ds_read_b128 v[60:63], %[vv] offset:34240\n"
"v_exp_f32 v0, v0\n"
"v_exp_f32 v1, v1\n"
"v_exp_f32 v2, v2\n"
"v_exp_f32 v3, v3\n"
"v_mfma_f32_32x32x16_bf16 v[16:31], v[48:51], v[84:87], v[16:31]\n"
"v_exp_f32 v4, v4\n"
"v_exp_f32 v5, v5\n"
"v_exp_f32 v6, v6\n"
"v_exp_f32 v7, v7\n"
"v_smfmac_f32_32x32x32_bf16 v[126:141], v[68:71], v[40:47], v104\n"
"v_exp_f32 v8, v8\n"
"v_exp_f32 v9, v9\n"
"v_exp_f32 v10, v10\n"
"v_exp_f32 v11, v11\n"
"v_smfmac_f32_32x32x32_bf16 v[110:125], v[56:59], v[0:7], v104\n"
"s_waitcnt lgkmcnt(1)\n"
"v_exp_f32 v12, v12\n"
"v_exp_f32 v13, v13\n"
"v_exp_f32 v14, v14\n"
"v_exp_f32 v15, v15\n"
"v_mfma_f32_32x32x8bf16_1k v[32:47], v[78:79], v[90:91], 0\n"
"ds_read_b128 v[48:51], %[vk] offset:35456\n"
"v_exp_f32 v16, v16\n"
"v_exp_f32 v17, v17\n"
"v_exp_f32 v18, v18\n"
"v_exp_f32 v19, v19\n"
"v_mfma_f32_32x32x16_bf16 v[32:47], v[52:55], v[80:83], v[32:47]\n"
"ds_read_b128 v[68:71], %[vv] offset:34848\n"
"v_exp_f32 v20, v20\n"
"v_exp_f32 v21, v21\n"
"v_exp_f32 v22, v22\n"
"v_exp_f32 v23, v23\n"
"v_smfmac_f32_32x32x32_bf16 v[110:125], v[64:67], v[8:15], v104\n"
"v_exp_f32 v24, v24\n"
"v_exp_f32 v25, v25\n"
"v_exp_f32 v26, v26\n"
"v_exp_f32 v27, v27\n"
"v_smfmac_f32_32x32x32_bf16 v[126:141], v[56:59], v[16:23], v104\n"
"s_waitcnt lgkmcnt(2)\n"
"v_exp_f32 v28, v28\n"
"v_exp_f32 v29, v29\n"
"v_exp_f32 v30, v30\n"
"v_exp_f32 v31, v31\n"
"v_mfma_f32_32x32x8bf16_1k v[0:15], v[78:79], v[98:99], 0\n"
"ds_read_b128 v[56:59], %[vv] offset:36480\n"
"v_exp_f32 v32, v32\n"
"v_exp_f32 v33, v33\n"
"v_exp_f32 v34, v34\n"
"v_exp_f32 v35, v35\n"
"v_mfma_f32_32x32x16_bf16 v[0:15], v[52:55], v[84:87], v[0:15]\n"
"ds_read_b128 v[72:75], %[vk] offset:2048\n"
"v_exp_f32 v36, v36\n"
"v_exp_f32 v37, v37\n"
"v_exp_f32 v38, v38\n"
"v_exp_f32 v39, v39\n"
"v_smfmac_f32_32x32x32_bf16 v[126:141], v[64:67], v[24:31], v104\n"
"v_exp_f32 v40, v40\n"
"v_exp_f32 v41, v41\n"
"v_exp_f32 v42, v42\n"
"v_exp_f32 v43, v43\n"
"v_smfmac_f32_32x32x32_bf16 v[110:125], v[60:63], v[32:39], v104\n"
"s_waitcnt lgkmcnt(2)\n"
"v_exp_f32 v44, v44\n"
"v_exp_f32 v45, v45\n"
"v_exp_f32 v46, v46\n"
"v_exp_f32 v47, v47\n"
"v_mfma_f32_32x32x8bf16_1k v[16:31], v[78:79], v[92:93], 0\n"
"ds_read_b128 v[52:55], %[vk] offset:37696\n"
"v_exp_f32 v0, v0\n"
"v_exp_f32 v1, v1\n"
"v_exp_f32 v2, v2\n"
"v_exp_f32 v3, v3\n"
"v_mfma_f32_32x32x16_bf16 v[16:31], v[48:51], v[80:83], v[16:31]\n"
"ds_read_b128 v[64:67], %[vv] offset:37088\n"
"v_exp_f32 v4, v4\n"
"v_exp_f32 v5, v5\n"
"v_exp_f32 v6, v6\n"
"v_exp_f32 v7, v7\n"
"v_smfmac_f32_32x32x32_bf16 v[110:125], v[68:71], v[40:47], v104\n"
"v_exp_f32 v8, v8\n"
"v_exp_f32 v9, v9\n"
"v_exp_f32 v10, v10\n"
"v_exp_f32 v11, v11\n"
"v_smfmac_f32_32x32x32_bf16 v[126:141], v[60:63], v[0:7], v104\n"
"s_waitcnt lgkmcnt(3)\n"
"v_exp_f32 v12, v12\n"
"v_exp_f32 v13, v13\n"
"v_exp_f32 v14, v14\n"
"v_exp_f32 v15, v15\n"
"v_mfma_f32_32x32x8bf16_1k v[32:47], v[78:79], v[100:101], 0\n"
"ds_read_b128 v[60:63], %[vv] offset:38720\n"
"v_exp_f32 v16, v16\n"
"v_exp_f32 v17, v17\n"
"v_exp_f32 v18, v18\n"
"v_exp_f32 v19, v19\n"
"v_mfma_f32_32x32x16_bf16 v[32:47], v[48:51], v[84:87], v[32:47]\n"
"v_exp_f32 v20, v20\n"
"v_exp_f32 v21, v21\n"
"v_exp_f32 v22, v22\n"
"v_exp_f32 v23, v23\n"
"v_smfmac_f32_32x32x32_bf16 v[126:141], v[68:71], v[8:15], v104\n"
"v_exp_f32 v24, v24\n"
"v_exp_f32 v25, v25\n"
"v_exp_f32 v26, v26\n"
"v_exp_f32 v27, v27\n"
"v_smfmac_f32_32x32x32_bf16 v[110:125], v[56:59], v[16:23], v104\n"
"s_waitcnt lgkmcnt(1)\n"
"v_exp_f32 v28, v28\n"
"v_exp_f32 v29, v29\n"
"v_exp_f32 v30, v30\n"
"v_exp_f32 v31, v31\n"
"v_mfma_f32_32x32x8bf16_1k v[0:15], v[78:79], v[94:95], 0\n"
"ds_read_b128 v[48:51], %[vk] offset:39936\n"
"v_exp_f32 v32, v32\n"
"v_exp_f32 v33, v33\n"
"v_exp_f32 v34, v34\n"
"v_exp_f32 v35, v35\n"
"v_mfma_f32_32x32x16_bf16 v[0:15], v[52:55], v[80:83], v[0:15]\n"
"ds_read_b128 v[68:71], %[vv] offset:39328\n"
"v_exp_f32 v36, v36\n"
"v_exp_f32 v37, v37\n"
"v_exp_f32 v38, v38\n"
"v_exp_f32 v39, v39\n"
"v_smfmac_f32_32x32x32_bf16 v[110:125], v[64:67], v[24:31], v104\n"
"v_exp_f32 v40, v40\n"
"v_exp_f32 v41, v41\n"
"v_exp_f32 v42, v42\n"
"v_exp_f32 v43, v43\n"
"v_smfmac_f32_32x32x32_bf16 v[126:141], v[56:59], v[32:39], v104\n"
"s_waitcnt lgkmcnt(2)\n"
"v_exp_f32 v44, v44\n"
"v_exp_f32 v45, v45\n"
"v_exp_f32 v46, v46\n"
"v_exp_f32 v47, v47\n"
"v_mfma_f32_32x32x8bf16_1k v[16:31], v[78:79], v[102:103], 0\n"
"ds_read_b128 v[56:59], %[vv] offset:40960\n"
"v_exp_f32 v0, v0\n"
"v_exp_f32 v1, v1\n"
"v_exp_f32 v2, v2\n"
"v_exp_f32 v3, v3\n"
"v_mfma_f32_32x32x16_bf16 v[16:31], v[52:55], v[84:87], v[16:31]\n"
"v_exp_f32 v4, v4\n"
"v_exp_f32 v5, v5\n"
"v_exp_f32 v6, v6\n"
"v_exp_f32 v7, v7\n"
"v_smfmac_f32_32x32x32_bf16 v[126:141], v[64:67], v[40:47], v104\n"
"v_exp_f32 v8, v8\n"
"v_exp_f32 v9, v9\n"
"v_exp_f32 v10, v10\n"
"v_exp_f32 v11, v11\n"
"v_smfmac_f32_32x32x32_bf16 v[110:125], v[60:63], v[0:7], v104\n"
"s_waitcnt lgkmcnt(1)\n"
"v_exp_f32 v12, v12\n"
"v_exp_f32 v13, v13\n"
"v_exp_f32 v14, v14\n"
"v_exp_f32 v15, v15\n"
"v_mfma_f32_32x32x8bf16_1k v[32:47], v[72:73], v[88:89], 0\n"
"ds_read_b128 v[52:55], %[vk] offset:42176\n"
"v_exp_f32 v16, v16\n"
"v_exp_f32 v17, v17\n"
"v_exp_f32 v18, v18\n"
"v_exp_f32 v19, v19\n"
"v_mfma_f32_32x32x16_bf16 v[32:47], v[48:51], v[80:83], v[32:47]\n"
"ds_read_b128 v[64:67], %[vv] offset:41568\n"
"v_exp_f32 v20, v20\n"
"v_exp_f32 v21, v21\n"
"v_exp_f32 v22, v22\n"
"v_exp_f32 v23, v23\n"
"v_smfmac_f32_32x32x32_bf16 v[110:125], v[68:71], v[8:15], v104\n"
"v_exp_f32 v24, v24\n"
"v_exp_f32 v25, v25\n"
"v_exp_f32 v26, v26\n"
"v_exp_f32 v27, v27\n"
"v_smfmac_f32_32x32x32_bf16 v[126:141], v[60:63], v[16:23], v104\n"
"s_waitcnt lgkmcnt(2)\n"
"v_exp_f32 v28, v28\n"
"v_exp_f32 v29, v29\n"
"v_exp_f32 v30, v30\n"
"v_exp_f32 v31, v31\n"
"v_mfma_f32_32x32x8bf16_1k v[0:15], v[72:73], v[96:97], 0\n"
"ds_read_b128 v[60:63], %[vv] offset:43200\n"
"v_exp_f32 v32, v32\n"
"v_exp_f32 v33, v33\n"
"v_exp_f32 v34, v34\n"
"v_exp_f32 v35, v35\n"
"v_mfma_f32_32x32x16_bf16 v[0:15], v[48:51], v[84:87], v[0:15]\n"
"v_exp_f32 v36, v36\n"
"v_exp_f32 v37, v37\n"
"v_exp_f32 v38, v38\n"
"v_exp_f32 v39, v39\n"
"v_smfmac_f32_32x32x32_bf16 v[126:141], v[68:71], v[24:31], v104\n"
"v_exp_f32 v40, v40\n"
"v_exp_f32 v41, v41\n"
"v_exp_f32 v42, v42\n"
"v_exp_f32 v43, v43\n"
"v_smfmac_f32_32x32x32_bf16 v[110:125], v[56:59], v[32:39], v104\n"
"s_waitcnt lgkmcnt(1)\n"
"v_exp_f32 v44, v44\n"
"v_exp_f32 v45, v45\n"
"v_exp_f32 v46, v46\n"
"v_exp_f32 v47, v47\n"
"v_mfma_f32_32x32x8bf16_1k v[16:31], v[72:73], v[90:91], 0\n"
"ds_read_b128 v[48:51], %[vk] offset:44416\n"
"v_exp_f32 v0, v0\n"
"v_exp_f32 v1, v1\n"
"v_exp_f32 v2, v2\n"
"v_exp_f32 v3, v3\n"
"v_mfma_f32_32x32x16_bf16 v[16:31], v[52:55], v[80:83], v[16:31]\n"
"ds_read_b128 v[68:71], %[vv] offset:43808\n"
"v_exp_f32 v4, v4\n"
"v_exp_f32 v5, v5\n"
"v_exp_f32 v6, v6\n"
"v_exp_f32 v7, v7\n"
"v_smfmac_f32_32x32x32_bf16 v[110:125], v[64:67], v[40:47], v104\n"
"v_exp_f32 v8, v8\n"
"v_exp_f32 v9, v9\n"
"v_exp_f32 v10, v10\n"
"v_exp_f32 v11, v11\n"
"v_smfmac_f32_32x32x32_bf16 v[126:141], v[56:59], v[0:7], v104\n"
"s_waitcnt lgkmcnt(2)\n"
"v_exp_f32 v12, v12\n"
"v_exp_f32 v13, v13\n"
"v_exp_f32 v14, v14\n"
"v_exp_f32 v15, v15\n"
"v_mfma_f32_32x32x8bf16_1k v[32:47], v[72:73], v[98:99], 0\n"
"ds_read_b128 v[56:59], %[vv] offset:45440\n"
"v_exp_f32 v16, v16\n"
"v_exp_f32 v17, v17\n"
"v_exp_f32 v18, v18\n"
"v_exp_f32 v19, v19\n"
"v_mfma_f32_32x32x16_bf16 v[32:47], v[52:55], v[84:87], v[32:47]\n"
"v_exp_f32 v20, v20\n"
"v_exp_f32 v21, v21\n"
"v_exp_f32 v22, v22\n"
"v_exp_f32 v23, v23\n"
"v_smfmac_f32_32x32x32_bf16 v[126:141], v[64:67], v[8:15], v104\n"
"v_exp_f32 v24, v24\n"
"v_exp_f32 v25, v25\n"
"v_exp_f32 v26, v26\n"
"v_exp_f32 v27, v27\n"
"v_smfmac_f32_32x32x32_bf16 v[110:125], v[60:63], v[16:23], v104\n"
"s_waitcnt lgkmcnt(1)\n"
"v_exp_f32 v28, v28\n"
"v_exp_f32 v29, v29\n"
"v_exp_f32 v30, v30\n"
"v_exp_f32 v31, v31\n"
"v_mfma_f32_32x32x8bf16_1k v[0:15], v[72:73], v[92:93], 0\n"
"ds_read_b128 v[52:55], %[vk] offset:46656\n"
"v_exp_f32 v32, v32\n"
"v_exp_f32 v33, v33\n"
"v_exp_f32 v34, v34\n"
"v_exp_f32 v35, v35\n"
"v_mfma_f32_32x32x16_bf16 v[0:15], v[48:51], v[80:83], v[0:15]\n"
"ds_read_b128 v[64:67], %[vv] offset:46048\n"
"v_exp_f32 v36, v36\n"
"v_exp_f32 v37, v37\n"
"v_exp_f32 v38, v38\n"
"v_exp_f32 v39, v39\n"
"v_smfmac_f32_32x32x32_bf16 v[110:125], v[68:71], v[24:31], v104\n"
"v_exp_f32 v40, v40\n"
"v_exp_f32 v41, v41\n"
"v_exp_f32 v42, v42\n"
"v_exp_f32 v43, v43\n"
"v_smfmac_f32_32x32x32_bf16 v[126:141], v[60:63], v[32:39], v104\n"
"s_waitcnt lgkmcnt(2)\n"
"v_exp_f32 v44, v44\n"
"v_exp_f32 v45, v45\n"
"v_exp_f32 v46, v46\n"
"v_exp_f32 v47, v47\n"
"v_mfma_f32_32x32x8bf16_1k v[16:31], v[72:73], v[100:101], 0\n"
"ds_read_b128 v[60:63], %[vv] offset:47680\n"
"v_exp_f32 v0, v0\n"
"v_exp_f32 v1, v1\n"
"v_exp_f32 v2, v2\n"
"v_exp_f32 v3, v3\n"
"v_mfma_f32_32x32x16_bf16 v[16:31], v[48:51], v[84:87], v[16:31]\n"
"v_exp_f32 v4, v4\n"
"v_exp_f32 v5, v5\n"
"v_exp_f32 v6, v6\n"
"v_exp_f32 v7, v7\n"
"v_smfmac_f32_32x32x32_bf16 v[126:141], v[68:71], v[40:47], v104\n"
"v_exp_f32 v8, v8\n"
"v_exp_f32 v9, v9\n"
"v_exp_f32 v10, v10\n"
"v_exp_f32 v11, v11\n"
"v_smfmac_f32_32x32x32_bf16 v[110:125], v[56:59], v[0:7], v104\n"
"s_waitcnt lgkmcnt(1)\n"
"v_exp_f32 v12, v12\n"
"v_exp_f32 v13, v13\n"
"v_exp_f32 v14, v14\n"
"v_exp_f32 v15, v15\n"
"v_mfma_f32_32x32x8bf16_1k v[32:47], v[72:73], v[94:95], 0\n"
"ds_read_b128 v[48:51], %[vk] offset:48896\n"
"v_exp_f32 v16, v16\n"
"v_exp_f32 v17, v17\n"
"v_exp_f32 v18, v18\n"
"v_exp_f32 v19, v19\n"
"v_mfma_f32_32x32x16_bf16 v[32:47], v[52:55], v[80:83], v[32:47]\n"
"ds_read_b128 v[68:71], %[vv] offset:48288\n"
"v_exp_f32 v20, v20\n"
"v_exp_f32 v21, v21\n"
"v_exp_f32 v22, v22\n"
"v_exp_f32 v23, v23\n"
"v_smfmac_f32_32x32x32_bf16 v[110:125], v[64:67], v[8:15], v104\n"
"v_exp_f32 v24, v24\n"
"v_exp_f32 v25, v25\n"
"v_exp_f32 v26, v26\n"
"v_exp_f32 v27, v27\n"
"v_smfmac_f32_32x32x32_bf16 v[126:141], v[56:59], v[16:23], v104\n"
"s_waitcnt lgkmcnt(2)\n"
"v_exp_f32 v28, v28\n"
"v_exp_f32 v29, v29\n"
"v_exp_f32 v30, v30\n"
"v_exp_f32 v31, v31\n"
"v_mfma_f32_32x32x8bf16_1k v[0:15], v[72:73], v[102:103], 0\n"
"ds_read_b128 v[56:59], %[vv] offset:49920\n"
"v_exp_f32 v32, v32\n"
"v_exp_f32 v33, v33\n"
"v_exp_f32 v34, v34\n"
"v_exp_f32 v35, v35\n"
"v_mfma_f32_32x32x16_bf16 v[0:15], v[52:55], v[84:87], v[0:15]\n"
"v_exp_f32 v36, v36\n"
"v_exp_f32 v37, v37\n"
"v_exp_f32 v38, v38\n"
"v_exp_f32 v39, v39\n"
"v_smfmac_f32_32x32x32_bf16 v[126:141], v[64:67], v[24:31], v104\n"
"v_exp_f32 v40, v40\n"
"v_exp_f32 v41, v41\n"
"v_exp_f32 v42, v42\n"
"v_exp_f32 v43, v43\n"
"v_smfmac_f32_32x32x32_bf16 v[110:125], v[60:63], v[32:39], v104\n"
"s_waitcnt lgkmcnt(1)\n"
"v_exp_f32 v44, v44\n"
"v_exp_f32 v45, v45\n"
"v_exp_f32 v46, v46\n"
"v_exp_f32 v47, v47\n"
"v_mfma_f32_32x32x8bf16_1k v[16:31], v[74:75], v[88:89], 0\n"
"ds_read_b128 v[52:55], %[vk] offset:51136\n"
"v_exp_f32 v0, v0\n"
"v_exp_f32 v1, v1\n"
"v_exp_f32 v2, v2\n"
"v_exp_f32 v3, v3\n"
"v_mfma_f32_32x32x16_bf16 v[16:31], v[48:51], v[80:83], v[16:31]\n"
"ds_read_b128 v[64:67], %[vv] offset:50528\n"
"v_exp_f32 v4, v4\n"
"v_exp_f32 v5, v5\n"
"v_exp_f32 v6, v6\n"
"v_exp_f32 v7, v7\n"
"v_smfmac_f32_32x32x32_bf16 v[110:125], v[68:71], v[40:47], v104\n"
"v_exp_f32 v8, v8\n"
"v_exp_f32 v9, v9\n"
"v_exp_f32 v10, v10\n"
"v_exp_f32 v11, v11\n"
"v_smfmac_f32_32x32x32_bf16 v[126:141], v[60:63], v[0:7], v104\n"
"s_waitcnt lgkmcnt(2)\n"
"v_exp_f32 v12, v12\n"
"v_exp_f32 v13, v13\n"
"v_exp_f32 v14, v14\n"
"v_exp_f32 v15, v15\n"
"v_mfma_f32_32x32x8bf16_1k v[32:47], v[74:75], v[96:97], 0\n"
"ds_read_b128 v[60:63], %[vv] offset:52160\n"
"v_exp_f32 v16, v16\n"
"v_exp_f32 v17, v17\n"
"v_exp_f32 v18, v18\n"
"v_exp_f32 v19, v19\n"
"v_mfma_f32_32x32x16_bf16 v[32:47], v[48:51], v[84:87], v[32:47]\n"
"v_exp_f32 v20, v20\n"
"v_exp_f32 v21, v21\n"
"v_exp_f32 v22, v22\n"
"v_exp_f32 v23, v23\n"
"v_smfmac_f32_32x32x32_bf16 v[126:141], v[68:71], v[8:15], v104\n"
"v_exp_f32 v24, v24\n"
"v_exp_f32 v25, v25\n"
"v_exp_f32 v26, v26\n"
"v_exp_f32 v27, v27\n"
"v_smfmac_f32_32x32x32_bf16 v[110:125], v[56:59], v[16:23], v104\n"
"s_waitcnt lgkmcnt(1)\n"
"v_exp_f32 v28, v28\n"
"v_exp_f32 v29, v29\n"
"v_exp_f32 v30, v30\n"
"v_exp_f32 v31, v31\n"
"v_mfma_f32_32x32x8bf16_1k v[0:15], v[74:75], v[90:91], 0\n"
"ds_read_b128 v[48:51], %[vk] offset:53376\n"
"v_exp_f32 v32, v32\n"
"v_exp_f32 v33, v33\n"
"v_exp_f32 v34, v34\n"
"v_exp_f32 v35, v35\n"
"v_mfma_f32_32x32x16_bf16 v[0:15], v[52:55], v[80:83], v[0:15]\n"
"ds_read_b128 v[68:71], %[vv] offset:52768\n"
"v_exp_f32 v36, v36\n"
"v_exp_f32 v37, v37\n"
"v_exp_f32 v38, v38\n"
"v_exp_f32 v39, v39\n"
"v_smfmac_f32_32x32x32_bf16 v[110:125], v[64:67], v[24:31], v104\n"
"v_exp_f32 v40, v40\n"
"v_exp_f32 v41, v41\n"
"v_exp_f32 v42, v42\n"
"v_exp_f32 v43, v43\n"
"v_smfmac_f32_32x32x32_bf16 v[126:141], v[56:59], v[32:39], v104\n"
"s_waitcnt lgkmcnt(2)\n"
"v_exp_f32 v44, v44\n"
"v_exp_f32 v45, v45\n"
"v_exp_f32 v46, v46\n"
"v_exp_f32 v47, v47\n"
"v_mfma_f32_32x32x8bf16_1k v[16:31], v[74:75], v[98:99], 0\n"
"ds_read_b128 v[56:59], %[vv] offset:54400\n"
"v_exp_f32 v0, v0\n"
"v_exp_f32 v1, v1\n"
"v_exp_f32 v2, v2\n"
"v_exp_f32 v3, v3\n"
"v_mfma_f32_32x32x16_bf16 v[16:31], v[52:55], v[84:87], v[16:31]\n"
"ds_read_b128 v[76:79], %[vk] offset:3072\n"
"v_exp_f32 v4, v4\n"
"v_exp_f32 v5, v5\n"
"v_exp_f32 v6, v6\n"
"v_exp_f32 v7, v7\n"
"v_smfmac_f32_32x32x32_bf16 v[126:141], v[64:67], v[40:47], v104\n"
"v_exp_f32 v8, v8\n"
"v_exp_f32 v9, v9\n"
"v_exp_f32 v10, v10\n"
"v_exp_f32 v11, v11\n"
"v_smfmac_f32_32x32x32_bf16 v[110:125], v[60:63], v[0:7], v104\n"
"s_waitcnt lgkmcnt(2)\n"
"v_exp_f32 v12, v12\n"
"v_exp_f32 v13, v13\n"
"v_exp_f32 v14, v14\n"
"v_exp_f32 v15, v15\n"
"v_mfma_f32_32x32x8bf16_1k v[32:47], v[74:75], v[92:93], 0\n"
"ds_read_b128 v[52:55], %[vk] offset:55616\n"
"v_exp_f32 v16, v16\n"
"v_exp_f32 v17, v17\n"
"v_exp_f32 v18, v18\n"
"v_exp_f32 v19, v19\n"
"v_mfma_f32_32x32x16_bf16 v[32:47], v[48:51], v[80:83], v[32:47]\n"
"ds_read_b128 v[64:67], %[vv] offset:55008\n"
"v_exp_f32 v20, v20\n"
"v_exp_f32 v21, v21\n"
"v_exp_f32 v22, v22\n"
"v_exp_f32 v23, v23\n"
"v_smfmac_f32_32x32x32_bf16 v[110:125], v[68:71], v[8:15], v104\n"
"v_exp_f32 v24, v24\n"
"v_exp_f32 v25, v25\n"
"v_exp_f32 v26, v26\n"
"v_exp_f32 v27, v27\n"
"v_smfmac_f32_32x32x32_bf16 v[126:141], v[60:63], v[16:23], v104\n"
"s_waitcnt lgkmcnt(3)\n"
"v_exp_f32 v28, v28\n"
"v_exp_f32 v29, v29\n"
"v_exp_f32 v30, v30\n"
"v_exp_f32 v31, v31\n"
"v_mfma_f32_32x32x8bf16_1k v[0:15], v[74:75], v[100:101], 0\n"
"ds_read_b128 v[60:63], %[vv] offset:56640\n"
"v_exp_f32 v32, v32\n"
"v_exp_f32 v33, v33\n"
"v_exp_f32 v34, v34\n"
"v_exp_f32 v35, v35\n"
"v_mfma_f32_32x32x16_bf16 v[0:15], v[48:51], v[84:87], v[0:15]\n"
"v_exp_f32 v36, v36\n"
"v_exp_f32 v37, v37\n"
"v_exp_f32 v38, v38\n"
"v_exp_f32 v39, v39\n"
"v_smfmac_f32_32x32x32_bf16 v[126:141], v[68:71], v[24:31], v104\n"
"v_exp_f32 v40, v40\n"
"v_exp_f32 v41, v41\n"
"v_exp_f32 v42, v42\n"
"v_exp_f32 v43, v43\n"
"v_smfmac_f32_32x32x32_bf16 v[110:125], v[56:59], v[32:39], v104\n"
"s_waitcnt lgkmcnt(1)\n"
"v_exp_f32 v44, v44\n"
"v_exp_f32 v45, v45\n"
"v_exp_f32 v46, v46\n"
"v_exp_f32 v47, v47\n"
"v_mfma_f32_32x32x8bf16_1k v[16:31], v[74:75], v[94:95], 0\n"
"ds_read_b128 v[48:51], %[vk] offset:57856\n"
"v_exp_f32 v0, v0\n"
"v_exp_f32 v1, v1\n"
"v_exp_f32 v2, v2\n"
"v_exp_f32 v3, v3\n"
"v_mfma_f32_32x32x16_bf16 v[16:31], v[52:55], v[80:83], v[16:31]\n"
"ds_read_b128 v[68:71], %[vv] offset:57248\n"
"v_exp_f32 v4, v4\n"
"v_exp_f32 v5, v5\n"
"v_exp_f32 v6, v6\n"
"v_exp_f32 v7, v7\n"
"v_smfmac_f32_32x32x32_bf16 v[110:125], v[64:67], v[40:47], v104\n"
"v_exp_f32 v8, v8\n"
"v_exp_f32 v9, v9\n"
"v_exp_f32 v10, v10\n"
"v_exp_f32 v11, v11\n"
"v_smfmac_f32_32x32x32_bf16 v[126:141], v[56:59], v[0:7], v104\n"
"s_waitcnt lgkmcnt(2)\n"
"v_exp_f32 v12, v12\n"
"v_exp_f32 v13, v13\n"
"v_exp_f32 v14, v14\n"
"v_exp_f32 v15, v15\n"
"v_mfma_f32_32x32x8bf16_1k v[32:47], v[74:75], v[102:103], 0\n"
"ds_read_b128 v[56:59], %[vv] offset:58880\n"
"v_exp_f32 v16, v16\n"
"v_exp_f32 v17, v17\n"
"v_exp_f32 v18, v18\n"
"v_exp_f32 v19, v19\n"
"v_mfma_f32_32x32x16_bf16 v[32:47], v[52:55], v[84:87], v[32:47]\n"
"v_exp_f32 v20, v20\n"
"v_exp_f32 v21, v21\n"
"v_exp_f32 v22, v22\n"
"v_exp_f32 v23, v23\n"
"v_smfmac_f32_32x32x32_bf16 v[126:141], v[64:67], v[8:15], v104\n"
"v_exp_f32 v24, v24\n"
"v_exp_f32 v25, v25\n"
"v_exp_f32 v26, v26\n"
"v_exp_f32 v27, v27\n"
"v_smfmac_f32_32x32x32_bf16 v[110:125], v[60:63], v[16:23], v104\n"
"s_waitcnt lgkmcnt(1)\n"
"v_exp_f32 v28, v28\n"
"v_exp_f32 v29, v29\n"
"v_exp_f32 v30, v30\n"
"v_exp_f32 v31, v31\n"
"v_mfma_f32_32x32x8bf16_1k v[0:15], v[76:77], v[88:89], 0\n"
"ds_read_b128 v[52:55], %[vk] offset:60096\n"
"v_exp_f32 v32, v32\n"
"v_exp_f32 v33, v33\n"
"v_exp_f32 v34, v34\n"
"v_exp_f32 v35, v35\n"
"v_mfma_f32_32x32x16_bf16 v[0:15], v[48:51], v[80:83], v[0:15]\n"
"ds_read_b128 v[64:67], %[vv] offset:59488\n"
"v_exp_f32 v36, v36\n"
"v_exp_f32 v37, v37\n"
"v_exp_f32 v38, v38\n"
"v_exp_f32 v39, v39\n"
"v_smfmac_f32_32x32x32_bf16 v[110:125], v[68:71], v[24:31], v104\n"
"v_exp_f32 v40, v40\n"
"v_exp_f32 v41, v41\n"
"v_exp_f32 v42, v42\n"
"v_exp_f32 v43, v43\n"
"v_smfmac_f32_32x32x32_bf16 v[126:141], v[60:63], v[32:39], v104\n"
"s_waitcnt lgkmcnt(2)\n"
"v_exp_f32 v44, v44\n"
"v_exp_f32 v45, v45\n"
"v_exp_f32 v46, v46\n"
"v_exp_f32 v47, v47\n"
"v_mfma_f32_32x32x8bf16_1k v[16:31], v[76:77], v[96:97], 0\n"
"ds_read_b128 v[60:63], %[vv] offset:61120\n"
"v_exp_f32 v0, v0\n"
"v_exp_f32 v1, v1\n"
"v_exp_f32 v2, v2\n"
"v_exp_f32 v3, v3\n"
"v_mfma_f32_32x32x16_bf16 v[16:31], v[48:51], v[84:87], v[16:31]\n"
"v_exp_f32 v4, v4\n"
"v_exp_f32 v5, v5\n"
"v_exp_f32 v6, v6\n"
"v_exp_f32 v7, v7\n"
"v_smfmac_f32_32x32x32_bf16 v[126:141], v[68:71], v[40:47], v104\n"
"v_exp_f32 v8, v8\n"
"v_exp_f32 v9, v9\n"
"v_exp_f32 v10, v10\n"
"v_exp_f32 v11, v11\n"
"v_smfmac_f32_32x32x32_bf16 v[110:125], v[56:59], v[0:7], v104\n"
"s_waitcnt lgkmcnt(1)\n"
"v_exp_f32 v12, v12\n"
"v_exp_f32 v13, v13\n"
"v_exp_f32 v14, v14\n"
"v_exp_f32 v15, v15\n"
"v_mfma_f32_32x32x8bf16_1k v[32:47], v[76:77], v[90:91], 0\n"
"ds_read_b128 v[48:51], %[vk] offset:62336\n"
"v_exp_f32 v16, v16\n"
"v_exp_f32 v17, v17\n"
"v_exp_f32 v18, v18\n"
"v_exp_f32 v19, v19\n"
"v_mfma_f32_32x32x16_bf16 v[32:47], v[52:55], v[80:83], v[32:47]\n"
"ds_read_b128 v[68:71], %[vv] offset:61728\n"
"v_exp_f32 v20, v20\n"
"v_exp_f32 v21, v21\n"
"v_exp_f32 v22, v22\n"
"v_exp_f32 v23, v23\n"
"v_smfmac_f32_32x32x32_bf16 v[110:125], v[64:67], v[8:15], v104\n"
"v_exp_f32 v24, v24\n"
"v_exp_f32 v25, v25\n"
"v_exp_f32 v26, v26\n"
"v_exp_f32 v27, v27\n"
"v_smfmac_f32_32x32x32_bf16 v[126:141], v[56:59], v[16:23], v104\n"
"s_waitcnt lgkmcnt(2)\n"
"v_exp_f32 v28, v28\n"
"v_exp_f32 v29, v29\n"
"v_exp_f32 v30, v30\n"
"v_exp_f32 v31, v31\n"
"v_mfma_f32_32x32x8bf16_1k v[0:15], v[76:77], v[98:99], 0\n"
"ds_read_b128 v[56:59], %[vv] offset:63360\n"
"v_exp_f32 v32, v32\n"
"v_exp_f32 v33, v33\n"
"v_exp_f32 v34, v34\n"
"v_exp_f32 v35, v35\n"
"v_mfma_f32_32x32x16_bf16 v[0:15], v[52:55], v[84:87], v[0:15]\n"
"v_exp_f32 v36, v36\n"
"v_exp_f32 v37, v37\n"
"v_exp_f32 v38, v38\n"
"v_exp_f32 v39, v39\n"
"v_smfmac_f32_32x32x32_bf16 v[126:141], v[64:67], v[24:31], v104\n"
"v_exp_f32 v40, v40\n"
"v_exp_f32 v41, v41\n"
"v_exp_f32 v42, v42\n"
"v_exp_f32 v43, v43\n"
"v_smfmac_f32_32x32x32_bf16 v[110:125], v[60:63], v[32:39], v104\n"
"s_waitcnt lgkmcnt(1)\n"
"v_exp_f32 v44, v44\n"
"v_exp_f32 v45, v45\n"
"v_exp_f32 v46, v46\n"
"v_exp_f32 v47, v47\n"
"v_mfma_f32_32x32x8bf16_1k v[16:31], v[76:77], v[92:93], 0\n"
"ds_read_b128 v[52:55], v105 offset:23616\n"
"v_exp_f32 v0, v0\n"
"v_exp_f32 v1, v1\n"
"v_exp_f32 v2, v2\n"
"v_exp_f32 v3, v3\n"
"v_mfma_f32_32x32x16_bf16 v[16:31], v[48:51], v[80:83], v[16:31]\n"
"ds_read_b128 v[64:67], %[vv] offset:63968\n"
"v_exp_f32 v4, v4\n"
"v_exp_f32 v5, v5\n"
"v_exp_f32 v6, v6\n"
"v_exp_f32 v7, v7\n"
"v_smfmac_f32_32x32x32_bf16 v[110:125], v[68:71], v[40:47], v104\n"
"v_exp_f32 v8, v8\n"
"v_exp_f32 v9, v9\n"
"v_exp_f32 v10, v10\n"
"v_exp_f32 v11, v11\n"
"v_smfmac_f32_32x32x32_bf16 v[126:141], v[60:63], v[0:7], v104\n"
"s_waitcnt lgkmcnt(2)\n"
"v_exp_f32 v12, v12\n"
"v_exp_f32 v13, v13\n"
"v_exp_f32 v14, v14\n"
"v_exp_f32 v15, v15\n"
"v_mfma_f32_32x32x8bf16_1k v[32:47], v[76:77], v[100:101], 0\n"
"ds_read_b128 v[60:63], v106 offset:24640\n"
"v_exp_f32 v16, v16\n"
"v_exp_f32 v17, v17\n"
"v_exp_f32 v18, v18\n"
"v_exp_f32 v19, v19\n"
"v_mfma_f32_32x32x16_bf16 v[32:47], v[48:51], v[84:87], v[32:47]\n"
"v_exp_f32 v20, v20\n"
"v_exp_f32 v21, v21\n"
"v_exp_f32 v22, v22\n"
"v_exp_f32 v23, v23\n"
"v_smfmac_f32_32x32x32_bf16 v[126:141], v[68:71], v[8:15], v104\n"
"v_exp_f32 v24, v24\n"
"v_exp_f32 v25, v25\n"
"v_exp_f32 v26, v26\n"
"v_exp_f32 v27, v27\n"
"v_smfmac_f32_32x32x32_bf16 v[110:125], v[56:59], v[16:23], v104\n"
"s_waitcnt lgkmcnt(1)\n"
"v_exp_f32 v28, v28\n"
"v_exp_f32 v29, v29\n"
"v_exp_f32 v30, v30\n"
"v_exp_f32 v31, v31\n"
"v_mfma_f32_32x32x8bf16_1k v[0:15], v[76:77], v[94:95], 0\n"
"ds_read_b128 v[48:51], v105 offset:25856\n"
"v_exp_f32 v32, v32\n"
"v_exp_f32 v33, v33\n"
"v_exp_f32 v34, v34\n"
"v_exp_f32 v35, v35\n"
"v_mfma_f32_32x32x16_bf16 v[0:15], v[52:55], v[80:83], v[0:15]\n"
"ds_read_b128 v[68:71], v106 offset:25248\n"
"v_exp_f32 v36, v36\n"
"v_exp_f32 v37, v37\n"
"v_exp_f32 v38, v38\n"
"v_exp_f32 v39, v39\n"
"v_smfmac_f32_32x32x32_bf16 v[110:125], v[64:67], v[24:31], v104\n"
"v_exp_f32 v40, v40\n"
"v_exp_f32 v41, v41\n"
"v_exp_f32 v42, v42\n"
"v_exp_f32 v43, v43\n"
"v_smfmac_f32_32x32x32_bf16 v[126:141], v[56:59], v[32:39], v104\n"
"s_waitcnt lgkmcnt(2)\n"
"v_exp_f32 v44, v44\n"
"v_exp_f32 v45, v45\n"
"v_exp_f32 v46, v46\n"
"v_exp_f32 v47, v47\n"
"v_mfma_f32_32x32x8bf16_1k v[16:31], v[76:77], v[102:103], 0\n"
"ds_read_b128 v[56:59], v106 offset:26880\n"
"v_exp_f32 v0, v0\n"
"v_exp_f32 v1, v1\n"
"v_exp_f32 v2, v2\n"
"v_exp_f32 v3, v3\n"
"v_mfma_f32_32x32x16_bf16 v[16:31], v[52:55], v[84:87], v[16:31]\n"
"v_exp_f32 v4, v4\n"
"v_exp_f32 v5, v5\n"
"v_exp_f32 v6, v6\n"
"v_exp_f32 v7, v7\n"
"v_smfmac_f32_32x32x32_bf16 v[126:141], v[64:67], v[40:47], v104\n"
"v_exp_f32 v8, v8\n"
"v_exp_f32 v9, v9\n"
"v_exp_f32 v10, v10\n"
"v_exp_f32 v11, v11\n"
"v_smfmac_f32_32x32x32_bf16 v[110:125], v[60:63], v[0:7], v104\n"
"s_waitcnt lgkmcnt(1)\n"
"v_exp_f32 v12, v12\n"
"v_exp_f32 v13, v13\n"
"v_exp_f32 v14, v14\n"
"v_exp_f32 v15, v15\n"
"v_mfma_f32_32x32x8bf16_1k v[32:47], v[78:79], v[88:89], 0\n"
"ds_read_b128 v[52:55], v105 offset:28096\n"
"v_exp_f32 v16, v16\n"
"v_exp_f32 v17, v17\n"
"v_exp_f32 v18, v18\n"
"v_exp_f32 v19, v19\n"
"v_mfma_f32_32x32x16_bf16 v[32:47], v[48:51], v[80:83], v[32:47]\n"
"ds_read_b128 v[64:67], v106 offset:27488\n"
"v_exp_f32 v20, v20\n"
"v_exp_f32 v21, v21\n"
"v_exp_f32 v22, v22\n"
"v_exp_f32 v23, v23\n"
"v_smfmac_f32_32x32x32_bf16 v[110:125], v[68:71], v[8:15], v104\n"
"v_exp_f32 v24, v24\n"
"v_exp_f32 v25, v25\n"
"v_exp_f32 v26, v26\n"
"v_exp_f32 v27, v27\n"
"v_smfmac_f32_32x32x32_bf16 v[126:141], v[60:63], v[16:23], v104\n"
"s_waitcnt lgkmcnt(2)\n"
"v_exp_f32 v28, v28\n"
"v_exp_f32 v29, v29\n"
"v_exp_f32 v30, v30\n"
"v_exp_f32 v31, v31\n"
"v_mfma_f32_32x32x8bf16_1k v[0:15], v[78:79], v[96:97], 0\n"
"ds_read_b128 v[60:63], v106 offset:29120\n"
"v_exp_f32 v32, v32\n"
"v_exp_f32 v33, v33\n"
"v_exp_f32 v34, v34\n"
"v_exp_f32 v35, v35\n"
"v_mfma_f32_32x32x16_bf16 v[0:15], v[48:51], v[84:87], v[0:15]\n"
"v_exp_f32 v36, v36\n"
"v_exp_f32 v37, v37\n"
"v_exp_f32 v38, v38\n"
"v_exp_f32 v39, v39\n"
"v_smfmac_f32_32x32x32_bf16 v[126:141], v[68:71], v[24:31], v104\n"
"v_exp_f32 v40, v40\n"
"v_exp_f32 v41, v41\n"
"v_exp_f32 v42, v42\n"
"v_exp_f32 v43, v43\n"
"v_smfmac_f32_32x32x32_bf16 v[110:125], v[56:59], v[32:39], v104\n"
"s_waitcnt lgkmcnt(1)\n"
"v_exp_f32 v44, v44\n"
"v_exp_f32 v45, v45\n"
"v_exp_f32 v46, v46\n"
"v_exp_f32 v47, v47\n"
"v_mfma_f32_32x32x8bf16_1k v[16:31], v[78:79], v[90:91], 0\n"
"ds_read_b128 v[48:51], v105 offset:30336\n"
"v_exp_f32 v0, v0\n"
"v_exp_f32 v1, v1\n"
"v_exp_f32 v2, v2\n"
"v_exp_f32 v3, v3\n"
"v_mfma_f32_32x32x16_bf16 v[16:31], v[52:55], v[80:83], v[16:31]\n"
"ds_read_b128 v[68:71], v106 offset:29728\n"
"v_exp_f32 v4, v4\n"
"v_exp_f32 v5, v5\n"
"v_exp_f32 v6, v6\n"
"v_exp_f32 v7, v7\n"
"v_smfmac_f32_32x32x32_bf16 v[110:125], v[64:67], v[40:47], v104\n"
"v_exp_f32 v8, v8\n"
"v_exp_f32 v9, v9\n"
"v_exp_f32 v10, v10\n"
"v_exp_f32 v11, v11\n"
"v_smfmac_f32_32x32x32_bf16 v[126:141], v[56:59], v[0:7], v104\n"
"s_waitcnt lgkmcnt(2)\n"
"v_exp_f32 v12, v12\n"
"v_exp_f32 v13, v13\n"
"v_exp_f32 v14, v14\n"
"v_exp_f32 v15, v15\n"
"v_mfma_f32_32x32x8bf16_1k v[32:47], v[78:79], v[98:99], 0\n"
"ds_read_b128 v[56:59], v106 offset:31360\n"
"v_exp_f32 v16, v16\n"
"v_exp_f32 v17, v17\n"
"v_exp_f32 v18, v18\n"
"v_exp_f32 v19, v19\n"
"v_mfma_f32_32x32x16_bf16 v[32:47], v[52:55], v[84:87], v[32:47]\n"
"v_exp_f32 v20, v20\n"
"v_exp_f32 v21, v21\n"
"v_exp_f32 v22, v22\n"
"v_exp_f32 v23, v23\n"
"v_smfmac_f32_32x32x32_bf16 v[126:141], v[64:67], v[8:15], v104\n"
"v_exp_f32 v24, v24\n"
"v_exp_f32 v25, v25\n"
"v_exp_f32 v26, v26\n"
"v_exp_f32 v27, v27\n"
"v_smfmac_f32_32x32x32_bf16 v[110:125], v[60:63], v[16:23], v104\n"
"s_waitcnt lgkmcnt(1)\n"
"v_exp_f32 v28, v28\n"
"v_exp_f32 v29, v29\n"
"v_exp_f32 v30, v30\n"
"v_exp_f32 v31, v31\n"
"v_mfma_f32_32x32x8bf16_1k v[0:15], v[78:79], v[92:93], 0\n"
"ds_read_b128 v[52:55], v105 offset:32576\n"
"v_exp_f32 v32, v32\n"
"v_exp_f32 v33, v33\n"
"v_exp_f32 v34, v34\n"
"v_exp_f32 v35, v35\n"
"v_mfma_f32_32x32x16_bf16 v[0:15], v[48:51], v[80:83], v[0:15]\n"
"ds_read_b128 v[64:67], v106 offset:31968\n"
"v_exp_f32 v36, v36\n"
"v_exp_f32 v37, v37\n"
"v_exp_f32 v38, v38\n"
"v_exp_f32 v39, v39\n"
"v_smfmac_f32_32x32x32_bf16 v[110:125], v[68:71], v[24:31], v104\n"
"v_exp_f32 v40, v40\n"
"v_exp_f32 v41, v41\n"
"v_exp_f32 v42, v42\n"
"v_exp_f32 v43, v43\n"
"v_smfmac_f32_32x32x32_bf16 v[126:141], v[60:63], v[32:39], v104\n"
"s_waitcnt lgkmcnt(2)\n"
"v_exp_f32 v44, v44\n"
"v_exp_f32 v45, v45\n"
"v_exp_f32 v46, v46\n"
"v_exp_f32 v47, v47\n"
"v_mfma_f32_32x32x8bf16_1k v[16:31], v[78:79], v[100:101], 0\n"
"ds_read_b128 v[60:63], v106 offset:33600\n"
"v_exp_f32 v0, v0\n"
"v_exp_f32 v1, v1\n"
"v_exp_f32 v2, v2\n"
"v_exp_f32 v3, v3\n"
"v_mfma_f32_32x32x16_bf16 v[16:31], v[48:51], v[84:87], v[16:31]\n"
"v_exp_f32 v4, v4\n"
"v_exp_f32 v5, v5\n"
"v_exp_f32 v6, v6\n"
"v_exp_f32 v7, v7\n"
"v_smfmac_f32_32x32x32_bf16 v[126:141], v[68:71], v[40:47], v104\n"
"v_exp_f32 v8, v8\n"
"v_exp_f32 v9, v9\n"
"v_exp_f32 v10, v10\n"
"v_exp_f32 v11, v11\n"
"v_smfmac_f32_32x32x32_bf16 v[110:125], v[56:59], v[0:7], v104\n"
"s_waitcnt lgkmcnt(1)\n"
"v_exp_f32 v12, v12\n"
"v_exp_f32 v13, v13\n"
"v_exp_f32 v14, v14\n"
"v_exp_f32 v15, v15\n"
"v_mfma_f32_32x32x8bf16_1k v[32:47], v[78:79], v[94:95], 0\n"
"v_exp_f32 v16, v16\n"
"v_exp_f32 v17, v17\n"
"v_exp_f32 v18, v18\n"
"v_exp_f32 v19, v19\n"
"v_mfma_f32_32x32x16_bf16 v[32:47], v[52:55], v[80:83], v[32:47]\n"
"ds_read_b128 v[68:71], v106 offset:34208\n"
"v_exp_f32 v20, v20\n"
"v_exp_f32 v21, v21\n"
"v_exp_f32 v22, v22\n"
"v_exp_f32 v23, v23\n"
"v_smfmac_f32_32x32x32_bf16 v[110:125], v[64:67], v[8:15], v104\n"
"v_exp_f32 v24, v24\n"
"v_exp_f32 v25, v25\n"
"v_exp_f32 v26, v26\n"
"v_exp_f32 v27, v27\n"
"v_smfmac_f32_32x32x32_bf16 v[126:141], v[56:59], v[16:23], v104\n"
"s_waitcnt lgkmcnt(1)\n"
"v_exp_f32 v28, v28\n"
"v_exp_f32 v29, v29\n"
"v_exp_f32 v30, v30\n"
"v_exp_f32 v31, v31\n"
"v_mfma_f32_32x32x8bf16_1k v[0:15], v[78:79], v[102:103], 0\n"
"v_exp_f32 v32, v32\n"
"v_exp_f32 v33, v33\n"
"v_exp_f32 v34, v34\n"
"v_exp_f32 v35, v35\n"
"v_mfma_f32_32x32x16_bf16 v[0:15], v[52:55], v[84:87], v[0:15]\n"
"v_exp_f32 v36, v36\n"
"v_exp_f32 v37, v37\n"
"v_exp_f32 v38, v38\n"
"v_exp_f32 v39, v39\n"
"v_smfmac_f32_32x32x32_bf16 v[126:141], v[64:67], v[24:31], v104\n"
"v_exp_f32 v40, v40\n"
"v_exp_f32 v41, v41\n"
"v_exp_f32 v42, v42\n"
"v_exp_f32 v43, v43\n"
"v_smfmac_f32_32x32x32_bf16 v[110:125], v[60:63], v[32:39], v104\n"
"s_waitcnt lgkmcnt(0)\n"
"v_exp_f32 v44, v44\n"
"v_exp_f32 v45, v45\n"
"v_exp_f32 v46, v46\n"
"v_exp_f32 v47, v47\n"
"v_exp_f32 v0, v0\n"
"v_exp_f32 v1, v1\n"
"v_exp_f32 v2, v2\n"
"v_exp_f32 v3, v3\n"
"v_exp_f32 v4, v4\n"
"v_exp_f32 v5, v5\n"
"v_exp_f32 v6, v6\n"
"v_exp_f32 v7, v7\n"
"v_smfmac_f32_32x32x32_bf16 v[110:125], v[68:71], v[40:47], v104\n"
"v_exp_f32 v8, v8\n"
"v_exp_f32 v9, v9\n"
"v_exp_f32 v10, v10\n"
"v_exp_f32 v11, v11\n"
"v_smfmac_f32_32x32x32_bf16 v[126:141], v[60:63], v[0:7], v104\n"
"v_exp_f32 v12, v12\n"
"v_exp_f32 v13, v13\n"
"v_exp_f32 v14, v14\n"
"v_exp_f32 v15, v15\n"
"s_nop 1\n"
"v_smfmac_f32_32x32x32_bf16 v[126:141], v[68:71], v[8:15], v104\n"
"s_nop 1\n"
"ds_write_b128 %[vex], v[110:113] offset:0\n"
"ds_write_b128 %[vex], v[114:117] offset:1024\n"
"ds_write_b128 %[vex], v[118:121] offset:2048\n"
"s_waitcnt lgkmcnt(0)\n"
"s_barrier\n"
"ds_read_b128 v[0:3], %[ver] offset:0\n"
"ds_read_b128 v[4:7], %[ver] offset:1024\n"
"ds_read_b128 v[8:11], %[ver] offset:2048\n"
"s_nop 7\n"
"s_waitcnt lgkmcnt(0)\n"
"v_pk_add_f32 v[8:9], v[8:9], v[134:135]\n"
"v_pk_add_f32 v[10:11], v[10:11], v[136:137]\n"
"v_pk_add_f32 v[0:1], v[0:1], v[126:127]\n"
"v_pk_add_f32 v[2:3], v[2:3], v[128:129]\n"
"v_pk_add_f32 v[4:5], v[4:5], v[130:131]\n"
"v_pk_add_f32 v[6:7], v[6:7], v[132:133]\n"
"v_cndmask_b32 v12, v8, v10, vcc\n"
"v_pk_add_f32 v[14:15], v[0:1], v[2:3]\n"
"v_pk_add_f32 v[16:17], v[4:5], v[6:7]\n"
"v_rcp_f32 v13, v12\n"
"v_subrev_u32 v12, 0x1f800000, v12\n"
"v_pk_add_f32 v[14:15], v[14:15], v[8:9]\n"
"v_cmp_lt_u32 vcc, 0x52000000, v12\n"
"v_pk_add_f32 v[14:15], v[14:15], v[16:17]\n"
"v_pk_mul_f32 v[0:1], v[0:1], v[12:13] op_sel:[0,1] op_sel_hi:[1,1]\n"
"v_pk_mul_f32 v[2:3], v[2:3], v[12:13] op_sel:[0,1] op_sel_hi:[1,1]\n"
"v_pk_mul_f32 v[4:5], v[4:5], v[12:13] op_sel:[0,1] op_sel_hi:[1,1]\n"
"v_pk_mul_f32 v[6:7], v[6:7], v[12:13] op_sel:[0,1] op_sel_hi:[1,1]\n"
"v_pk_mul_f32 v[8:9], v[8:9], v[12:13] op_sel:[0,1] op_sel_hi:[1,1]\n"
"v_add_f32 v14, v14, v15\n"
"v_mov_b32 v16, 0x207\n"
"v_cvt_pk_bf16_f32 v0, v0, v1\n"
"v_cvt_pk_bf16_f32 v1, v2, v3\n"
"v_cvt_pk_bf16_f32 v2, v4, v5\n"
"v_cvt_pk_bf16_f32 v3, v6, v7\n"
"v_cvt_pk_bf16_f32 v4, v8, v9\n"
"v_cmp_class_f32 %[bad], v14, v16\n"
"s_nop 0\n"
"s_or_b64 %[bad], %[bad], vcc\n"
"s_cbranch_scc1 .Lsl_%=\n"
"buffer_store_dwordx4 v[0:3], %[vo1], %[ro], 0 offen\n"
"buffer_store_dword v4, %[vo2], %[ro], 0 offen\n"
"s_endpgm\n"
".Lsl_%=:\n"
"s_nop 1\n"
      : [bad] "=&s"(bad), "+{v[80:83]}"(q0), "+{v[84:87]}"(q1), "+{v107}"(t0), "+{v108}"(t1)
      : [vl] "v"(vl), [vk] "v"(vk), [vv] "v"(vv), [kh] "s"(kh), [vex] "v"(ex + kh * KVH), [ver] "v"(ex + (kh ^ 1) * KVH), [ro] "s"(ro), [vo1] "v"(vo1_), [vo2] "v"(vo2_)
      : "memory","scc","v0","vcc","v1","v2","v3","v4","v5","v6","v7","v8","v9","v10","v11","v12","v13","v14","v15","v16","v17","v18","v19","v20","v21","v22","v23","v24","v25","v26","v27","v28","v29","v30","v31","v32","v33","v34","v35","v36","v37","v38","v39","v40","v41","v42","v43","v44","v45","v46","v47","v48","v49","v50","v51","v52","v53","v54","v55","v56","v57","v58","v59","v60","v61","v62","v63","v64","v65","v66","v67","v68","v69","v70","v71","v72","v73","v74","v75","v76","v77","v78","v79","v88","v89","v90","v91","v92","v93","v94","v95","v96","v97","v98","v99","v100","v101","v102","v103","v104","v105","v106","v110","v111","v112","v113","v114","v115","v116","v117","v118","v119","v120","v121","v122","v123","v124","v125","v126","v127","v128","v129","v130","v131","v132","v133","v134","v135","v136","v137","v138","v139","v140","v141");
  }
  if (__builtin_expect(__builtin_amdgcn_readfirstlane((u32)bad | (u32)(bad >> 32)) != 0, 0)) {
    float o[10];
    attn_slow(ws, h, st, l, o);
    const __amdgpu_buffer_rsrc_t r2 = __builtin_amdgcn_make_buffer_rsrc((char*)ob0, 0, 36864, 0x00020000);
    __builtin_amdgcn_raw_buffer_store_b128(u32x4{pk2(o[0], o[1]), pk2(o[2], o[3]), pk2(o[4], o[5]), pk2(o[6], o[7])}, r2, vo1_, 0, 0);
    __builtin_amdgcn_raw_buffer_store_b32(pk2(o[8], o[9]), r2, vo2_, 0, 0);
  }
}

__global__ __launch_bounds__(256) void k5_ffn(char*, u16*) {
  __shared__ __attribute__((aligned(16))) char sm[160256];
  int b = blockIdx.x, tid = threadIdx.x;
  uint64_t pw = TAB(0), px = TAB(1), ra, rb;
  u16* out = GP(u16, TAB(5));
  int w = __builtin_amdgcn_readfirstlane(tid >> 6);
  const u32 lb = (u32)(uintptr_t)sm, vl = (tid & 63) * 16;
  if (w < 3) {
    const u32 l4 = lb + 77824, al = l4 + w * 2048;
    u32 rv;
    asm volatile(
"s_nop 4\n"
"s_add_u32 s22, %[w0], %[ab]\n"
"s_addc_u32 s23, %[w1], 0\n"
"s_add_u32 s20, %[w0], %[wb]\n"
"s_addc_u32 s21, %[w1], 0\n"
"v_add_u32 v104, 17408, %[vl]\n"
"s_add_u32 m0, %[al], 0\n"
"s_nop 0\n"
"global_load_lds_dwordx4 %[vl], s[22:23] offset:0\n"
"global_load_lds_dwordx4 v104, s[22:23] offset:1024\n"
"global_load_dwordx4 a[0:3], %[vl], s[20:21] offset:-4096\n"
"global_load_dwordx4 a[4:7], %[vl], s[20:21] offset:-3072\n"
"global_load_dwordx4 a[8:11], %[vl], s[20:21] offset:-2048\n"
"s_add_u32 m0, %[al], 6144\n"
"s_add_u32 s22, s22, 3072\n"
"s_addc_u32 s23, s23, 0\n"
"global_load_lds_dwordx4 %[vl], s[22:23] offset:0\n"
"global_load_lds_dwordx4 v104, s[22:23] offset:1024\n"
"global_load_dwordx4 a[12:15], %[vl], s[20:21] offset:-1024\n"
"global_load_dwordx4 a[16:19], %[vl], s[20:21] offset:0\n"
"global_load_dwordx4 a[20:23], %[vl], s[20:21] offset:1024\n"
"global_load_dwordx4 a[24:27], %[vl], s[20:21] offset:2048\n"
"global_load_dwordx4 a[28:31], %[vl], s[20:21] offset:3072\n"
"s_add_u32 s20, s20, 8192\n"
"s_addc_u32 s21, s21, 0\n"
"global_load_dwordx4 a[32:35], %[vl], s[20:21] offset:-4096\n"
"global_load_dwordx4 a[36:39], %[vl], s[20:21] offset:-3072\n"
"global_load_dwordx4 a[40:43], %[vl], s[20:21] offset:-2048\n"
"global_load_dwordx4 a[44:47], %[vl], s[20:21] offset:-1024\n"
"s_add_u32 m0, %[al], 12288\n"
"s_add_u32 s22, s22, 3072\n"
"s_addc_u32 s23, s23, 0\n"
"global_load_lds_dwordx4 %[vl], s[22:23] offset:0\n"
"global_load_lds_dwordx4 v104, s[22:23] offset:1024\n"
"global_load_dwordx4 a[48:51], %[vl], s[20:21] offset:0\n"
"global_load_dwordx4 a[52:55], %[vl], s[20:21] offset:1024\n"
"global_load_dwordx4 a[56:59], %[vl], s[20:21] offset:2048\n"
"global_load_dwordx4 a[60:63], %[vl], s[20:21] offset:3072\n"
"s_add_u32 s20, s20, 8192\n"
"s_addc_u32 s21, s21, 0\n"
"global_load_dwordx4 a[64:67], %[vl], s[20:21] offset:-4096\n"
"global_load_dwordx4 a[68:71], %[vl], s[20:21] offset:-3072\n"
      : "={s[22:23]}"(ra), "={s[20:21]}"(rb), "={v104}"(rv)
      : [w0] "s"((u32)pw), [w1] "s"((u32)(pw >> 32)), [ab] "s"((u32)O_OFR + (2 * (b >> 3) + ((b >> 2) & 1)) * 36864 + w * 1024),
        [wb] "s"((u32)O_WO + (3 * (b & 3) + w) * 55296 + 4096), [al] "s"(al), [vl] "v"(vl)
      : "memory","scc","a0","a1","a2","a3","a4","a5","a6","a7","a8","a9","a10","a11","a12","a13","a14","a15","a16","a17","a18","a19","a20","a21","a22","a23","a24","a25","a26","a27","a28","a29","a30","a31","a32","a33","a34","a35","a36","a37","a38","a39","a40","a41","a42","a43","a44","a45","a46","a47","a48","a49","a50","a51","a52","a53","a54","a55","a56","a57","a58","a59","a60","a61","a62","a63","a64","a65","a66","a67","a68","a69","a70","a71","a72","a73","a74","a75","a76","a77","a78","a79","a80","a81","a82","a83","a84","a85","a86","a87","a88","a89","a90","a91","a92","a93","a94","a95","a96","a97","a98","a99","a100","a101","a102","a103","a104","a105","a106","a107","a108","a109","a110","a111","a112","a113","a114","a115","a116","a117","a118","a119","a120","a121","a122","a123","a124","a125","a126","a127","a128","a129","a130","a131","a132","a133","a134","a135","a136","a137","a138","a139","a140","a141","a142","a143","a144","a145","a146","a147","a148","a149","a150","a151","a152","a153","a154","a155","a156","a157","a158","a159","a160","a161","a162","a163","a164","a165","a166","a167","a168","a169","a170","a171","a172","a173","a174","a175","a176","a177","a178","a179","a180","a181","a182","a183","a184","a185","a186","a187","a188","a189","a190","a191","a192","a193","a194","a195","a196","a197","a198","a199","a200","a201","a202","a203","a204","a205","a206","a207","a208","a209","a210","a211","a212","a213","a214","a215","v0","v1","v2","v3","v4","v5","v6","v7","v8","v9","v10","v11","v12","v13","v14","v15","v16","v17","v18","v19","v20","v21","v22","v23","v24","v25","v26","v27","v28","v29","v30","v31","v32","v33","v34","v35","v36","v37","v38","v39","v40","v41","v42","v43","v44","v45","v46","v47","v48","v49","v50","v51","v52","v53","v54","v55","v56","v57","v58","v59","v60","v61","v62","v63","v64","v65","v66","v67","v68","v69","v70","v71","v72","v73","v74","v75","v76","v77","v78","v79","v80","v81","v82","v83","v84","v85","v86","v87","v88","v89","v90","v91","v92","v93","v94","v95","v96","v97","v98","v99","v100","v101","v102","v103");
    asm volatile("" : "+v"(tid)); asm volatile("" : "+s"(b), "+s"(w), "+s"(pw), "+s"(px));
    char* ws = GP(char, pw); const u16* x = GP(const u16, px);
    const int l = tid & 63, n = l & 15, g = l >> 4;
    const int rt = 2 * (b >> 3) + ((b >> 2) & 1), cg = b & 3;
    const int T0 = 9 * cg + 3 * w;
    const char* xb = (const char*)x + (u32)(rt * 32 * 1152 + T0 * 32);
    const char* gb = ws + (u32)(O_MOD + 2 * D_ * 4 + T0 * 64);
    char* ob = ws + (u32)(O_X1 + rt * 36864 + T0 * 512);
    char* stb = ws + (u32)(O_ST + (8 * rt + cg) * 384 + w * 8);
    const u32 xl = l4 + 36864 + w * 3072;
    const u32 va = l4 + l * 16, vr = xl + n * 96 + g * 8, vg = g * 16;
    const u32 vx0 = (l / 6) * 1152 + (l % 6) * 16, vx1 = ((l + 64) / 6) * 1152 + ((l + 64) % 6) * 16 - 1024, vx2 = ((l + 128) / 6) * 1152 + ((l + 128) % 6) * 16 - 2048;
    const u32 vo = (g & 1) * 18432 + (g >> 1) * 256 + n * 16, vs = (g & 1) * 1536 + n * 24;
    asm volatile(
"s_nop 4\n"
"v_mov_b32 v84, 0\n"
"v_mov_b32 v85, 0\n"
"s_waitcnt vmcnt(22)\n"
"s_barrier\n"
"ds_read_b128 v[88:91], %[va] offset:0\n"
"ds_read_b128 v[92:95], %[va] offset:1024\n"
"s_waitcnt lgkmcnt(0)\n"
"s_waitcnt vmcnt(21)\n"
"v_mfma_f32_16x16x32_bf16 v[0:3], a[0:3], v[88:91], 0\n"
"global_load_dwordx4 a[72:75], %[vl], s[20:21] offset:-2048\n"
"ds_read_b128 v[96:99], %[va] offset:2048\n"
"v_mfma_f32_16x16x32_bf16 v[4:7], a[0:3], v[92:95], 0\n"
"global_load_dwordx4 a[76:79], %[vl], s[20:21] offset:-1024\n"
"ds_read_b128 v[100:103], %[va] offset:3072\n"
"s_waitcnt vmcnt(22)\n"
"v_mfma_f32_16x16x32_bf16 v[8:11], a[4:7], v[88:91], 0\n"
"global_load_dwordx4 a[80:83], %[vl], s[20:21] offset:0\n"
"v_mfma_f32_16x16x32_bf16 v[12:15], a[4:7], v[92:95], 0\n"
"s_add_u32 m0, %[al], 18432\n"
"s_add_u32 s22, s22, 3072\n"
"s_addc_u32 s23, s23, 0\n"
"global_load_lds_dwordx4 %[vl], s[22:23] offset:0\n"
"s_waitcnt vmcnt(23)\n"
"v_mfma_f32_16x16x32_bf16 v[16:19], a[8:11], v[88:91], 0\n"
"global_load_lds_dwordx4 v104, s[22:23] offset:1024\n"
"v_mfma_f32_16x16x32_bf16 v[20:23], a[8:11], v[92:95], 0\n"
"global_load_dwordx4 a[84:87], %[vl], s[20:21] offset:1024\n"
"s_waitcnt lgkmcnt(0)\n"
"s_waitcnt vmcnt(22)\n"
"v_mfma_f32_16x16x32_bf16 v[0:3], a[12:15], v[96:99], v[0:3]\n"
"global_load_dwordx4 a[88:91], %[vl], s[20:21] offset:2048\n"
"ds_read_b128 v[88:91], %[va] offset:4096\n"
"v_mfma_f32_16x16x32_bf16 v[4:7], a[12:15], v[100:103], v[4:7]\n"
"global_load_dwordx4 a[92:95], %[vl], s[20:21] offset:3072\n"
"ds_read_b128 v[92:95], %[va] offset:5120\n"
"s_waitcnt vmcnt(23)\n"
"v_mfma_f32_16x16x32_bf16 v[8:11], a[16:19], v[96:99], v[8:11]\n"
"s_add_u32 s20, s20, 8192\n"
"s_addc_u32 s21, s21, 0\n"
"global_load_dwordx4 a[96:99], %[vl], s[20:21] offset:-4096\n"
"v_mfma_f32_16x16x32_bf16 v[12:15], a[16:19], v[100:103], v[12:15]\n"
"global_load_dwordx4 a[100:103], %[vl], s[20:21] offset:-3072\n"
"s_waitcnt vmcnt(24)\n"
"v_mfma_f32_16x16x32_bf16 v[16:19], a[20:23], v[96:99], v[16:19]\n"
"global_load_dwordx4 a[104:107], %[vl], s[20:21] offset:-2048\n"
"v_mfma_f32_16x16x32_bf16 v[20:23], a[20:23], v[100:103], v[20:23]\n"
"global_load_dwordx4 a[108:111], %[vl], s[20:21] offset:-1024\n"
"s_waitcnt lgkmcnt(0)\n"
"s_waitcnt vmcnt(25)\n"
"v_mfma_f32_16x16x32_bf16 v[0:3], a[24:27], v[88:91], v[0:3]\n"
"s_mov_b32 m0, %[xl]\n"
"s_nop 0\n"
"global_load_lds_dwordx4 %[vx0], %[xb] offset:0\n"
"s_barrier\n"
"ds_read_b128 v[96:99], %[va] offset:6144\n"
"v_mfma_f32_16x16x32_bf16 v[4:7], a[24:27], v[92:95], v[4:7]\n"
"global_load_lds_dwordx4 %[vx1], %[xb] offset:1024\n"
"ds_read_b128 v[100:103], %[va] offset:7168\n"
"s_waitcnt vmcnt(26)\n"
"v_mfma_f32_16x16x32_bf16 v[8:11], a[28:31], v[88:91], v[8:11]\n"
"global_load_lds_dwordx4 %[vx2], %[xb] offset:2048\n"
"v_mfma_f32_16x16x32_bf16 v[12:15], a[28:31], v[92:95], v[12:15]\n"
"global_load_dwordx4 a[112:115], %[vl], s[20:21] offset:0\n"
"s_waitcnt vmcnt(27)\n"
"v_mfma_f32_16x16x32_bf16 v[16:19], a[32:35], v[88:91], v[16:19]\n"
"global_load_dwordx4 a[116:119], %[vl], s[20:21] offset:1024\n"
"v_mfma_f32_16x16x32_bf16 v[20:23], a[32:35], v[92:95], v[20:23]\n"
"s_add_u32 m0, %[al], 24576\n"
"s_add_u32 s22, s22, 3072\n"
"s_addc_u32 s23, s23, 0\n"
"global_load_lds_dwordx4 %[vl], s[22:23] offset:0\n"
"s_waitcnt lgkmcnt(0)\n"
"s_waitcnt vmcnt(28)\n"
"v_mfma_f32_16x16x32_bf16 v[0:3], a[36:39], v[96:99], v[0:3]\n"
"global_load_lds_dwordx4 v104, s[22:23] offset:1024\n"
"ds_read_b128 v[88:91], %[va] offset:8192\n"
"v_mfma_f32_16x16x32_bf16 v[4:7], a[36:39], v[100:103], v[4:7]\n"
"global_load_dwordx4 a[120:123], %[vl], s[20:21] offset:2048\n"
"ds_read_b128 v[92:95], %[va] offset:9216\n"
"s_waitcnt vmcnt(29)\n"
"v_mfma_f32_16x16x32_bf16 v[8:11], a[40:43], v[96:99], v[8:11]\n"
"global_load_dwordx4 v[60:63], %[vg], %[gb] offset:0\n"
"v_mfma_f32_16x16x32_bf16 v[12:15], a[40:43], v[100:103], v[12:15]\n"
"global_load_dwordx4 v[64:67], %[vg], %[gb] offset:64\n"
"s_waitcnt vmcnt(30)\n"
"v_mfma_f32_16x16x32_bf16 v[16:19], a[44:47], v[96:99], v[16:19]\n"
"global_load_dwordx4 v[68:71], %[vg], %[gb] offset:128\n"
"v_mfma_f32_16x16x32_bf16 v[20:23], a[44:47], v[100:103], v[20:23]\n"
"global_load_dwordx4 a[124:127], %[vl], s[20:21] offset:3072\n"
"s_waitcnt lgkmcnt(0)\n"
"s_waitcnt vmcnt(29)\n"
"v_mfma_f32_16x16x32_bf16 v[0:3], a[48:51], v[88:91], v[0:3]\n"
"s_add_u32 s20, s20, 8192\n"
"s_addc_u32 s21, s21, 0\n"
"global_load_dwordx4 a[128:131], %[vl], s[20:21] offset:-4096\n"
"ds_read_b128 v[96:99], %[va] offset:10240\n"
"v_mfma_f32_16x16x32_bf16 v[4:7], a[48:51], v[92:95], v[4:7]\n"
"global_load_dwordx4 a[132:135], %[vl], s[20:21] offset:-3072\n"
"ds_read_b128 v[100:103], %[va] offset:11264\n"
"s_waitcnt vmcnt(30)\n"
"v_mfma_f32_16x16x32_bf16 v[8:11], a[52:55], v[88:91], v[8:11]\n"
"global_load_dwordx4 a[136:139], %[vl], s[20:21] offset:-2048\n"
"v_mfma_f32_16x16x32_bf16 v[12:15], a[52:55], v[92:95], v[12:15]\n"
"global_load_dwordx4 a[140:143], %[vl], s[20:21] offset:-1024\n"
"s_waitcnt vmcnt(31)\n"
"v_mfma_f32_16x16x32_bf16 v[16:19], a[56:59], v[88:91], v[16:19]\n"
"global_load_dwordx4 a[144:147], %[vl], s[20:21] offset:0\n"
"v_mfma_f32_16x16x32_bf16 v[20:23], a[56:59], v[92:95], v[20:23]\n"
"global_load_dwordx4 a[148:151], %[vl], s[20:21] offset:1024\n"
"s_waitcnt lgkmcnt(0)\n"
"s_waitcnt vmcnt(32)\n"
"v_mfma_f32_16x16x32_bf16 v[0:3], a[60:63], v[96:99], v[0:3]\n"
"global_load_dwordx4 a[152:155], %[vl], s[20:21] offset:2048\n"
"s_barrier\n"
"ds_read_b128 v[88:91], %[va] offset:12288\n"
"v_mfma_f32_16x16x32_bf16 v[4:7], a[60:63], v[100:103], v[4:7]\n"
"s_add_u32 m0, %[al], 30720\n"
"s_add_u32 s22, s22, 3072\n"
"s_addc_u32 s23, s23, 0\n"
"global_load_lds_dwordx4 %[vl], s[22:23] offset:0\n"
"ds_read_b128 v[92:95], %[va] offset:13312\n"
"s_waitcnt vmcnt(33)\n"
"v_mfma_f32_16x16x32_bf16 v[8:11], a[64:67], v[96:99], v[8:11]\n"
"global_load_lds_dwordx4 v104, s[22:23] offset:1024\n"
"v_mfma_f32_16x16x32_bf16 v[12:15], a[64:67], v[100:103], v[12:15]\n"
"global_load_dwordx4 a[156:159], %[vl], s[20:21] offset:3072\n"
"s_waitcnt vmcnt(34)\n"
"v_mfma_f32_16x16x32_bf16 v[16:19], a[68:71], v[96:99], v[16:19]\n"
"s_add_u32 s20, s20, 8192\n"
"s_addc_u32 s21, s21, 0\n"
"global_load_dwordx4 a[160:163], %[vl], s[20:21] offset:-4096\n"
"v_mfma_f32_16x16x32_bf16 v[20:23], a[68:71], v[100:103], v[20:23]\n"
"global_load_dwordx4 a[164:167], %[vl], s[20:21] offset:-3072\n"
"s_waitcnt lgkmcnt(0)\n"
"s_waitcnt vmcnt(35)\n"
"v_mfma_f32_16x16x32_bf16 v[0:3], a[72:75], v[88:91], v[0:3]\n"
"global_load_dwordx4 a[168:171], %[vl], s[20:21] offset:-2048\n"
"ds_read_b128 v[96:99], %[va] offset:14336\n"
"v_mfma_f32_16x16x32_bf16 v[4:7], a[72:75], v[92:95], v[4:7]\n"
"global_load_dwordx4 a[172:175], %[vl], s[20:21] offset:-1024\n"
"ds_read_b128 v[100:103], %[va] offset:15360\n"
"s_waitcnt vmcnt(36)\n"
"v_mfma_f32_16x16x32_bf16 v[8:11], a[76:79], v[88:91], v[8:11]\n"
"global_load_dwordx4 a[176:179], %[vl], s[20:21] offset:0\n"
"v_mfma_f32_16x16x32_bf16 v[12:15], a[76:79], v[92:95], v[12:15]\n"
"global_load_dwordx4 a[180:183], %[vl], s[20:21] offset:1024\n"
"s_waitcnt vmcnt(37)\n"
"v_mfma_f32_16x16x32_bf16 v[16:19], a[80:83], v[88:91], v[16:19]\n"
"global_load_dwordx4 a[184:187], %[vl], s[20:21] offset:2048\n"
"v_mfma_f32_16x16x32_bf16 v[20:23], a[80:83], v[92:95], v[20:23]\n"
"global_load_dwordx4 a[188:191], %[vl], s[20:21] offset:3072\n"
"s_waitcnt lgkmcnt(0)\n"
"s_waitcnt vmcnt(36)\n"
"v_mfma_f32_16x16x32_bf16 v[0:3], a[84:87], v[96:99], v[0:3]\n"
"s_add_u32 s20, s20, 8192\n"
"s_addc_u32 s21, s21, 0\n"
"global_load_dwordx4 a[192:195], %[vl], s[20:21] offset:-4096\n"
"ds_read_b128 v[88:91], %[va] offset:16384\n"
"v_mfma_f32_16x16x32_bf16 v[4:7], a[84:87], v[100:103], v[4:7]\n"
"global_load_dwordx4 a[196:199], %[vl], s[20:21] offset:-3072\n"
"ds_read_b128 v[92:95], %[va] offset:17408\n"
"s_waitcnt vmcnt(37)\n"
"v_mfma_f32_16x16x32_bf16 v[8:11], a[88:91], v[96:99], v[8:11]\n"
"global_load_dwordx4 a[200:203], %[vl], s[20:21] offset:-2048\n"
"v_mfma_f32_16x16x32_bf16 v[12:15], a[88:91], v[100:103], v[12:15]\n"
"global_load_dwordx4 a[204:207], %[vl], s[20:21] offset:-1024\n"
"s_waitcnt vmcnt(38)\n"
"v_mfma_f32_16x16x32_bf16 v[16:19], a[92:95], v[96:99], v[16:19]\n"
"global_load_dwordx4 a[208:211], %[vl], s[20:21] offset:0\n"
"v_mfma_f32_16x16x32_bf16 v[20:23], a[92:95], v[100:103], v[20:23]\n"
"global_load_dwordx4 a[212:215], %[vl], s[20:21] offset:1024\n"
"s_waitcnt lgkmcnt(0)\n"
"s_waitcnt vmcnt(39)\n"
"v_mfma_f32_16x16x32_bf16 v[0:3], a[96:99], v[88:91], v[0:3]\n"
"s_barrier\n"
"ds_read_b128 v[96:99], %[va] offset:18432\n"
"v_mfma_f32_16x16x32_bf16 v[4:7], a[96:99], v[92:95], v[4:7]\n"
"ds_read_b128 v[100:103], %[va] offset:19456\n"
"s_waitcnt vmcnt(38)\n"
"v_mfma_f32_16x16x32_bf16 v[8:11], a[100:103], v[88:91], v[8:11]\n"
"v_mfma_f32_16x16x32_bf16 v[12:15], a[100:103], v[92:95], v[12:15]\n"
"s_waitcnt vmcnt(37)\n"
"v_mfma_f32_16x16x32_bf16 v[16:19], a[104:107], v[88:91], v[16:19]\n"
"v_mfma_f32_16x16x32_bf16 v[20:23], a[104:107], v[92:95], v[20:23]\n"
"s_waitcnt lgkmcnt(0)\n"
"s_waitcnt vmcnt(36)\n"
"v_mfma_f32_16x16x32_bf16 v[0:3], a[108:111], v[96:99], v[0:3]\n"
"ds_read_b128 v[88:91], %[va] offset:20480\n"
"v_mfma_f32_16x16x32_bf16 v[4:7], a[108:111], v[100:103], v[4:7]\n"
"ds_read_b128 v[92:95], %[va] offset:21504\n"
"s_waitcnt vmcnt(32)\n"
"v_mfma_f32_16x16x32_bf16 v[8:11], a[112:115], v[96:99], v[8:11]\n"
"v_mfma_f32_16x16x32_bf16 v[12:15], a[112:115], v[100:103], v[12:15]\n"
"s_waitcnt vmcnt(31)\n"
"v_mfma_f32_16x16x32_bf16 v[16:19], a[116:119], v[96:99], v[16:19]\n"
"v_mfma_f32_16x16x32_bf16 v[20:23], a[116:119], v[100:103], v[20:23]\n"
"s_waitcnt lgkmcnt(0)\n"
"s_waitcnt vmcnt(28)\n"
"v_mfma_f32_16x16x32_bf16 v[0:3], a[120:123], v[88:91], v[0:3]\n"
"ds_read_b128 v[96:99], %[va] offset:22528\n"
"v_mfma_f32_16x16x32_bf16 v[4:7], a[120:123], v[92:95], v[4:7]\n"
"ds_read_b128 v[100:103], %[va] offset:23552\n"
"s_waitcnt vmcnt(24)\n"
"v_mfma_f32_16x16x32_bf16 v[8:11], a[124:127], v[88:91], v[8:11]\n"
"v_mfma_f32_16x16x32_bf16 v[12:15], a[124:127], v[92:95], v[12:15]\n"
"s_waitcnt vmcnt(23)\n"
"v_mfma_f32_16x16x32_bf16 v[16:19], a[128:131], v[88:91], v[16:19]\n"
"v_mfma_f32_16x16x32_bf16 v[20:23], a[128:131], v[92:95], v[20:23]\n"
"s_waitcnt lgkmcnt(0)\n"
"s_waitcnt vmcnt(22)\n"
"v_mfma_f32_16x16x32_bf16 v[0:3], a[132:135], v[96:99], v[0:3]\n"
"s_barrier\n"
"ds_read_b128 v[88:91], %[va] offset:24576\n"
"v_mfma_f32_16x16x32_bf16 v[4:7], a[132:135], v[100:103], v[4:7]\n"
"ds_read_b128 v[92:95], %[va] offset:25600\n"
"s_waitcnt vmcnt(21)\n"
"v_mfma_f32_16x16x32_bf16 v[8:11], a[136:139], v[96:99], v[8:11]\n"
"v_mfma_f32_16x16x32_bf16 v[12:15], a[136:139], v[100:103], v[12:15]\n"
"s_waitcnt vmcnt(20)\n"
"v_mfma_f32_16x16x32_bf16 v[16:19], a[140:143], v[96:99], v[16:19]\n"
"v_mfma_f32_16x16x32_bf16 v[20:23], a[140:143], v[100:103], v[20:23]\n"
"s_waitcnt lgkmcnt(0)\n"
"s_waitcnt vmcnt(19)\n"
"v_mfma_f32_16x16x32_bf16 v[0:3], a[144:147], v[88:91], v[0:3]\n"
"ds_read_b128 v[96:99], %[va] offset:26624\n"
"v_mfma_f32_16x16x32_bf16 v[4:7], a[144:147], v[92:95], v[4:7]\n"
"ds_read_b128 v[100:103], %[va] offset:27648\n"
"s_waitcnt vmcnt(18)\n"
"v_mfma_f32_16x16x32_bf16 v[8:11], a[148:151], v[88:91], v[8:11]\n"
"v_mfma_f32_16x16x32_bf16 v[12:15], a[148:151], v[92:95], v[12:15]\n"
"s_waitcnt vmcnt(17)\n"
"v_mfma_f32_16x16x32_bf16 v[16:19], a[152:155], v[88:91], v[16:19]\n"
"v_mfma_f32_16x16x32_bf16 v[20:23], a[152:155], v[92:95], v[20:23]\n"
"s_waitcnt lgkmcnt(0)\n"
"s_waitcnt vmcnt(14)\n"
"v_mfma_f32_16x16x32_bf16 v[0:3], a[156:159], v[96:99], v[0:3]\n"
"ds_read_b128 v[88:91], %[va] offset:28672\n"
"v_mfma_f32_16x16x32_bf16 v[4:7], a[156:159], v[100:103], v[4:7]\n"
"ds_read_b128 v[92:95], %[va] offset:29696\n"
"s_waitcnt vmcnt(13)\n"
"v_mfma_f32_16x16x32_bf16 v[8:11], a[160:163], v[96:99], v[8:11]\n"
"v_mfma_f32_16x16x32_bf16 v[12:15], a[160:163], v[100:103], v[12:15]\n"
"s_waitcnt vmcnt(12)\n"
"v_mfma_f32_16x16x32_bf16 v[16:19], a[164:167], v[96:99], v[16:19]\n"
"v_mfma_f32_16x16x32_bf16 v[20:23], a[164:167], v[100:103], v[20:23]\n"
"s_waitcnt lgkmcnt(0)\n"
"s_waitcnt vmcnt(11)\n"
"v_mfma_f32_16x16x32_bf16 v[0:3], a[168:171], v[88:91], v[0:3]\n"
"s_barrier\n"
"ds_read_b128 v[96:99], %[va] offset:30720\n"
"v_mfma_f32_16x16x32_bf16 v[4:7], a[168:171], v[92:95], v[4:7]\n"
"ds_read_b128 v[100:103], %[va] offset:31744\n"
"s_waitcnt vmcnt(10)\n"
"v_mfma_f32_16x16x32_bf16 v[8:11], a[172:175], v[88:91], v[8:11]\n"
"ds_read_b64 v[48:49], %[vr] offset:0\n"
"v_mfma_f32_16x16x32_bf16 v[12:15], a[172:175], v[92:95], v[12:15]\n"
"ds_read_b64 v[50:51], %[vr] offset:1536\n"
"s_waitcnt vmcnt(9)\n"
"v_mfma_f32_16x16x32_bf16 v[16:19], a[176:179], v[88:91], v[16:19]\n"
"ds_read_b64 v[52:53], %[vr] offset:32\n"
"v_mfma_f32_16x16x32_bf16 v[20:23], a[176:179], v[92:95], v[20:23]\n"
"s_waitcnt lgkmcnt(3)\n"
"s_waitcnt vmcnt(8)\n"
"v_mfma_f32_16x16x32_bf16 v[0:3], a[180:183], v[96:99], v[0:3]\n"
"ds_read_b128 v[88:91], %[va] offset:32768\n"
"v_mfma_f32_16x16x32_bf16 v[4:7], a[180:183], v[100:103], v[4:7]\n"
"ds_read_b128 v[92:95], %[va] offset:33792\n"
"s_waitcnt vmcnt(7)\n"
"v_mfma_f32_16x16x32_bf16 v[8:11], a[184:187], v[96:99], v[8:11]\n"
"ds_read_b64 v[54:55], %[vr] offset:1568\n"
"v_mfma_f32_16x16x32_bf16 v[12:15], a[184:187], v[100:103], v[12:15]\n"
"ds_read_b64 v[56:57], %[vr] offset:64\n"
"s_waitcnt vmcnt(6)\n"
"v_mfma_f32_16x16x32_bf16 v[16:19], a[188:191], v[96:99], v[16:19]\n"
"ds_read_b64 v[58:59], %[vr] offset:1600\n"
"v_mfma_f32_16x16x32_bf16 v[20:23], a[188:191], v[100:103], v[20:23]\n"
"s_waitcnt lgkmcnt(3)\n"
"s_waitcnt vmcnt(5)\n"
"v_mfma_f32_16x16x32_bf16 v[0:3], a[192:195], v[88:91], v[0:3]\n"
"ds_read_b128 v[96:99], %[va] offset:34816\n"
"v_lshlrev_b32 v24, 16, v48\n"
"v_and_b32 v25, 0xffff0000, v48\n"
"v_mfma_f32_16x16x32_bf16 v[4:7], a[192:195], v[92:95], v[4:7]\n"
"ds_read_b128 v[100:103], %[va] offset:35840\n"
"v_lshlrev_b32 v26, 16, v49\n"
"v_and_b32 v27, 0xffff0000, v49\n"
"s_waitcnt vmcnt(4)\n"
"v_mfma_f32_16x16x32_bf16 v[8:11], a[196:199], v[88:91], v[8:11]\n"
"v_lshlrev_b32 v28, 16, v50\n"
"v_and_b32 v29, 0xffff0000, v50\n"
"v_mfma_f32_16x16x32_bf16 v[12:15], a[196:199], v[92:95], v[12:15]\n"
"v_lshlrev_b32 v30, 16, v51\n"
"v_and_b32 v31, 0xffff0000, v51\n"
"s_waitcnt vmcnt(3)\n"
"v_mfma_f32_16x16x32_bf16 v[16:19], a[200:203], v[88:91], v[16:19]\n"
"v_lshlrev_b32 v32, 16, v52\n"
"v_and_b32 v33, 0xffff0000, v52\n"
"v_mfma_f32_16x16x32_bf16 v[20:23], a[200:203], v[92:95], v[20:23]\n"
"v_lshlrev_b32 v34, 16, v53\n"
"v_and_b32 v35, 0xffff0000, v53\n"
"s_waitcnt lgkmcnt(0)\n"
"s_waitcnt vmcnt(2)\n"
"v_mfma_f32_16x16x32_bf16 v[0:3], a[204:207], v[96:99], v[0:3]\n"
"v_lshlrev_b32 v36, 16, v54\n"
"v_and_b32 v37, 0xffff0000, v54\n"
"v_mfma_f32_16x16x32_bf16 v[4:7], a[204:207], v[100:103], v[4:7]\n"
"v_lshlrev_b32 v38, 16, v55\n"
"v_and_b32 v39, 0xffff0000, v55\n"
"s_waitcnt vmcnt(1)\n"
"v_mfma_f32_16x16x32_bf16 v[8:11], a[208:211], v[96:99], v[8:11]\n"
"v_lshlrev_b32 v40, 16, v56\n"
"v_and_b32 v41, 0xffff0000, v56\n"
"v_mfma_f32_16x16x32_bf16 v[12:15], a[208:211], v[100:103], v[12:15]\n"
"v_lshlrev_b32 v42, 16, v57\n"
"v_and_b32 v43, 0xffff0000, v57\n"
"s_waitcnt vmcnt(0)\n"
"v_mfma_f32_16x16x32_bf16 v[16:19], a[212:215], v[96:99], v[16:19]\n"
"v_lshlrev_b32 v44, 16, v58\n"
"v_and_b32 v45, 0xffff0000, v58\n"
"v_mfma_f32_16x16x32_bf16 v[20:23], a[212:215], v[100:103], v[20:23]\n"
"v_lshlrev_b32 v46, 16, v59\n"
"v_and_b32 v47, 0xffff0000, v59\n"
"v_fmac_f32 v24, v60, v0\n"
"v_fmac_f32 v25, v61, v1\n"
"v_fmac_f32 v26, v62, v2\n"
"v_fmac_f32 v27, v63, v3\n"
"v_fmac_f32 v28, v60, v4\n"
"v_fmac_f32 v29, v61, v5\n"
"v_fmac_f32 v30, v62, v6\n"
"v_fmac_f32 v31, v63, v7\n"
"v_cvt_pk_bf16_f32 v72, v24, v25\n"
"v_cvt_pk_bf16_f32 v73, v26, v27\n"
"v_cvt_pk_bf16_f32 v74, v28, v29\n"
"v_cvt_pk_bf16_f32 v75, v30, v31\n"
"v_fmac_f32 v32, v64, v8\n"
"v_fmac_f32 v33, v65, v9\n"
"v_fmac_f32 v34, v66, v10\n"
"v_fmac_f32 v35, v67, v11\n"
"v_fmac_f32 v36, v64, v12\n"
"v_fmac_f32 v37, v65, v13\n"
"v_fmac_f32 v38, v66, v14\n"
"v_fmac_f32 v39, v67, v15\n"
"v_cvt_pk_bf16_f32 v76, v32, v33\n"
"v_cvt_pk_bf16_f32 v77, v34, v35\n"
"v_cvt_pk_bf16_f32 v78, v36, v37\n"
"v_cvt_pk_bf16_f32 v79, v38, v39\n"
"v_permlane16_swap_b32 v72, v74\n"
"v_permlane16_swap_b32 v73, v75\n"
"v_fmac_f32 v40, v68, v16\n"
"v_fmac_f32 v41, v69, v17\n"
"v_fmac_f32 v42, v70, v18\n"
"v_fmac_f32 v43, v71, v19\n"
"v_fmac_f32 v44, v68, v20\n"
"v_fmac_f32 v45, v69, v21\n"
"v_fmac_f32 v46, v70, v22\n"
"v_fmac_f32 v47, v71, v23\n"
"v_cvt_pk_bf16_f32 v80, v40, v41\n"
"v_cvt_pk_bf16_f32 v81, v42, v43\n"
"v_cvt_pk_bf16_f32 v82, v44, v45\n"
"v_cvt_pk_bf16_f32 v83, v46, v47\n"
"global_store_dwordx4 %[vo], v[72:75], %[ob] offset:0 sc1\n"
"v_permlane16_swap_b32 v76, v78\n"
"v_permlane16_swap_b32 v77, v79\n"
"v_dot2c_f32_bf16 v84, 0x3f803f80, v72\n"
"v_dot2c_f32_bf16 v85, v72, v72\n"
"v_dot2c_f32_bf16 v84, 0x3f803f80, v73\n"
"v_dot2c_f32_bf16 v85, v73, v73\n"
"v_dot2c_f32_bf16 v84, 0x3f803f80, v74\n"
"v_dot2c_f32_bf16 v85, v74, v74\n"
"v_dot2c_f32_bf16 v84, 0x3f803f80, v75\n"
"v_dot2c_f32_bf16 v85, v75, v75\n"
"global_store_dwordx4 %[vo], v[76:79], %[ob] offset:512 sc1\n"
"v_permlane16_swap_b32 v80, v82\n"
"v_permlane16_swap_b32 v81, v83\n"
"v_dot2c_f32_bf16 v84, 0x3f803f80, v76\n"
"v_dot2c_f32_bf16 v85, v76, v76\n"
"v_dot2c_f32_bf16 v84, 0x3f803f80, v77\n"
"v_dot2c_f32_bf16 v85, v77, v77\n"
"v_dot2c_f32_bf16 v84, 0x3f803f80, v78\n"
"v_dot2c_f32_bf16 v85, v78, v78\n"
"v_dot2c_f32_bf16 v84, 0x3f803f80, v79\n"
"v_dot2c_f32_bf16 v85, v79, v79\n"
"global_store_dwordx4 %[vo], v[80:83], %[ob] offset:1024 sc1\n"
"v_dot2c_f32_bf16 v84, 0x3f803f80, v80\n"
"v_dot2c_f32_bf16 v85, v80, v80\n"
"v_dot2c_f32_bf16 v84, 0x3f803f80, v81\n"
"v_dot2c_f32_bf16 v85, v81, v81\n"
"v_dot2c_f32_bf16 v84, 0x3f803f80, v82\n"
"v_dot2c_f32_bf16 v85, v82, v82\n"
"v_dot2c_f32_bf16 v84, 0x3f803f80, v83\n"
"v_dot2c_f32_bf16 v85, v83, v83\n"
"s_nop 1\n"
"v_mov_b32 v86, v84\n"
"v_mov_b32 v87, v85\n"
"s_nop 0\n"
"v_permlane32_swap_b32 v84, v86\n"
"v_permlane32_swap_b32 v85, v87\n"
"v_add_f32 v84, v84, v86\n"
"v_add_f32 v85, v85, v87\n"
"global_store_dwordx2 %[vs], v[84:85], %[stb] sc1\n"
"s_nop 1\n"
      : "+{s[22:23]}"(ra), "+{s[20:21]}"(rb), "+{v104}"(rv)
      : [xb] "s"(xb), [gb] "s"(gb), [ob] "s"(ob), [stb] "s"(stb), [al] "s"(al), [xl] "s"(xl), [vl] "v"(vl), [va] "v"(va),
        [vr] "v"(vr), [vg] "v"(vg), [vx0] "v"(vx0), [vx1] "v"(vx1), [vx2] "v"(vx2), [vo] "v"(vo), [vs] "v"(vs)
      : "memory","scc","a0","a1","a2","a3","a4","a5","a6","a7","a8","a9","a10","a11","a12","a13","a14","a15","a16","a17","a18","a19","a20","a21","a22","a23","a24","a25","a26","a27","a28","a29","a30","a31","a32","a33","a34","a35","a36","a37","a38","a39","a40","a41","a42","a43","a44","a45","a46","a47","a48","a49","a50","a51","a52","a53","a54","a55","a56","a57","a58","a59","a60","a61","a62","a63","a64","a65","a66","a67","a68","a69","a70","a71","a72","a73","a74","a75","a76","a77","a78","a79","a80","a81","a82","a83","a84","a85","a86","a87","a88","a89","a90","a91","a92","a93","a94","a95","a96","a97","a98","a99","a100","a101","a102","a103","a104","a105","a106","a107","a108","a109","a110","a111","a112","a113","a114","a115","a116","a117","a118","a119","a120","a121","a122","a123","a124","a125","a126","a127","a128","a129","a130","a131","a132","a133","a134","a135","a136","a137","a138","a139","a140","a141","a142","a143","a144","a145","a146","a147","a148","a149","a150","a151","a152","a153","a154","a155","a156","a157","a158","a159","a160","a161","a162","a163","a164","a165","a166","a167","a168","a169","a170","a171","a172","a173","a174","a175","a176","a177","a178","a179","a180","a181","a182","a183","a184","a185","a186","a187","a188","a189","a190","a191","a192","a193","a194","a195","a196","a197","a198","a199","a200","a201","a202","a203","a204","a205","a206","a207","a208","a209","a210","a211","a212","a213","a214","a215","v0","v1","v2","v3","v4","v5","v6","v7","v8","v9","v10","v11","v12","v13","v14","v15","v16","v17","v18","v19","v20","v21","v22","v23","v24","v25","v26","v27","v28","v29","v30","v31","v32","v33","v34","v35","v36","v37","v38","v39","v40","v41","v42","v43","v44","v45","v46","v47","v48","v49","v50","v51","v52","v53","v54","v55","v56","v57","v58","v59","v60","v61","v62","v63","v64","v65","v66","v67","v68","v69","v70","v71","v72","v73","v74","v75","v76","v77","v78","v79","v80","v81","v82","v83","v84","v85","v86","v87","v88","v89","v90","v91","v92","v93","v94","v95","v96","v97","v98","v99","v100","v101","v102","v103");
  } else {
    u32 t;
    asm volatile("s_barrier\ns_barrier\ns_barrier\ns_barrier\ns_barrier\ns_barrier\nv_lshlrev_b32 %0, 3, %1\nglobal_load_dword a0, %0, %2\nv_add_u32 %0, 8192, %0\nglobal_load_dword a1, %0, %2\nv_add_u32 %0, 8192, %0\nglobal_load_dword a2, %0, %2\nv_add_u32 %0, 8192, %0\nglobal_load_dword a3, %0, %2\nv_add_u32 %0, 8192, %0\nglobal_load_dword a4, %0, %2\nv_add_u32 %0, 8192, %0\nv_min_u32 %0, 42496, %0\nglobal_load_dword a5, %0, %2\n"
      : "=&v"(t) : "v"(vl), "s"(pw + (u32)(O_WF + (b & 7) * 4 * 333 * 1024 + (b >> 3) * 333 * 128)) : "memory", "a0", "a1", "a2", "a3", "a4", "a5");
  }
  asm volatile("" : "+v"(tid)); asm volatile("" : "+s"(b), "+s"(w), "+s"(pw));
  asm volatile(
"s_nop 4\n"
"s_add_u32 s22, %[w0], %[xb]\n"
"s_addc_u32 s23, %[w1], 0\n"
"s_nop 0\n"
"s_add_u32 s20, %[w0], %[wb]\n"
"s_addc_u32 s21, %[w1], 0\n"
"s_add_u32 s26, %[w0], %[f0]\n"
"s_addc_u32 s27, %[w1], 0\n"
"v_lshlrev_b32 v0, 4, %[vl]\n"
"v_mov_b32 v1, 1\n"
"s_cmp_eq_u32 %[w], 3\n"
"s_cbranch_scc1 .Lsk_%=\n"
"s_waitcnt vmcnt(0)\n"
"s_mov_b64 exec, 255\n"
"global_store_dword v0, v1, s[26:27] sc1\n"
"s_mov_b64 exec, -1\n"
".Lsk_%=:\n"
"global_load_dwordx4 a[64:67], %[vl], s[20:21] offset:-4096\n"
"global_load_dwordx4 a[68:71], %[vl], s[20:21] offset:-3072\n"
"global_load_dwordx4 a[72:75], %[vl], s[20:21] offset:-2048\n"
"global_load_dwordx4 a[76:79], %[vl], s[20:21] offset:-1024\n"
    : "=&{s[22:23]}"(ra), "=&{s[20:21]}"(rb)
    : [w0] "s"((u32)pw), [w1] "s"((u32)(pw >> 32)), [xb] "s"((u32)O_X1 + (4 * (b >> 3) + w) * 18432), [wb] "s"((u32)O_WF + ((b & 7) * 4 + w) * 340992 + 4096),
      [f0] "s"((u32)O_FL0 + (b >> 3) * 2048 + (b & 7) * 16 + w * 4), [w] "s"(w), [vl] "v"(vl)
    : "memory","scc","a0","a1","a2","a3","a4","a5","a6","a7","a8","a9","a10","a11","a12","a13","a14","a15","a16","a17","a18","a19","a20","a21","a22","a23","a24","a25","a26","a27","a28","a29","a30","a31","a32","a33","a34","a35","a36","a37","a38","a39","a40","a41","a42","a43","a44","a45","a46","a47","a48","a49","a50","a51","a52","a53","a54","a55","a56","a57","a58","a59","a60","a61","a62","a63","a64","a65","a66","a67","a68","a69","a70","a71","a72","a73","a74","a75","a76","a77","a78","a79","a80","a81","a82","a83","a84","a85","a86","a87","a88","a89","a90","a91","a92","a93","a94","a95","a96","a97","a98","a99","a100","a101","a102","a103","a104","a105","a106","a107","a108","a109","a110","a111","a112","a113","a114","a115","a116","a117","a118","a119","a120","a121","a122","a123","a124","a125","a126","a127","a128","a129","a130","a131","a132","a133","a134","a135","a136","a137","a138","a139","a140","a141","a142","a143","a144","a145","a146","a147","a148","a149","a150","a151","a152","a153","a154","a155","a156","a157","a158","a159","a160","a161","a162","a163","a164","a165","a166","a167","a168","a169","a170","a171","a172","a173","a174","a175","a176","a177","a178","a179","a180","a181","a182","a183","a184","a185","a186","a187","a188","a189","a190","a191","s24","s26","s27","s28","s29","s30","s31","s36","s37","s38","s39","s40","s41","s42","s43","s44","s45","s46","v0","vcc","v1","v2","v3","v4","v5","v6","v7","v8","v9","v10","v11","v12","v13","v14","v15","v16","v17","v18","v19","v20","v21","v22","v23","v24","v25","v26","v27","v28","v29","v30","v31","v32","v33","v34","v35","v36","v37","v38","v39","v40","v41","v42","v43","v44","v45","v46","v47","v48","v49","v50","v51","v52","v53","v54","v55","v56","v57","v58","v59","v60","v61","v62","v63","v64","v65","v66","v67","v68","v69","v70","v71","v72","v73","v74","v75","v76","v77","v78","v79","v80","v81","v82","v83","v84","v85","v86","v87","v88","v89","v90","v91","v92","v93","v94","v95","v96","v97","v98","v99","v100","v101","v102","v103","v104","v105","v106","v107","v108","v109","v110","v111","v112","v113","v114","v115","v116","v117","v118","v119","v120","v121","v122","v123","v124","v125","v126","v127","v128","v129","v130","v131","v132","v133","v134","v135","v136","v137","v138","v139","v140","v141","v142","v143","v144","v145","v146","v147","v148","v149","v150","v151","v152","v153","v154","v155","v156","v157","v158","v159","v160","v161","v162","v163","v164","v165","v166","v167","v168","v169","v170","v171","v172","v173","v174","v175","v176","v177","v178","v179","v180","v181","v182","v183","v184","v185","v186","v187","v188","v189","v190","v191","v192","v193","v194","v195","v196","v197","v198","v199","v200","v201","v202","v203","v204","v205","v206","v207","v208","v209","v210","v211","v212","v213","v214","v215","v216","v217","v218","v219","v220","v221","v222","v223","v224","v225","v226","v227","v228","v229","v230","v231","v232","v233","v234","v235","v236","v237","v238","v239","v240","v241","v242","v243","v244","v245","v246","v247");
  asm volatile("" : "+v"(tid)); asm volatile("" : "+s"(b), "+s"(w), "+s"(pw));
  char* ws = GP(char, pw);
  const int l = tid & 63, n = l & 15, g = l >> 4;
  const int s = b & 7, R = b >> 3;
  const char* sb = ws + (u32)(O_ST + (4 * R + w) * 1536);
  char* pb = ws + (u32)(O_PART + ((s * 32 + R) * 72 + w * 18) * 1024);
  const u32 xl = lb + w * 19 * 1024;
  const u32 vx0 = lb + l * 16, vst = g * 384 + n * 24, vsw = lb + 151552 + (16 * w + n) * 8, vsr = lb + 151552 + n * 8;
  const u32 vxe = xl + 18 * 1024 + l * 16, vhw = lb + 77824 + w * 8192 + l * 16, vh4 = lb + 77824 + (8 + (w >> 1)) * 4096 + (w & 1) * 8 + l * 16;
  const char* pr = ws + (u32)(O_PART + R * 73728);
  const u32x4 rp = {(u32)(uintptr_t)pr, (u32)((uintptr_t)pr >> 32) & 0xffff, 7 * 32 * 73728 + 73728, 0x00020000};
  const char* fl = ws + (u32)(O_FLG + R * 2048);
  const char* ob = (const char*)out + (u32)(64 * R * 1152);
  asm volatile(
"s_nop 4\n"
"v_bfe_u32 v3, %[vl], 2, 6\n"
"s_mov_b32 m0, %[xl]\n"
"s_mov_b32 s27, 0x100000\n"
".Lf_%=:\n"
"global_load_dword v2, v3, %[c0] sc1\n"
"s_sub_u32 s27, s27, 1\n"
"s_cbranch_scc1 .Lfk_%=\n"
"s_waitcnt vmcnt(0)\n"
"v_cmp_ne_u32 vcc, 1, v2\n"
"s_cbranch_vccz .Lfk_%=\n"
"s_sleep 2\n"
"s_branch .Lf_%=\n"
".Lfk_%=:\n"
"s_waitcnt vmcnt(0)\n"
"global_load_lds_dwordx4 %[vl], s[22:23] offset:0 sc1\n"
"global_load_lds_dwordx4 %[vl], s[22:23] offset:1024 sc1\n"
"global_load_dwordx4 a[80:83], %[vl], s[20:21] offset:0\n"
"global_load_lds_dwordx4 %[vl], s[22:23] offset:2048 sc1\n"
"global_load_lds_dwordx4 %[vl], s[22:23] offset:3072 sc1\n"
"global_load_dwordx4 a[84:87], %[vl], s[20:21] offset:1024\n"
"global_load_dwordx4 a[88:91], %[vl], s[20:21] offset:2048\n"
"global_load_dwordx4 a[92:95], %[vl], s[20:21] offset:3072\n"
"s_add_u32 s20, s20, 8192\n"
"s_addc_u32 s21, s21, 0\n"
"global_load_dwordx4 a[96:99], %[vl], s[20:21] offset:-4096\n"
"global_load_dwordx4 a[100:103], %[vl], s[20:21] offset:-3072\n"
"global_load_dwordx4 a[104:107], %[vl], s[20:21] offset:-2048\n"
"global_load_dwordx4 a[108:111], %[vl], s[20:21] offset:-1024\n"
"global_load_dwordx4 a[112:115], %[vl], s[20:21] offset:0\n"
"global_load_dwordx4 a[116:119], %[vl], s[20:21] offset:1024\n"
"global_load_dwordx4 a[120:123], %[vl], s[20:21] offset:2048\n"
"global_load_dwordx4 v[232:235], %[vst], %[sb] sc1\n"
"global_load_dwordx2 v[236:237], %[vst], %[sb] offset:16 sc1\n"
"v_add_u32 v246, 0x10000, %[vx0]\n"
"v_add_u32 v247, 0x20000, %[vx0]\n"
"s_waitcnt vmcnt(16)\n"
"s_barrier\n"
"ds_read_b128 a[0:3], %[vx0] offset:0\n"
"ds_read_b128 a[4:7], %[vx0] offset:19456\n"
"ds_read_b128 a[8:11], %[vx0] offset:38912\n"
"ds_read_b128 a[12:15], %[vx0] offset:58368\n"
"s_waitcnt lgkmcnt(0)\n"
"v_mfma_f32_16x16x32_bf16 v[0:3], a[64:67], a[0:3], 0\n"
"s_waitcnt vmcnt(15)\n"
"s_barrier\n"
"ds_read_b128 a[16:19], %[vx0] offset:1024\n"
"v_mfma_f32_16x16x32_bf16 v[4:7], a[64:67], a[4:7], 0\n"
"ds_read_b128 a[20:23], %[vx0] offset:20480\n"
"v_mfma_f32_16x16x32_bf16 v[8:11], a[64:67], a[8:11], 0\n"
"ds_read_b128 a[24:27], %[vx0] offset:39936\n"
"v_mfma_f32_16x16x32_bf16 v[12:15], a[64:67], a[12:15], 0\n"
"ds_read_b128 a[28:31], %[vx0] offset:59392\n"
"v_mfma_f32_16x16x32_bf16 v[28:31], a[68:71], a[12:15], 0\n"
"v_mfma_f32_16x16x32_bf16 v[24:27], a[68:71], a[8:11], 0\n"
"v_mfma_f32_16x16x32_bf16 v[20:23], a[68:71], a[4:7], 0\n"
"v_mfma_f32_16x16x32_bf16 v[16:19], a[68:71], a[0:3], 0\n"
"v_mfma_f32_16x16x32_bf16 v[32:35], a[72:75], a[0:3], 0\n"
"v_mfma_f32_16x16x32_bf16 v[36:39], a[72:75], a[4:7], 0\n"
"v_mfma_f32_16x16x32_bf16 v[40:43], a[72:75], a[8:11], 0\n"
"v_mfma_f32_16x16x32_bf16 v[44:47], a[72:75], a[12:15], 0\n"
"s_add_u32 m0, m0, 4096\n"
"s_add_u32 s22, s22, 4096\n"
"s_addc_u32 s23, s23, 0\n"
"global_load_lds_dwordx4 %[vl], s[22:23] offset:0 sc1\n"
"v_mfma_f32_16x16x32_bf16 v[60:63], a[76:79], a[12:15], 0\n"
"v_mfma_f32_16x16x32_bf16 v[56:59], a[76:79], a[8:11], 0\n"
"v_mfma_f32_16x16x32_bf16 v[52:55], a[76:79], a[4:7], 0\n"
"global_load_lds_dwordx4 %[vl], s[22:23] offset:1024 sc1\n"
"v_mfma_f32_16x16x32_bf16 v[48:51], a[76:79], a[0:3], 0\n"
"s_waitcnt vmcnt(16)\n"
"v_mfma_f32_16x16x32_bf16 v[64:67], a[80:83], a[0:3], 0\n"
"v_mfma_f32_16x16x32_bf16 v[68:71], a[80:83], a[4:7], 0\n"
"global_load_lds_dwordx4 %[vl], s[22:23] offset:2048 sc1\n"
"v_mfma_f32_16x16x32_bf16 v[72:75], a[80:83], a[8:11], 0\n"
"v_mfma_f32_16x16x32_bf16 v[76:79], a[80:83], a[12:15], 0\n"
"s_waitcnt lgkmcnt(0)\n"
"s_waitcnt vmcnt(14)\n"
"v_mfma_f32_16x16x32_bf16 v[0:3], a[84:87], a[16:19], v[0:3]\n"
"global_load_dwordx4 a[124:127], %[vl], s[20:21] offset:3072\n"
"s_barrier\n"
"ds_read_b128 a[0:3], %[vx0] offset:2048\n"
"v_mfma_f32_16x16x32_bf16 v[4:7], a[84:87], a[20:23], v[4:7]\n"
"ds_read_b128 a[4:7], %[vx0] offset:21504\n"
"v_mfma_f32_16x16x32_bf16 v[8:11], a[84:87], a[24:27], v[8:11]\n"
"ds_read_b128 a[8:11], %[vx0] offset:40960\n"
"v_mfma_f32_16x16x32_bf16 v[12:15], a[84:87], a[28:31], v[12:15]\n"
"s_add_u32 s20, s20, 8192\n"
"s_addc_u32 s21, s21, 0\n"
"global_load_dwordx4 a[128:131], %[vl], s[20:21] offset:-4096\n"
"ds_read_b128 a[12:15], %[vx0] offset:60416\n"
"s_waitcnt vmcnt(15)\n"
"v_mfma_f32_16x16x32_bf16 v[28:31], a[88:91], a[28:31], v[28:31]\n"
"v_mfma_f32_16x16x32_bf16 v[24:27], a[88:91], a[24:27], v[24:27]\n"
"v_mfma_f32_16x16x32_bf16 v[20:23], a[88:91], a[20:23], v[20:23]\n"
"global_load_dwordx4 a[132:135], %[vl], s[20:21] offset:-3072\n"
"v_mfma_f32_16x16x32_bf16 v[16:19], a[88:91], a[16:19], v[16:19]\n"
"s_waitcnt vmcnt(15)\n"
"v_mfma_f32_16x16x32_bf16 v[32:35], a[92:95], a[16:19], v[32:35]\n"
"v_mfma_f32_16x16x32_bf16 v[36:39], a[92:95], a[20:23], v[36:39]\n"
"global_load_dwordx4 a[136:139], %[vl], s[20:21] offset:-2048\n"
"v_mfma_f32_16x16x32_bf16 v[40:43], a[92:95], a[24:27], v[40:43]\n"
"v_mfma_f32_16x16x32_bf16 v[44:47], a[92:95], a[28:31], v[44:47]\n"
"s_waitcnt vmcnt(15)\n"
"v_mfma_f32_16x16x32_bf16 v[60:63], a[96:99], a[28:31], v[60:63]\n"
"global_load_dwordx4 a[140:143], %[vl], s[20:21] offset:-1024\n"
"v_mfma_f32_16x16x32_bf16 v[56:59], a[96:99], a[24:27], v[56:59]\n"
"v_mfma_f32_16x16x32_bf16 v[52:55], a[96:99], a[20:23], v[52:55]\n"
"v_mfma_f32_16x16x32_bf16 v[48:51], a[96:99], a[16:19], v[48:51]\n"
"global_load_dwordx4 a[144:147], %[vl], s[20:21] offset:0\n"
"s_waitcnt vmcnt(16)\n"
"v_mfma_f32_16x16x32_bf16 v[64:67], a[100:103], a[16:19], v[64:67]\n"
"v_mfma_f32_16x16x32_bf16 v[68:71], a[100:103], a[20:23], v[68:71]\n"
"v_mfma_f32_16x16x32_bf16 v[72:75], a[100:103], a[24:27], v[72:75]\n"
"global_load_dwordx4 a[148:151], %[vl], s[20:21] offset:1024\n"
"v_mfma_f32_16x16x32_bf16 v[76:79], a[100:103], a[28:31], v[76:79]\n"
"s_waitcnt lgkmcnt(0)\n"
"s_waitcnt vmcnt(16)\n"
"v_mfma_f32_16x16x32_bf16 v[0:3], a[104:107], a[0:3], v[0:3]\n"
"ds_read_b128 a[16:19], %[vx0] offset:3072\n"
"v_mfma_f32_16x16x32_bf16 v[4:7], a[104:107], a[4:7], v[4:7]\n"
"global_load_dwordx4 a[152:155], %[vl], s[20:21] offset:2048\n"
"ds_read_b128 a[20:23], %[vx0] offset:22528\n"
"v_mfma_f32_16x16x32_bf16 v[8:11], a[104:107], a[8:11], v[8:11]\n"
"ds_read_b128 a[24:27], %[vx0] offset:41984\n"
"v_mfma_f32_16x16x32_bf16 v[12:15], a[104:107], a[12:15], v[12:15]\n"
"ds_read_b128 a[28:31], %[vx0] offset:61440\n"
"s_waitcnt vmcnt(16)\n"
"v_mfma_f32_16x16x32_bf16 v[28:31], a[108:111], a[12:15], v[28:31]\n"
"global_load_dwordx4 a[156:159], %[vl], s[20:21] offset:3072\n"
"v_mfma_f32_16x16x32_bf16 v[24:27], a[108:111], a[8:11], v[24:27]\n"
"v_mfma_f32_16x16x32_bf16 v[20:23], a[108:111], a[4:7], v[20:23]\n"
"v_mfma_f32_16x16x32_bf16 v[16:19], a[108:111], a[0:3], v[16:19]\n"
"s_add_u32 s20, s20, 8192\n"
"s_addc_u32 s21, s21, 0\n"
"global_load_dwordx4 a[160:163], %[vl], s[20:21] offset:-4096\n"
"s_waitcnt vmcnt(17)\n"
"v_mfma_f32_16x16x32_bf16 v[32:35], a[112:115], a[0:3], v[32:35]\n"
"v_mfma_f32_16x16x32_bf16 v[36:39], a[112:115], a[4:7], v[36:39]\n"
"v_mfma_f32_16x16x32_bf16 v[40:43], a[112:115], a[8:11], v[40:43]\n"
"v_mfma_f32_16x16x32_bf16 v[44:47], a[112:115], a[12:15], v[44:47]\n"
"global_load_dwordx4 a[164:167], %[vl], s[20:21] offset:-3072\n"
"s_waitcnt vmcnt(17)\n"
"v_mfma_f32_16x16x32_bf16 v[60:63], a[116:119], a[12:15], v[60:63]\n"
"v_mfma_f32_16x16x32_bf16 v[56:59], a[116:119], a[8:11], v[56:59]\n"
"v_mfma_f32_16x16x32_bf16 v[52:55], a[116:119], a[4:7], v[52:55]\n"
"v_mfma_f32_16x16x32_bf16 v[48:51], a[116:119], a[0:3], v[48:51]\n"
"global_load_dwordx4 a[168:171], %[vl], s[20:21] offset:-2048\n"
"s_waitcnt vmcnt(17)\n"
"v_mfma_f32_16x16x32_bf16 v[64:67], a[120:123], a[0:3], v[64:67]\n"
"v_mfma_f32_16x16x32_bf16 v[68:71], a[120:123], a[4:7], v[68:71]\n"
"v_mfma_f32_16x16x32_bf16 v[72:75], a[120:123], a[8:11], v[72:75]\n"
"v_mfma_f32_16x16x32_bf16 v[76:79], a[120:123], a[12:15], v[76:79]\n"
"global_load_dwordx4 a[172:175], %[vl], s[20:21] offset:-1024\n"
"s_waitcnt lgkmcnt(0)\n"
"s_waitcnt vmcnt(12)\n"
"v_mfma_f32_16x16x32_bf16 v[0:3], a[124:127], a[16:19], v[0:3]\n"
"s_barrier\n"
"ds_read_b128 a[0:3], %[vx0] offset:4096\n"
"v_mfma_f32_16x16x32_bf16 v[4:7], a[124:127], a[20:23], v[4:7]\n"
"ds_read_b128 a[4:7], %[vx0] offset:23552\n"
"v_mfma_f32_16x16x32_bf16 v[8:11], a[124:127], a[24:27], v[8:11]\n"
"ds_read_b128 a[8:11], %[vx0] offset:43008\n"
"v_mfma_f32_16x16x32_bf16 v[12:15], a[124:127], a[28:31], v[12:15]\n"
"global_load_dwordx4 a[176:179], %[vl], s[20:21] offset:0\n"
"ds_read_b128 a[12:15], %[vx0] offset:62464\n"
"s_waitcnt vmcnt(12)\n"
"v_mfma_f32_16x16x32_bf16 v[28:31], a[128:131], a[28:31], v[28:31]\n"
"v_mfma_f32_16x16x32_bf16 v[24:27], a[128:131], a[24:27], v[24:27]\n"
"v_mfma_f32_16x16x32_bf16 v[20:23], a[128:131], a[20:23], v[20:23]\n"
"v_mfma_f32_16x16x32_bf16 v[16:19], a[128:131], a[16:19], v[16:19]\n"
"global_load_dwordx4 a[180:183], %[vl], s[20:21] offset:1024\n"
"s_waitcnt vmcnt(12)\n"
"v_mfma_f32_16x16x32_bf16 v[32:35], a[132:135], a[16:19], v[32:35]\n"
"v_mfma_f32_16x16x32_bf16 v[36:39], a[132:135], a[20:23], v[36:39]\n"
"v_mfma_f32_16x16x32_bf16 v[40:43], a[132:135], a[24:27], v[40:43]\n"
"v_mfma_f32_16x16x32_bf16 v[44:47], a[132:135], a[28:31], v[44:47]\n"
"global_load_lds_dwordx4 %[vl], s[22:23] offset:3072 sc1\n"
"s_waitcnt vmcnt(12)\n"
"v_mfma_f32_16x16x32_bf16 v[60:63], a[136:139], a[28:31], v[60:63]\n"
"v_mfma_f32_16x16x32_bf16 v[56:59], a[136:139], a[24:27], v[56:59]\n"
"v_mfma_f32_16x16x32_bf16 v[52:55], a[136:139], a[20:23], v[52:55]\n"
"s_add_u32 m0, m0, 4096\n"
"s_add_u32 s22, s22, 4096\n"
"s_addc_u32 s23, s23, 0\n"
"global_load_lds_dwordx4 %[vl], s[22:23] offset:0 sc1\n"
"v_mfma_f32_16x16x32_bf16 v[48:51], a[136:139], a[16:19], v[48:51]\n"
"s_waitcnt vmcnt(12)\n"
"v_mfma_f32_16x16x32_bf16 v[64:67], a[140:143], a[16:19], v[64:67]\n"
"v_mfma_f32_16x16x32_bf16 v[68:71], a[140:143], a[20:23], v[68:71]\n"
"global_load_lds_dwordx4 %[vl], s[22:23] offset:1024 sc1\n"
"v_mfma_f32_16x16x32_bf16 v[72:75], a[140:143], a[24:27], v[72:75]\n"
"v_mfma_f32_16x16x32_bf16 v[76:79], a[140:143], a[28:31], v[76:79]\n"
"s_waitcnt lgkmcnt(0)\n"
"s_waitcnt vmcnt(12)\n"
"v_mfma_f32_16x16x32_bf16 v[0:3], a[144:147], a[0:3], v[0:3]\n"
"global_load_lds_dwordx4 %[vl], s[22:23] offset:2048 sc1\n"
"ds_read_b128 a[16:19], %[vx0] offset:5120\n"
"v_mfma_f32_16x16x32_bf16 v[4:7], a[144:147], a[4:7], v[4:7]\n"
"ds_read_b128 a[20:23], %[vx0] offset:24576\n"
"v_mfma_f32_16x16x32_bf16 v[8:11], a[144:147], a[8:11], v[8:11]\n"
"ds_read_b128 a[24:27], %[vx0] offset:44032\n"
"v_mfma_f32_16x16x32_bf16 v[12:15], a[144:147], a[12:15], v[12:15]\n"
"global_load_dwordx4 a[184:187], %[vl], s[20:21] offset:2048\n"
"ds_read_b128 a[28:31], %[vx0] offset:63488\n"
"s_waitcnt vmcnt(13)\n"
"v_mfma_f32_16x16x32_bf16 v[28:31], a[148:151], a[12:15], v[28:31]\n"
"v_mfma_f32_16x16x32_bf16 v[24:27], a[148:151], a[8:11], v[24:27]\n"
"v_mfma_f32_16x16x32_bf16 v[20:23], a[148:151], a[4:7], v[20:23]\n"
"global_load_dwordx4 a[188:191], %[vl], s[20:21] offset:3072\n"
"v_mfma_f32_16x16x32_bf16 v[16:19], a[148:151], a[0:3], v[16:19]\n"
"s_waitcnt vmcnt(13)\n"
"v_mfma_f32_16x16x32_bf16 v[32:35], a[152:155], a[0:3], v[32:35]\n"
"v_mfma_f32_16x16x32_bf16 v[36:39], a[152:155], a[4:7], v[36:39]\n"
"s_add_u32 s20, s20, 8192\n"
"s_addc_u32 s21, s21, 0\n"
"global_load_dwordx4 a[64:67], %[vl], s[20:21] offset:-4096\n"
"v_mfma_f32_16x16x32_bf16 v[40:43], a[152:155], a[8:11], v[40:43]\n"
"v_mfma_f32_16x16x32_bf16 v[44:47], a[152:155], a[12:15], v[44:47]\n"
"s_waitcnt vmcnt(13)\n"
"v_mfma_f32_16x16x32_bf16 v[60:63], a[156:159], a[12:15], v[60:63]\n"
"global_load_dwordx4 a[68:71], %[vl], s[20:21] offset:-3072\n"
"v_mfma_f32_16x16x32_bf16 v[56:59], a[156:159], a[8:11], v[56:59]\n"
"v_mfma_f32_16x16x32_bf16 v[52:55], a[156:159], a[4:7], v[52:55]\n"
"v_mfma_f32_16x16x32_bf16 v[48:51], a[156:159], a[0:3], v[48:51]\n"
"global_load_dwordx4 a[72:75], %[vl], s[20:21] offset:-2048\n"
"s_waitcnt vmcnt(14)\n"
"v_mfma_f32_16x16x32_bf16 v[64:67], a[160:163], a[0:3], v[64:67]\n"
"v_mfma_f32_16x16x32_bf16 v[68:71], a[160:163], a[4:7], v[68:71]\n"
"v_mfma_f32_16x16x32_bf16 v[72:75], a[160:163], a[8:11], v[72:75]\n"
"global_load_dwordx4 a[76:79], %[vl], s[20:21] offset:-1024\n"
"v_mfma_f32_16x16x32_bf16 v[76:79], a[160:163], a[12:15], v[76:79]\n"
"s_waitcnt lgkmcnt(0)\n"
"s_waitcnt vmcnt(14)\n"
"v_mfma_f32_16x16x32_bf16 v[0:3], a[164:167], a[16:19], v[0:3]\n"
"ds_read_b128 a[0:3], %[vx0] offset:6144\n"
"v_mfma_f32_16x16x32_bf16 v[4:7], a[164:167], a[20:23], v[4:7]\n"
"global_load_dwordx4 a[80:83], %[vl], s[20:21] offset:0\n"
"ds_read_b128 a[4:7], %[vx0] offset:25600\n"
"v_mfma_f32_16x16x32_bf16 v[8:11], a[164:167], a[24:27], v[8:11]\n"
"ds_read_b128 a[8:11], %[vx0] offset:45056\n"
"v_mfma_f32_16x16x32_bf16 v[12:15], a[164:167], a[28:31], v[12:15]\n"
"ds_read_b128 a[12:15], %[vx0] offset:64512\n"
"v_add_f32 v238, v232, v234\n"
"v_add_f32 v240, v233, v235\n"
"v_add_f32 v238, v238, v236\n"
"s_waitcnt vmcnt(14)\n"
"v_mfma_f32_16x16x32_bf16 v[28:31], a[168:171], a[28:31], v[28:31]\n"
"global_load_dwordx4 a[84:87], %[vl], s[20:21] offset:1024\n"
"v_add_f32 v240, v240, v237\n"
"v_mov_b32 v239, v238\n"
"v_mfma_f32_16x16x32_bf16 v[24:27], a[168:171], a[24:27], v[24:27]\n"
"v_mov_b32 v241, v240\n"
"v_mfma_f32_16x16x32_bf16 v[20:23], a[168:171], a[20:23], v[20:23]\n"
"v_permlane16_swap_b32 v238, v239\n"
"v_permlane16_swap_b32 v240, v241\n"
"v_add_f32 v238, v238, v239\n"
"v_mfma_f32_16x16x32_bf16 v[16:19], a[168:171], a[16:19], v[16:19]\n"
"global_load_dwordx4 a[88:91], %[vl], s[20:21] offset:2048\n"
"v_add_f32 v240, v240, v241\n"
"s_waitcnt vmcnt(15)\n"
"v_mfma_f32_16x16x32_bf16 v[32:35], a[172:175], a[16:19], v[32:35]\n"
"v_mov_b32 v239, v238\n"
"v_mov_b32 v241, v240\n"
"v_mfma_f32_16x16x32_bf16 v[36:39], a[172:175], a[20:23], v[36:39]\n"
"v_permlane32_swap_b32 v238, v239\n"
"v_permlane32_swap_b32 v240, v241\n"
"v_mfma_f32_16x16x32_bf16 v[40:43], a[172:175], a[24:27], v[40:43]\n"
"global_load_dwordx4 a[92:95], %[vl], s[20:21] offset:3072\n"
"v_add_f32 v238, v238, v239\n"
"v_add_f32 v240, v240, v241\n"
"v_mfma_f32_16x16x32_bf16 v[44:47], a[172:175], a[28:31], v[44:47]\n"
"v_mul_f32 v238, 0x3ae38e39, v238\n"
"v_mul_f32 v240, 0x3ae38e39, v240\n"
"s_waitcnt vmcnt(15)\n"
"v_mfma_f32_16x16x32_bf16 v[60:63], a[176:179], a[28:31], v[60:63]\n"
"v_fma_f32 v240, -v238, v238, v240\n"
"v_max_f32 v240, 0, v240\n"
"v_add_f32 v239, 0x3727c5ac, v240\n"
"v_mfma_f32_16x16x32_bf16 v[56:59], a[176:179], a[24:27], v[56:59]\n"
"s_add_u32 s20, s20, 8192\n"
"s_addc_u32 s21, s21, 0\n"
"global_load_dwordx4 a[96:99], %[vl], s[20:21] offset:-4096\n"
"v_mfma_f32_16x16x32_bf16 v[52:55], a[176:179], a[20:23], v[52:55]\n"
"v_sqrt_f32 v232, v239\n"
"v_cvt_pk_bf16_f32 v234, -v238, -v238\n"
"v_mfma_f32_16x16x32_bf16 v[48:51], a[176:179], a[16:19], v[48:51]\n"
"v_rcp_f32 v233, v232\n"
"v_cvt_pk_bf16_f32 v236, v232, v232\n"
"s_waitcnt vmcnt(15)\n"
"v_mfma_f32_16x16x32_bf16 v[64:67], a[180:183], a[16:19], v[64:67]\n"
"global_load_dwordx4 a[100:103], %[vl], s[20:21] offset:-3072\n"
"v_mul_f32 v233, 0xbfb8aa3b, v233\n"
"v_and_b32 v239, 0xffff0000, v234\n"
"v_mfma_f32_16x16x32_bf16 v[68:71], a[180:183], a[20:23], v[68:71]\n"
"v_and_b32 v240, 0xffff0000, v236\n"
"v_add_f32 v239, v238, v239\n"
"v_mfma_f32_16x16x32_bf16 v[72:75], a[180:183], a[24:27], v[72:75]\n"
"v_sub_f32 v240, v232, v240\n"
"v_cvt_pk_bf16_f32 v235, -v239, -v239\n"
"v_cvt_pk_bf16_f32 v237, v240, v240\n"
"v_mfma_f32_16x16x32_bf16 v[76:79], a[180:183], a[28:31], v[76:79]\n"
"global_load_dwordx4 a[104:107], %[vl], s[20:21] offset:-2048\n"
"ds_write_b64 %[vsw], v[232:233]\n"
"s_waitcnt lgkmcnt(1)\n"
"s_waitcnt vmcnt(12)\n"
"v_mfma_f32_16x16x32_bf16 v[0:3], a[184:187], a[0:3], v[0:3]\n"
"s_barrier\n"
"ds_read_b128 a[16:19], %[vx0] offset:7168\n"
"v_mfma_f32_16x16x32_bf16 v[4:7], a[184:187], a[4:7], v[4:7]\n"
"ds_read_b128 a[20:23], %[vx0] offset:26624\n"
"v_mfma_f32_16x16x32_bf16 v[8:11], a[184:187], a[8:11], v[8:11]\n"
"ds_read_b128 a[24:27], %[vx0] offset:46080\n"
"v_mfma_f32_16x16x32_bf16 v[12:15], a[184:187], a[12:15], v[12:15]\n"
"global_load_dwordx4 a[108:111], %[vl], s[20:21] offset:-1024\n"
"ds_read_b128 a[28:31], v246 offset:0\n"
"v_lshrrev_b32 v241, 8, %[vl]\n"
"s_waitcnt vmcnt(12)\n"
"v_mfma_f32_16x16x32_bf16 v[28:31], a[188:191], a[12:15], v[28:31]\n"
"v_cmp_eq_u32 vcc, 1, v241\n"
"s_nop 1\n"
"v_cndmask_b32 v234, v234, v235, vcc\n"
"v_mfma_f32_16x16x32_bf16 v[24:27], a[188:191], a[8:11], v[24:27]\n"
"v_cmp_eq_u32 vcc, 3, v241\n"
"s_nop 1\n"
"v_cndmask_b32 v238, v234, v236, vcc\n"
"v_mfma_f32_16x16x32_bf16 v[20:23], a[188:191], a[4:7], v[20:23]\n"
"v_cndmask_b32 v241, v237, 0, vcc\n"
"v_mov_b32 v239, v238\n"
"v_mov_b32 v240, v238\n"
"v_mfma_f32_16x16x32_bf16 v[16:19], a[188:191], a[0:3], v[16:19]\n"
"global_load_dwordx4 a[112:115], %[vl], s[20:21] offset:0\n"
"ds_write_b128 %[vxe], v[238:241]\n"
"s_waitcnt vmcnt(12)\n"
"v_mfma_f32_16x16x32_bf16 v[32:35], a[64:67], a[0:3], v[32:35]\n"
"v_mfma_f32_16x16x32_bf16 v[36:39], a[64:67], a[4:7], v[36:39]\n"
"v_mfma_f32_16x16x32_bf16 v[40:43], a[64:67], a[8:11], v[40:43]\n"
"v_mfma_f32_16x16x32_bf16 v[44:47], a[64:67], a[12:15], v[44:47]\n"
"global_load_dwordx4 a[116:119], %[vl], s[20:21] offset:1024\n"
"s_waitcnt vmcnt(12)\n"
"v_mfma_f32_16x16x32_bf16 v[60:63], a[68:71], a[12:15], v[60:63]\n"
"v_mfma_f32_16x16x32_bf16 v[56:59], a[68:71], a[8:11], v[56:59]\n"
"v_mfma_f32_16x16x32_bf16 v[52:55], a[68:71], a[4:7], v[52:55]\n"
"v_mfma_f32_16x16x32_bf16 v[48:51], a[68:71], a[0:3], v[48:51]\n"
"global_load_dwordx4 a[120:123], %[vl], s[20:21] offset:2048\n"
"s_waitcnt vmcnt(12)\n"
"v_mfma_f32_16x16x32_bf16 v[64:67], a[72:75], a[0:3], v[64:67]\n"
"v_mfma_f32_16x16x32_bf16 v[68:71], a[72:75], a[4:7], v[68:71]\n"
"v_mfma_f32_16x16x32_bf16 v[72:75], a[72:75], a[8:11], v[72:75]\n"
"v_mfma_f32_16x16x32_bf16 v[76:79], a[72:75], a[12:15], v[76:79]\n"
"global_load_dwordx4 a[124:127], %[vl], s[20:21] offset:3072\n"
"s_waitcnt lgkmcnt(1)\n"
"s_waitcnt vmcnt(12)\n"
"v_mfma_f32_16x16x32_bf16 v[0:3], a[76:79], a[16:19], v[0:3]\n"
"ds_read_b128 a[0:3], %[vx0] offset:8192\n"
"v_mfma_f32_16x16x32_bf16 v[4:7], a[76:79], a[20:23], v[4:7]\n"
"ds_read_b128 a[4:7], %[vx0] offset:27648\n"
"v_mfma_f32_16x16x32_bf16 v[8:11], a[76:79], a[24:27], v[8:11]\n"
"ds_read_b128 a[8:11], %[vx0] offset:47104\n"
"v_mfma_f32_16x16x32_bf16 v[12:15], a[76:79], a[28:31], v[12:15]\n"
"s_add_u32 s20, s20, 8192\n"
"s_addc_u32 s21, s21, 0\n"
"global_load_dwordx4 a[128:131], %[vl], s[20:21] offset:-4096\n"
"ds_read_b128 a[12:15], v246 offset:1024\n"
"s_waitcnt vmcnt(12)\n"
"v_mfma_f32_16x16x32_bf16 v[28:31], a[80:83], a[28:31], v[28:31]\n"
"v_mfma_f32_16x16x32_bf16 v[24:27], a[80:83], a[24:27], v[24:27]\n"
"v_mfma_f32_16x16x32_bf16 v[20:23], a[80:83], a[20:23], v[20:23]\n"
"v_mfma_f32_16x16x32_bf16 v[16:19], a[80:83], a[16:19], v[16:19]\n"
"global_load_dwordx4 a[132:135], %[vl], s[20:21] offset:-3072\n"
"s_waitcnt vmcnt(12)\n"
"v_mfma_f32_16x16x32_bf16 v[32:35], a[84:87], a[16:19], v[32:35]\n"
"v_mfma_f32_16x16x32_bf16 v[36:39], a[84:87], a[20:23], v[36:39]\n"
"v_mfma_f32_16x16x32_bf16 v[40:43], a[84:87], a[24:27], v[40:43]\n"
"v_mfma_f32_16x16x32_bf16 v[44:47], a[84:87], a[28:31], v[44:47]\n"
"global_load_lds_dwordx4 %[vl], s[22:23] offset:3072 sc1\n"
"s_waitcnt vmcnt(12)\n"
"v_mfma_f32_16x16x32_bf16 v[60:63], a[88:91], a[28:31], v[60:63]\n"
"v_mfma_f32_16x16x32_bf16 v[56:59], a[88:91], a[24:27], v[56:59]\n"
"v_mfma_f32_16x16x32_bf16 v[52:55], a[88:91], a[20:23], v[52:55]\n"
"s_add_u32 m0, m0, 4096\n"
"s_add_u32 s22, s22, 4096\n"
"s_addc_u32 s23, s23, 0\n"
"global_load_lds_dwordx4 %[vl], s[22:23] offset:0 sc1\n"
"v_mfma_f32_16x16x32_bf16 v[48:51], a[88:91], a[16:19], v[48:51]\n"
"s_waitcnt vmcnt(12)\n"
"v_mfma_f32_16x16x32_bf16 v[64:67], a[92:95], a[16:19], v[64:67]\n"
"v_mfma_f32_16x16x32_bf16 v[68:71], a[92:95], a[20:23], v[68:71]\n"
"global_load_lds_dwordx4 %[vl], s[22:23] offset:1024 sc1\n"
"v_mfma_f32_16x16x32_bf16 v[72:75], a[92:95], a[24:27], v[72:75]\n"
"v_mfma_f32_16x16x32_bf16 v[76:79], a[92:95], a[28:31], v[76:79]\n"
"s_waitcnt lgkmcnt(0)\n"
"s_waitcnt vmcnt(12)\n"
"v_mfma_f32_16x16x32_bf16 v[0:3], a[96:99], a[0:3], v[0:3]\n"
"global_load_lds_dwordx4 %[vl], s[22:23] offset:2048 sc1\n"
"ds_read_b128 a[16:19], %[vx0] offset:9216\n"
"v_mfma_f32_16x16x32_bf16 v[4:7], a[96:99], a[4:7], v[4:7]\n"
"ds_read_b128 a[20:23], %[vx0] offset:28672\n"
"v_mfma_f32_16x16x32_bf16 v[8:11], a[96:99], a[8:11], v[8:11]\n"
"ds_read_b128 a[24:27], %[vx0] offset:48128\n"
"v_mfma_f32_16x16x32_bf16 v[12:15], a[96:99], a[12:15], v[12:15]\n"
"global_load_dwordx4 a[136:139], %[vl], s[20:21] offset:-2048\n"
"ds_read_b128 a[28:31], v246 offset:2048\n"
"s_waitcnt vmcnt(13)\n"
"v_mfma_f32_16x16x32_bf16 v[28:31], a[100:103], a[12:15], v[28:31]\n"
"v_mfma_f32_16x16x32_bf16 v[24:27], a[100:103], a[8:11], v[24:27]\n"
"v_mfma_f32_16x16x32_bf16 v[20:23], a[100:103], a[4:7], v[20:23]\n"
"global_load_dwordx4 a[140:143], %[vl], s[20:21] offset:-1024\n"
"v_mfma_f32_16x16x32_bf16 v[16:19], a[100:103], a[0:3], v[16:19]\n"
"s_waitcnt vmcnt(13)\n"
"v_mfma_f32_16x16x32_bf16 v[32:35], a[104:107], a[0:3], v[32:35]\n"
"v_mfma_f32_16x16x32_bf16 v[36:39], a[104:107], a[4:7], v[36:39]\n"
"global_load_dwordx4 a[144:147], %[vl], s[20:21] offset:0\n"
"v_mfma_f32_16x16x32_bf16 v[40:43], a[104:107], a[8:11], v[40:43]\n"
"v_mfma_f32_16x16x32_bf16 v[44:47], a[104:107], a[12:15], v[44:47]\n"
"s_waitcnt vmcnt(13)\n"
"v_mfma_f32_16x16x32_bf16 v[60:63], a[108:111], a[12:15], v[60:63]\n"
"global_load_dwordx4 a[148:151], %[vl], s[20:21] offset:1024\n"
"v_mfma_f32_16x16x32_bf16 v[56:59], a[108:111], a[8:11], v[56:59]\n"
"v_mfma_f32_16x16x32_bf16 v[52:55], a[108:111], a[4:7], v[52:55]\n"
"v_mfma_f32_16x16x32_bf16 v[48:51], a[108:111], a[0:3], v[48:51]\n"
"global_load_dwordx4 a[152:155], %[vl], s[20:21] offset:2048\n"
"s_waitcnt vmcnt(14)\n"
"v_mfma_f32_16x16x32_bf16 v[64:67], a[112:115], a[0:3], v[64:67]\n"
"v_mfma_f32_16x16x32_bf16 v[68:71], a[112:115], a[4:7], v[68:71]\n"
"v_mfma_f32_16x16x32_bf16 v[72:75], a[112:115], a[8:11], v[72:75]\n"
"global_load_dwordx4 a[156:159], %[vl], s[20:21] offset:3072\n"
"v_mfma_f32_16x16x32_bf16 v[76:79], a[112:115], a[12:15], v[76:79]\n"
"s_waitcnt lgkmcnt(0)\n"
"s_waitcnt vmcnt(14)\n"
"v_mfma_f32_16x16x32_bf16 v[0:3], a[116:119], a[16:19], v[0:3]\n"
"ds_read_b128 a[0:3], %[vx0] offset:10240\n"
"v_mfma_f32_16x16x32_bf16 v[4:7], a[116:119], a[20:23], v[4:7]\n"
"s_add_u32 s20, s20, 8192\n"
"s_addc_u32 s21, s21, 0\n"
"global_load_dwordx4 a[160:163], %[vl], s[20:21] offset:-4096\n"
"ds_read_b128 a[4:7], %[vx0] offset:29696\n"
"v_mfma_f32_16x16x32_bf16 v[8:11], a[116:119], a[24:27], v[8:11]\n"
"ds_read_b128 a[8:11], %[vx0] offset:49152\n"
"v_mfma_f32_16x16x32_bf16 v[12:15], a[116:119], a[28:31], v[12:15]\n"
"ds_read_b128 a[12:15], v246 offset:3072\n"
"s_waitcnt vmcnt(14)\n"
"v_mfma_f32_16x16x32_bf16 v[28:31], a[120:123], a[28:31], v[28:31]\n"
"global_load_dwordx4 a[164:167], %[vl], s[20:21] offset:-3072\n"
"v_mfma_f32_16x16x32_bf16 v[24:27], a[120:123], a[24:27], v[24:27]\n"
"v_mfma_f32_16x16x32_bf16 v[20:23], a[120:123], a[20:23], v[20:23]\n"
"v_mfma_f32_16x16x32_bf16 v[16:19], a[120:123], a[16:19], v[16:19]\n"
"global_load_dwordx4 a[168:171], %[vl], s[20:21] offset:-2048\n"
"s_waitcnt vmcnt(15)\n"
"v_mfma_f32_16x16x32_bf16 v[32:35], a[124:127], a[16:19], v[32:35]\n"
"v_mfma_f32_16x16x32_bf16 v[36:39], a[124:127], a[20:23], v[36:39]\n"
"v_mfma_f32_16x16x32_bf16 v[40:43], a[124:127], a[24:27], v[40:43]\n"
"global_load_dwordx4 a[172:175], %[vl], s[20:21] offset:-1024\n"
"v_mfma_f32_16x16x32_bf16 v[44:47], a[124:127], a[28:31], v[44:47]\n"
"s_waitcnt vmcnt(15)\n"
"v_mfma_f32_16x16x32_bf16 v[60:63], a[128:131], a[28:31], v[60:63]\n"
"v_mfma_f32_16x16x32_bf16 v[56:59], a[128:131], a[24:27], v[56:59]\n"
"global_load_dwordx4 a[176:179], %[vl], s[20:21] offset:0\n"
"v_mfma_f32_16x16x32_bf16 v[52:55], a[128:131], a[20:23], v[52:55]\n"
"v_mfma_f32_16x16x32_bf16 v[48:51], a[128:131], a[16:19], v[48:51]\n"
"s_waitcnt vmcnt(15)\n"
"v_mfma_f32_16x16x32_bf16 v[64:67], a[132:135], a[16:19], v[64:67]\n"
"global_load_dwordx4 a[180:183], %[vl], s[20:21] offset:1024\n"
"v_mfma_f32_16x16x32_bf16 v[68:71], a[132:135], a[20:23], v[68:71]\n"
"v_mfma_f32_16x16x32_bf16 v[72:75], a[132:135], a[24:27], v[72:75]\n"
"v_mfma_f32_16x16x32_bf16 v[76:79], a[132:135], a[28:31], v[76:79]\n"
"global_load_dwordx4 a[184:187], %[vl], s[20:21] offset:2048\n"
"s_waitcnt lgkmcnt(0)\n"
"s_waitcnt vmcnt(12)\n"
"v_mfma_f32_16x16x32_bf16 v[0:3], a[136:139], a[0:3], v[0:3]\n"
"s_barrier\n"
"ds_read_b128 a[16:19], %[vx0] offset:11264\n"
"v_mfma_f32_16x16x32_bf16 v[4:7], a[136:139], a[4:7], v[4:7]\n"
"ds_read_b128 a[20:23], %[vx0] offset:30720\n"
"v_mfma_f32_16x16x32_bf16 v[8:11], a[136:139], a[8:11], v[8:11]\n"
"ds_read_b128 a[24:27], %[vx0] offset:50176\n"
"v_mfma_f32_16x16x32_bf16 v[12:15], a[136:139], a[12:15], v[12:15]\n"
"global_load_dwordx4 a[188:191], %[vl], s[20:21] offset:3072\n"
"ds_read_b128 a[28:31], v246 offset:4096\n"
"s_waitcnt vmcnt(12)\n"
"v_mfma_f32_16x16x32_bf16 v[28:31], a[140:143], a[12:15], v[28:31]\n"
"ds_read_b64 v[224:225], %[vsr] offset:0\n"
"v_mfma_f32_16x16x32_bf16 v[24:27], a[140:143], a[8:11], v[24:27]\n"
"ds_read_b64 v[226:227], %[vsr] offset:128\n"
"v_mfma_f32_16x16x32_bf16 v[20:23], a[140:143], a[4:7], v[20:23]\n"
"ds_read_b64 v[228:229], %[vsr] offset:256\n"
"v_mfma_f32_16x16x32_bf16 v[16:19], a[140:143], a[0:3], v[16:19]\n"
"s_add_u32 s20, s20, 8192\n"
"s_addc_u32 s21, s21, 0\n"
"global_load_dwordx4 a[64:67], %[vl], s[20:21] offset:-4096\n"
"ds_read_b64 v[230:231], %[vsr] offset:384\n"
"s_waitcnt vmcnt(12)\n"
"v_mfma_f32_16x16x32_bf16 v[32:35], a[144:147], a[0:3], v[32:35]\n"
"v_mfma_f32_16x16x32_bf16 v[36:39], a[144:147], a[4:7], v[36:39]\n"
"v_mfma_f32_16x16x32_bf16 v[40:43], a[144:147], a[8:11], v[40:43]\n"
"v_mfma_f32_16x16x32_bf16 v[44:47], a[144:147], a[12:15], v[44:47]\n"
"global_load_dwordx4 a[68:71], %[vl], s[20:21] offset:-3072\n"
"s_waitcnt vmcnt(12)\n"
"v_mfma_f32_16x16x32_bf16 v[60:63], a[148:151], a[12:15], v[60:63]\n"
"v_mfma_f32_16x16x32_bf16 v[56:59], a[148:151], a[8:11], v[56:59]\n"
"v_mfma_f32_16x16x32_bf16 v[52:55], a[148:151], a[4:7], v[52:55]\n"
"v_mfma_f32_16x16x32_bf16 v[48:51], a[148:151], a[0:3], v[48:51]\n"
"global_load_dwordx4 a[72:75], %[vl], s[20:21] offset:-2048\n"
"s_waitcnt vmcnt(12)\n"
"v_mfma_f32_16x16x32_bf16 v[64:67], a[152:155], a[0:3], v[64:67]\n"
"v_mfma_f32_16x16x32_bf16 v[68:71], a[152:155], a[4:7], v[68:71]\n"
"v_mfma_f32_16x16x32_bf16 v[72:75], a[152:155], a[8:11], v[72:75]\n"
"v_mfma_f32_16x16x32_bf16 v[76:79], a[152:155], a[12:15], v[76:79]\n"
"global_load_dwordx4 a[76:79], %[vl], s[20:21] offset:-1024\n"
"s_waitcnt lgkmcnt(4)\n"
"s_waitcnt vmcnt(12)\n"
"v_mfma_f32_16x16x32_bf16 v[0:3], a[156:159], a[16:19], v[0:3]\n"
"ds_read_b128 a[0:3], %[vx0] offset:12288\n"
"v_mfma_f32_16x16x32_bf16 v[4:7], a[156:159], a[20:23], v[4:7]\n"
"ds_read_b128 a[4:7], %[vx0] offset:31744\n"
"v_mfma_f32_16x16x32_bf16 v[8:11], a[156:159], a[24:27], v[8:11]\n"
"ds_read_b128 a[8:11], %[vx0] offset:51200\n"
"v_mfma_f32_16x16x32_bf16 v[12:15], a[156:159], a[28:31], v[12:15]\n"
"global_load_dwordx4 a[80:83], %[vl], s[20:21] offset:0\n"
"ds_read_b128 a[12:15], v246 offset:5120\n"
"s_waitcnt vmcnt(12)\n"
"v_mfma_f32_16x16x32_bf16 v[28:31], a[160:163], a[28:31], v[28:31]\n"
"v_mfma_f32_16x16x32_bf16 v[24:27], a[160:163], a[24:27], v[24:27]\n"
"v_mfma_f32_16x16x32_bf16 v[20:23], a[160:163], a[20:23], v[20:23]\n"
"v_mfma_f32_16x16x32_bf16 v[16:19], a[160:163], a[16:19], v[16:19]\n"
"global_load_dwordx4 a[84:87], %[vl], s[20:21] offset:1024\n"
"s_waitcnt vmcnt(12)\n"
"v_mfma_f32_16x16x32_bf16 v[32:35], a[164:167], a[16:19], v[32:35]\n"
"v_mfma_f32_16x16x32_bf16 v[36:39], a[164:167], a[20:23], v[36:39]\n"
"v_mfma_f32_16x16x32_bf16 v[40:43], a[164:167], a[24:27], v[40:43]\n"
"v_mfma_f32_16x16x32_bf16 v[44:47], a[164:167], a[28:31], v[44:47]\n"
"global_load_lds_dwordx4 %[vl], s[22:23] offset:3072 sc1\n"
"s_waitcnt vmcnt(12)\n"
"v_mfma_f32_16x16x32_bf16 v[60:63], a[168:171], a[28:31], v[60:63]\n"
"v_mfma_f32_16x16x32_bf16 v[56:59], a[168:171], a[24:27], v[56:59]\n"
"v_mfma_f32_16x16x32_bf16 v[52:55], a[168:171], a[20:23], v[52:55]\n"
"s_add_u32 m0, m0, 4096\n"
"s_add_u32 s22, s22, 4096\n"
"s_addc_u32 s23, s23, 0\n"
"global_load_lds_dwordx4 %[vl], s[22:23] offset:0 sc1\n"
"v_mfma_f32_16x16x32_bf16 v[48:51], a[168:171], a[16:19], v[48:51]\n"
"s_waitcnt vmcnt(12)\n"
"v_mfma_f32_16x16x32_bf16 v[64:67], a[172:175], a[16:19], v[64:67]\n"
"v_mfma_f32_16x16x32_bf16 v[68:71], a[172:175], a[20:23], v[68:71]\n"
"global_load_lds_dwordx4 %[vl], s[22:23] offset:1024 sc1\n"
"v_mfma_f32_16x16x32_bf16 v[72:75], a[172:175], a[24:27], v[72:75]\n"
"v_mfma_f32_16x16x32_bf16 v[76:79], a[172:175], a[28:31], v[76:79]\n"
"s_waitcnt lgkmcnt(0)\n"
"s_waitcnt vmcnt(12)\n"
"v_mfma_f32_16x16x32_bf16 v[0:3], a[176:179], a[0:3], v[0:3]\n"
"global_load_dwordx4 a[88:91], %[vl], s[20:21] offset:2048\n"
"ds_read_b128 a[16:19], %[vx0] offset:13312\n"
"v_mfma_f32_16x16x32_bf16 v[4:7], a[176:179], a[4:7], v[4:7]\n"
"ds_read_b128 a[20:23], %[vx0] offset:32768\n"
"v_mfma_f32_16x16x32_bf16 v[8:11], a[176:179], a[8:11], v[8:11]\n"
"ds_read_b128 a[24:27], %[vx0] offset:52224\n"
"v_mfma_f32_16x16x32_bf16 v[12:15], a[176:179], a[12:15], v[12:15]\n"
"global_load_dwordx4 a[92:95], %[vl], s[20:21] offset:3072\n"
"ds_read_b128 a[28:31], v246 offset:6144\n"
"s_waitcnt vmcnt(13)\n"
"v_mfma_f32_16x16x32_bf16 v[28:31], a[180:183], a[12:15], v[28:31]\n"
"v_mfma_f32_16x16x32_bf16 v[24:27], a[180:183], a[8:11], v[24:27]\n"
"v_mfma_f32_16x16x32_bf16 v[20:23], a[180:183], a[4:7], v[20:23]\n"
"s_add_u32 s20, s20, 8192\n"
"s_addc_u32 s21, s21, 0\n"
"global_load_dwordx4 a[96:99], %[vl], s[20:21] offset:-4096\n"
"v_mfma_f32_16x16x32_bf16 v[16:19], a[180:183], a[0:3], v[16:19]\n"
"s_waitcnt vmcnt(13)\n"
"v_mfma_f32_16x16x32_bf16 v[32:35], a[184:187], a[0:3], v[32:35]\n"
"v_mfma_f32_16x16x32_bf16 v[36:39], a[184:187], a[4:7], v[36:39]\n"
"global_load_dwordx4 a[100:103], %[vl], s[20:21] offset:-3072\n"
"v_mfma_f32_16x16x32_bf16 v[40:43], a[184:187], a[8:11], v[40:43]\n"
"v_mfma_f32_16x16x32_bf16 v[44:47], a[184:187], a[12:15], v[44:47]\n"
"s_waitcnt vmcnt(13)\n"
"v_mfma_f32_16x16x32_bf16 v[60:63], a[188:191], a[12:15], v[60:63]\n"
"global_load_dwordx4 a[104:107], %[vl], s[20:21] offset:-2048\n"
"v_mfma_f32_16x16x32_bf16 v[56:59], a[188:191], a[8:11], v[56:59]\n"
"v_mfma_f32_16x16x32_bf16 v[52:55], a[188:191], a[4:7], v[52:55]\n"
"v_mfma_f32_16x16x32_bf16 v[48:51], a[188:191], a[0:3], v[48:51]\n"
"global_load_dwordx4 a[108:111], %[vl], s[20:21] offset:-1024\n"
"s_waitcnt vmcnt(14)\n"
"v_mfma_f32_16x16x32_bf16 v[64:67], a[64:67], a[0:3], v[64:67]\n"
"v_mfma_f32_16x16x32_bf16 v[68:71], a[64:67], a[4:7], v[68:71]\n"
"v_mfma_f32_16x16x32_bf16 v[72:75], a[64:67], a[8:11], v[72:75]\n"
"global_load_dwordx4 a[112:115], %[vl], s[20:21] offset:0\n"
"v_mfma_f32_16x16x32_bf16 v[76:79], a[64:67], a[12:15], v[76:79]\n"
"s_waitcnt lgkmcnt(0)\n"
"s_waitcnt vmcnt(14)\n"
"v_mfma_f32_16x16x32_bf16 v[0:3], a[68:71], a[16:19], v[0:3]\n"
"ds_read_b128 a[0:3], %[vx0] offset:14336\n"
"v_mfma_f32_16x16x32_bf16 v[4:7], a[68:71], a[20:23], v[4:7]\n"
"global_load_dwordx4 a[116:119], %[vl], s[20:21] offset:1024\n"
"ds_read_b128 a[4:7], %[vx0] offset:33792\n"
"v_mfma_f32_16x16x32_bf16 v[8:11], a[68:71], a[24:27], v[8:11]\n"
"ds_read_b128 a[8:11], %[vx0] offset:53248\n"
"v_mfma_f32_16x16x32_bf16 v[12:15], a[68:71], a[28:31], v[12:15]\n"
"ds_read_b128 a[12:15], v246 offset:7168\n"
"s_waitcnt vmcnt(14)\n"
"v_mfma_f32_16x16x32_bf16 v[28:31], a[72:75], a[28:31], v[28:31]\n"
"global_load_dwordx4 a[120:123], %[vl], s[20:21] offset:2048\n"
"v_mfma_f32_16x16x32_bf16 v[24:27], a[72:75], a[24:27], v[24:27]\n"
"v_mfma_f32_16x16x32_bf16 v[20:23], a[72:75], a[20:23], v[20:23]\n"
"v_mfma_f32_16x16x32_bf16 v[16:19], a[72:75], a[16:19], v[16:19]\n"
"global_load_dwordx4 a[124:127], %[vl], s[20:21] offset:3072\n"
"s_waitcnt vmcnt(15)\n"
"v_mfma_f32_16x16x32_bf16 v[32:35], a[76:79], a[16:19], v[32:35]\n"
"v_mfma_f32_16x16x32_bf16 v[36:39], a[76:79], a[20:23], v[36:39]\n"
"v_mfma_f32_16x16x32_bf16 v[40:43], a[76:79], a[24:27], v[40:43]\n"
"v_mfma_f32_16x16x32_bf16 v[44:47], a[76:79], a[28:31], v[44:47]\n"
"s_add_u32 s20, s20, 8192\n"
"s_addc_u32 s21, s21, 0\n"
"global_load_dwordx4 a[128:131], %[vl], s[20:21] offset:-4096\n"
"s_waitcnt vmcnt(15)\n"
"v_mfma_f32_16x16x32_bf16 v[60:63], a[80:83], a[28:31], v[60:63]\n"
"v_mfma_f32_16x16x32_bf16 v[56:59], a[80:83], a[24:27], v[56:59]\n"
"v_mfma_f32_16x16x32_bf16 v[52:55], a[80:83], a[20:23], v[52:55]\n"
"v_mfma_f32_16x16x32_bf16 v[48:51], a[80:83], a[16:19], v[48:51]\n"
"global_load_dwordx4 a[132:135], %[vl], s[20:21] offset:-3072\n"
"s_waitcnt vmcnt(15)\n"
"v_mfma_f32_16x16x32_bf16 v[64:67], a[84:87], a[16:19], v[64:67]\n"
"v_mfma_f32_16x16x32_bf16 v[68:71], a[84:87], a[20:23], v[68:71]\n"
"v_mfma_f32_16x16x32_bf16 v[72:75], a[84:87], a[24:27], v[72:75]\n"
"v_mfma_f32_16x16x32_bf16 v[76:79], a[84:87], a[28:31], v[76:79]\n"
"global_load_dwordx4 a[136:139], %[vl], s[20:21] offset:-2048\n"
"s_waitcnt lgkmcnt(0)\n"
"s_waitcnt vmcnt(12)\n"
"v_mfma_f32_16x16x32_bf16 v[0:3], a[88:91], a[0:3], v[0:3]\n"
"s_barrier\n"
"ds_read_b128 a[16:19], %[vx0] offset:15360\n"
"v_mfma_f32_16x16x32_bf16 v[4:7], a[88:91], a[4:7], v[4:7]\n"
"ds_read_b128 a[20:23], %[vx0] offset:34816\n"
"v_mfma_f32_16x16x32_bf16 v[8:11], a[88:91], a[8:11], v[8:11]\n"
"ds_read_b128 a[24:27], %[vx0] offset:54272\n"
"v_mfma_f32_16x16x32_bf16 v[12:15], a[88:91], a[12:15], v[12:15]\n"
"global_load_dwordx4 a[140:143], %[vl], s[20:21] offset:-1024\n"
"ds_read_b128 a[28:31], v246 offset:8192\n"
"s_waitcnt vmcnt(12)\n"
"v_mfma_f32_16x16x32_bf16 v[28:31], a[92:95], a[12:15], v[28:31]\n"
"v_mfma_f32_16x16x32_bf16 v[24:27], a[92:95], a[8:11], v[24:27]\n"
"v_mfma_f32_16x16x32_bf16 v[20:23], a[92:95], a[4:7], v[20:23]\n"
"v_mfma_f32_16x16x32_bf16 v[16:19], a[92:95], a[0:3], v[16:19]\n"
"global_load_dwordx4 a[144:147], %[vl], s[20:21] offset:0\n"
"s_waitcnt vmcnt(12)\n"
"v_mfma_f32_16x16x32_bf16 v[32:35], a[96:99], a[0:3], v[32:35]\n"
"v_mfma_f32_16x16x32_bf16 v[36:39], a[96:99], a[4:7], v[36:39]\n"
"v_mfma_f32_16x16x32_bf16 v[40:43], a[96:99], a[8:11], v[40:43]\n"
"v_mfma_f32_16x16x32_bf16 v[44:47], a[96:99], a[12:15], v[44:47]\n"
"global_load_dwordx4 a[148:151], %[vl], s[20:21] offset:1024\n"
"s_waitcnt vmcnt(12)\n"
"v_mfma_f32_16x16x32_bf16 v[60:63], a[100:103], a[12:15], v[60:63]\n"
"v_mfma_f32_16x16x32_bf16 v[56:59], a[100:103], a[8:11], v[56:59]\n"
"v_mfma_f32_16x16x32_bf16 v[52:55], a[100:103], a[4:7], v[52:55]\n"
"v_mfma_f32_16x16x32_bf16 v[48:51], a[100:103], a[0:3], v[48:51]\n"
"global_load_dwordx4 a[152:155], %[vl], s[20:21] offset:2048\n"
"s_waitcnt vmcnt(12)\n"
"v_mfma_f32_16x16x32_bf16 v[64:67], a[104:107], a[0:3], v[64:67]\n"
"v_mfma_f32_16x16x32_bf16 v[68:71], a[104:107], a[4:7], v[68:71]\n"
"v_mfma_f32_16x16x32_bf16 v[72:75], a[104:107], a[8:11], v[72:75]\n"
"v_mfma_f32_16x16x32_bf16 v[76:79], a[104:107], a[12:15], v[76:79]\n"
"global_load_dwordx4 a[156:159], %[vl], s[20:21] offset:3072\n"
"s_waitcnt lgkmcnt(0)\n"
"s_waitcnt vmcnt(12)\n"
"v_mfma_f32_16x16x32_bf16 v[0:3], a[108:111], a[16:19], v[0:3]\n"
"ds_read_b128 a[0:3], %[vx0] offset:16384\n"
"v_mfma_f32_16x16x32_bf16 v[4:7], a[108:111], a[20:23], v[4:7]\n"
"ds_read_b128 a[4:7], %[vx0] offset:35840\n"
"v_mfma_f32_16x16x32_bf16 v[8:11], a[108:111], a[24:27], v[8:11]\n"
"ds_read_b128 a[8:11], %[vx0] offset:55296\n"
"v_mfma_f32_16x16x32_bf16 v[12:15], a[108:111], a[28:31], v[12:15]\n"
"s_add_u32 s20, s20, 8192\n"
"s_addc_u32 s21, s21, 0\n"
"global_load_dwordx4 a[160:163], %[vl], s[20:21] offset:-4096\n"
"ds_read_b128 a[12:15], v246 offset:9216\n"
"s_waitcnt vmcnt(12)\n"
"v_mfma_f32_16x16x32_bf16 v[28:31], a[112:115], a[28:31], v[28:31]\n"
"v_mfma_f32_16x16x32_bf16 v[24:27], a[112:115], a[24:27], v[24:27]\n"
"v_mfma_f32_16x16x32_bf16 v[20:23], a[112:115], a[20:23], v[20:23]\n"
"v_mfma_f32_16x16x32_bf16 v[16:19], a[112:115], a[16:19], v[16:19]\n"
"global_load_dwordx4 a[164:167], %[vl], s[20:21] offset:-3072\n"
"s_waitcnt vmcnt(12)\n"
"v_mfma_f32_16x16x32_bf16 v[32:35], a[116:119], a[16:19], v[32:35]\n"
"v_mfma_f32_16x16x32_bf16 v[36:39], a[116:119], a[20:23], v[36:39]\n"
"v_mfma_f32_16x16x32_bf16 v[40:43], a[116:119], a[24:27], v[40:43]\n"
"v_mfma_f32_16x16x32_bf16 v[44:47], a[116:119], a[28:31], v[44:47]\n"
"global_load_dwordx4 a[168:171], %[vl], s[20:21] offset:-2048\n"
"s_waitcnt vmcnt(12)\n"
"v_mfma_f32_16x16x32_bf16 v[60:63], a[120:123], a[28:31], v[60:63]\n"
"v_mfma_f32_16x16x32_bf16 v[56:59], a[120:123], a[24:27], v[56:59]\n"
"v_mfma_f32_16x16x32_bf16 v[52:55], a[120:123], a[20:23], v[52:55]\n"
"v_mfma_f32_16x16x32_bf16 v[48:51], a[120:123], a[16:19], v[48:51]\n"
"global_load_dwordx4 a[172:175], %[vl], s[20:21] offset:-1024\n"
"s_waitcnt vmcnt(12)\n"
"v_mfma_f32_16x16x32_bf16 v[64:67], a[124:127], a[16:19], v[64:67]\n"
"v_mfma_f32_16x16x32_bf16 v[68:71], a[124:127], a[20:23], v[68:71]\n"
"v_mfma_f32_16x16x32_bf16 v[72:75], a[124:127], a[24:27], v[72:75]\n"
"v_mfma_f32_16x16x32_bf16 v[76:79], a[124:127], a[28:31], v[76:79]\n"
"global_load_dwordx4 a[176:179], %[vl], s[20:21] offset:0\n"
"s_waitcnt lgkmcnt(0)\n"
"s_waitcnt vmcnt(12)\n"
"v_mfma_f32_16x16x32_bf16 v[0:3], a[128:131], a[0:3], v[0:3]\n"
"ds_read_b128 a[16:19], %[vx0] offset:17408\n"
"v_mfma_f32_16x16x32_bf16 v[4:7], a[128:131], a[4:7], v[4:7]\n"
"ds_read_b128 a[20:23], %[vx0] offset:36864\n"
"v_mfma_f32_16x16x32_bf16 v[8:11], a[128:131], a[8:11], v[8:11]\n"
"ds_read_b128 a[24:27], %[vx0] offset:56320\n"
"v_mfma_f32_16x16x32_bf16 v[12:15], a[128:131], a[12:15], v[12:15]\n"
"global_load_dwordx4 a[180:183], %[vl], s[20:21] offset:1024\n"
"ds_read_b128 a[28:31], v246 offset:10240\n"
"s_waitcnt vmcnt(12)\n"
"v_mfma_f32_16x16x32_bf16 v[28:31], a[132:135], a[12:15], v[28:31]\n"
"v_mfma_f32_16x16x32_bf16 v[24:27], a[132:135], a[8:11], v[24:27]\n"
"v_mfma_f32_16x16x32_bf16 v[20:23], a[132:135], a[4:7], v[20:23]\n"
"v_mfma_f32_16x16x32_bf16 v[16:19], a[132:135], a[0:3], v[16:19]\n"
"global_load_dwordx4 a[184:187], %[vl], s[20:21] offset:2048\n"
"s_waitcnt vmcnt(12)\n"
"v_mfma_f32_16x16x32_bf16 v[32:35], a[136:139], a[0:3], v[32:35]\n"
"v_mfma_f32_16x16x32_bf16 v[36:39], a[136:139], a[4:7], v[36:39]\n"
"v_mfma_f32_16x16x32_bf16 v[40:43], a[136:139], a[8:11], v[40:43]\n"
"v_mfma_f32_16x16x32_bf16 v[44:47], a[136:139], a[12:15], v[44:47]\n"
"global_load_dwordx4 a[188:191], %[vl], s[20:21] offset:3072\n"
"s_waitcnt vmcnt(12)\n"
"v_mfma_f32_16x16x32_bf16 v[60:63], a[140:143], a[12:15], v[60:63]\n"
"v_mfma_f32_16x16x32_bf16 v[56:59], a[140:143], a[8:11], v[56:59]\n"
"v_mfma_f32_16x16x32_bf16 v[52:55], a[140:143], a[4:7], v[52:55]\n"
"v_mfma_f32_16x16x32_bf16 v[48:51], a[140:143], a[0:3], v[48:51]\n"
"s_add_u32 s20, s20, 8192\n"
"s_addc_u32 s21, s21, 0\n"
"global_load_dwordx4 a[64:67], %[vl], s[20:21] offset:-4096\n"
"s_waitcnt vmcnt(12)\n"
"v_mfma_f32_16x16x32_bf16 v[64:67], a[144:147], a[0:3], v[64:67]\n"
"v_mfma_f32_16x16x32_bf16 v[68:71], a[144:147], a[4:7], v[68:71]\n"
"v_mfma_f32_16x16x32_bf16 v[72:75], a[144:147], a[8:11], v[72:75]\n"
"v_mfma_f32_16x16x32_bf16 v[76:79], a[144:147], a[12:15], v[76:79]\n"
"global_load_dwordx4 a[68:71], %[vl], s[20:21] offset:-3072\n"
"s_waitcnt lgkmcnt(0)\n"
"s_waitcnt vmcnt(12)\n"
"v_mfma_f32_16x16x32_bf16 v[0:3], a[148:151], a[16:19], v[0:3]\n"
"ds_read_b128 a[0:3], %[vx0] offset:18432\n"
"v_mfma_f32_16x16x32_bf16 v[4:7], a[148:151], a[20:23], v[4:7]\n"
"ds_read_b128 a[4:7], %[vx0] offset:37888\n"
"v_mfma_f32_16x16x32_bf16 v[8:11], a[148:151], a[24:27], v[8:11]\n"
"ds_read_b128 a[8:11], %[vx0] offset:57344\n"
"v_mfma_f32_16x16x32_bf16 v[12:15], a[148:151], a[28:31], v[12:15]\n"
"global_load_dwordx4 a[72:75], %[vl], s[20:21] offset:-2048\n"
"ds_read_b128 a[12:15], v246 offset:11264\n"
"s_waitcnt vmcnt(12)\n"
"v_mfma_f32_16x16x32_bf16 v[28:31], a[152:155], a[28:31], v[28:31]\n"
"v_mfma_f32_16x16x32_bf16 v[24:27], a[152:155], a[24:27], v[24:27]\n"
"v_mfma_f32_16x16x32_bf16 v[20:23], a[152:155], a[20:23], v[20:23]\n"
"v_mfma_f32_16x16x32_bf16 v[16:19], a[152:155], a[16:19], v[16:19]\n"
"global_load_dwordx4 a[76:79], %[vl], s[20:21] offset:-1024\n"
"s_waitcnt vmcnt(12)\n"
"v_mfma_f32_16x16x32_bf16 v[32:35], a[156:159], a[16:19], v[32:35]\n"
"v_mfma_f32_16x16x32_bf16 v[36:39], a[156:159], a[20:23], v[36:39]\n"
"v_mfma_f32_16x16x32_bf16 v[40:43], a[156:159], a[24:27], v[40:43]\n"
"v_mfma_f32_16x16x32_bf16 v[44:47], a[156:159], a[28:31], v[44:47]\n"
"global_load_dwordx4 a[80:83], %[vl], s[20:21] offset:0\n"
"s_waitcnt vmcnt(12)\n"
"v_mfma_f32_16x16x32_bf16 v[60:63], a[160:163], a[28:31], v[60:63]\n"
"v_mfma_f32_16x16x32_bf16 v[56:59], a[160:163], a[24:27], v[56:59]\n"
"v_mfma_f32_16x16x32_bf16 v[52:55], a[160:163], a[20:23], v[52:55]\n"
"v_mfma_f32_16x16x32_bf16 v[48:51], a[160:163], a[16:19], v[48:51]\n"
"global_load_dwordx4 a[84:87], %[vl], s[20:21] offset:1024\n"
"s_waitcnt vmcnt(12)\n"
"v_mfma_f32_16x16x32_bf16 v[64:67], a[164:167], a[16:19], v[64:67]\n"
"v_mfma_f32_16x16x32_bf16 v[68:71], a[164:167], a[20:23], v[68:71]\n"
"v_mfma_f32_16x16x32_bf16 v[72:75], a[164:167], a[24:27], v[72:75]\n"
"v_mfma_f32_16x16x32_bf16 v[76:79], a[164:167], a[28:31], v[76:79]\n"
"global_load_dwordx4 a[88:91], %[vl], s[20:21] offset:2048\n"
"s_waitcnt lgkmcnt(0)\n"
"s_waitcnt vmcnt(12)\n"
"v_mfma_f32_16x16x32_bf16 v[0:3], a[168:171], a[0:3], v[0:3]\n"
"ds_read_b128 a[16:19], %[vx0] offset:0\n"
"v_mfma_f32_16x16x32_bf16 v[4:7], a[168:171], a[4:7], v[4:7]\n"
"ds_read_b128 a[20:23], %[vx0] offset:19456\n"
"v_mfma_f32_16x16x32_bf16 v[8:11], a[168:171], a[8:11], v[8:11]\n"
"ds_read_b128 a[24:27], %[vx0] offset:38912\n"
"v_mfma_f32_16x16x32_bf16 v[12:15], a[168:171], a[12:15], v[12:15]\n"
"global_load_dwordx4 a[92:95], %[vl], s[20:21] offset:3072\n"
"ds_read_b128 a[28:31], %[vx0] offset:58368\n"
"s_waitcnt vmcnt(12)\n"
"v_mfma_f32_16x16x32_bf16 v[28:31], a[172:175], a[12:15], v[28:31]\n"
"v_mfma_f32_16x16x32_bf16 v[24:27], a[172:175], a[8:11], v[24:27]\n"
"v_mfma_f32_16x16x32_bf16 v[20:23], a[172:175], a[4:7], v[20:23]\n"
"v_mfma_f32_16x16x32_bf16 v[16:19], a[172:175], a[0:3], v[16:19]\n"
"s_add_u32 s20, s20, 8192\n"
"s_addc_u32 s21, s21, 0\n"
"global_load_dwordx4 a[96:99], %[vl], s[20:21] offset:-4096\n"
"s_waitcnt vmcnt(12)\n"
"v_mfma_f32_16x16x32_bf16 v[32:35], a[176:179], a[0:3], v[32:35]\n"
"v_mfma_f32_16x16x32_bf16 v[36:39], a[176:179], a[4:7], v[36:39]\n"
"v_mfma_f32_16x16x32_bf16 v[40:43], a[176:179], a[8:11], v[40:43]\n"
"v_mfma_f32_16x16x32_bf16 v[44:47], a[176:179], a[12:15], v[44:47]\n"
"global_load_dwordx4 a[100:103], %[vl], s[20:21] offset:-3072\n"
"s_waitcnt vmcnt(12)\n"
"v_mfma_f32_16x16x32_bf16 v[60:63], a[180:183], a[12:15], v[60:63]\n"
"v_mfma_f32_16x16x32_bf16 v[56:59], a[180:183], a[8:11], v[56:59]\n"
"v_mfma_f32_16x16x32_bf16 v[52:55], a[180:183], a[4:7], v[52:55]\n"
"v_mfma_f32_16x16x32_bf16 v[48:51], a[180:183], a[0:3], v[48:51]\n"
"global_load_dwordx4 a[104:107], %[vl], s[20:21] offset:-2048\n"
"s_waitcnt vmcnt(12)\n"
"v_mfma_f32_16x16x32_bf16 v[64:67], a[184:187], a[0:3], v[64:67]\n"
"v_mfma_f32_16x16x32_bf16 v[68:71], a[184:187], a[4:7], v[68:71]\n"
"v_mfma_f32_16x16x32_bf16 v[72:75], a[184:187], a[8:11], v[72:75]\n"
"v_mfma_f32_16x16x32_bf16 v[76:79], a[184:187], a[12:15], v[76:79]\n"
"global_load_dwordx4 a[108:111], %[vl], s[20:21] offset:-1024\n"
"s_waitcnt lgkmcnt(0)\n"
"s_waitcnt vmcnt(12)\n"
"v_mfma_f32_16x16x32_bf16 v[80:83], a[188:191], a[16:19], 0\n"
"ds_read_b128 a[0:3], %[vx0] offset:1024\n"
"v_mfma_f32_16x16x32_bf16 v[84:87], a[188:191], a[20:23], 0\n"
"ds_read_b128 a[4:7], %[vx0] offset:20480\n"
"v_mfma_f32_16x16x32_bf16 v[88:91], a[188:191], a[24:27], 0\n"
"ds_read_b128 a[8:11], %[vx0] offset:39936\n"
"v_mfma_f32_16x16x32_bf16 v[92:95], a[188:191], a[28:31], 0\n"
"global_load_dwordx4 a[112:115], %[vl], s[20:21] offset:0\n"
"ds_read_b128 a[12:15], %[vx0] offset:59392\n"
"v_mul_f32 v232, v0, v225\n"
"v_mul_f32 v233, v1, v225\n"
"s_waitcnt vmcnt(12)\n"
"v_mfma_f32_16x16x32_bf16 v[108:111], a[64:67], a[28:31], 0\n"
"v_mul_f32 v234, v2, v225\n"
"v_mul_f32 v235, v3, v225\n"
"v_mfma_f32_16x16x32_bf16 v[104:107], a[64:67], a[24:27], 0\n"
"v_exp_f32 v232, v232\n"
"v_mfma_f32_16x16x32_bf16 v[100:103], a[64:67], a[20:23], 0\n"
"v_exp_f32 v233, v233\n"
"v_exp_f32 v234, v234\n"
"v_mfma_f32_16x16x32_bf16 v[96:99], a[64:67], a[16:19], 0\n"
"global_load_dwordx4 a[116:119], %[vl], s[20:21] offset:1024\n"
"s_waitcnt vmcnt(12)\n"
"v_mfma_f32_16x16x32_bf16 v[112:115], a[68:71], a[16:19], 0\n"
"v_exp_f32 v235, v235\n"
"v_fma_f32 v232, v232, v224, v224\n"
"v_fma_f32 v233, v233, v224, v224\n"
"v_mfma_f32_16x16x32_bf16 v[116:119], a[68:71], a[20:23], 0\n"
"v_fma_f32 v234, v234, v224, v224\n"
"v_fma_f32 v235, v235, v224, v224\n"
"v_mfma_f32_16x16x32_bf16 v[120:123], a[68:71], a[24:27], 0\n"
"v_rcp_f32 v232, v232\n"
"v_mfma_f32_16x16x32_bf16 v[124:127], a[68:71], a[28:31], 0\n"
"global_load_dwordx4 a[120:123], %[vl], s[20:21] offset:2048\n"
"v_rcp_f32 v233, v233\n"
"s_waitcnt vmcnt(12)\n"
"v_mfma_f32_16x16x32_bf16 v[140:143], a[72:75], a[28:31], 0\n"
"v_rcp_f32 v234, v234\n"
"v_mfma_f32_16x16x32_bf16 v[136:139], a[72:75], a[24:27], 0\n"
"v_rcp_f32 v235, v235\n"
"v_mul_f32 v0, v0, v232\n"
"v_mfma_f32_16x16x32_bf16 v[132:135], a[72:75], a[20:23], 0\n"
"v_mul_f32 v1, v1, v233\n"
"v_mul_f32 v2, v2, v234\n"
"v_mul_f32 v3, v3, v235\n"
"v_mfma_f32_16x16x32_bf16 v[128:131], a[72:75], a[16:19], 0\n"
"global_load_dwordx4 a[124:127], %[vl], s[20:21] offset:3072\n"
"v_cvt_pk_bf16_f32 v238, v0, v1\n"
"s_waitcnt lgkmcnt(0)\n"
"s_waitcnt vmcnt(12)\n"
"v_mfma_f32_16x16x32_bf16 v[80:83], a[76:79], a[0:3], v[80:83]\n"
"ds_read_b128 a[16:19], %[vx0] offset:2048\n"
"v_mfma_f32_16x16x32_bf16 v[84:87], a[76:79], a[4:7], v[84:87]\n"
"ds_read_b128 a[20:23], %[vx0] offset:21504\n"
"v_mfma_f32_16x16x32_bf16 v[88:91], a[76:79], a[8:11], v[88:91]\n"
"ds_read_b128 a[24:27], %[vx0] offset:40960\n"
"v_mfma_f32_16x16x32_bf16 v[92:95], a[76:79], a[12:15], v[92:95]\n"
"s_add_u32 s20, s20, 8192\n"
"s_addc_u32 s21, s21, 0\n"
"global_load_dwordx4 a[128:131], %[vl], s[20:21] offset:-4096\n"
"ds_read_b128 a[28:31], %[vx0] offset:60416\n"
"v_cvt_pk_bf16_f32 v239, v2, v3\n"
"v_mul_f32 v232, v16, v225\n"
"s_waitcnt vmcnt(12)\n"
"v_mfma_f32_16x16x32_bf16 v[108:111], a[80:83], a[12:15], v[108:111]\n"
"v_mul_f32 v233, v17, v225\n"
"v_mul_f32 v234, v18, v225\n"
"v_mul_f32 v235, v19, v225\n"
"v_mfma_f32_16x16x32_bf16 v[104:107], a[80:83], a[8:11], v[104:107]\n"
"v_exp_f32 v232, v232\n"
"v_mfma_f32_16x16x32_bf16 v[100:103], a[80:83], a[4:7], v[100:103]\n"
"v_exp_f32 v233, v233\n"
"v_mfma_f32_16x16x32_bf16 v[96:99], a[80:83], a[0:3], v[96:99]\n"
"global_load_dwordx4 a[132:135], %[vl], s[20:21] offset:-3072\n"
"v_exp_f32 v234, v234\n"
"s_waitcnt vmcnt(12)\n"
"v_mfma_f32_16x16x32_bf16 v[112:115], a[84:87], a[0:3], v[112:115]\n"
"v_exp_f32 v235, v235\n"
"v_fma_f32 v232, v232, v224, v224\n"
"v_mfma_f32_16x16x32_bf16 v[116:119], a[84:87], a[4:7], v[116:119]\n"
"v_fma_f32 v233, v233, v224, v224\n"
"v_fma_f32 v234, v234, v224, v224\n"
"v_mfma_f32_16x16x32_bf16 v[120:123], a[84:87], a[8:11], v[120:123]\n"
"v_fma_f32 v235, v235, v224, v224\n"
"v_rcp_f32 v232, v232\n"
"v_mfma_f32_16x16x32_bf16 v[124:127], a[84:87], a[12:15], v[124:127]\n"
"global_load_dwordx4 a[136:139], %[vl], s[20:21] offset:-2048\n"
"s_waitcnt vmcnt(12)\n"
"v_mfma_f32_16x16x32_bf16 v[140:143], a[88:91], a[12:15], v[140:143]\n"
"v_rcp_f32 v233, v233\n"
"v_rcp_f32 v234, v234\n"
"v_mfma_f32_16x16x32_bf16 v[136:139], a[88:91], a[8:11], v[136:139]\n"
"v_rcp_f32 v235, v235\n"
"v_mfma_f32_16x16x32_bf16 v[132:135], a[88:91], a[4:7], v[132:135]\n"
"v_mul_f32 v16, v16, v232\n"
"v_mul_f32 v17, v17, v233\n"
"v_mul_f32 v18, v18, v234\n"
"v_mfma_f32_16x16x32_bf16 v[128:131], a[88:91], a[0:3], v[128:131]\n"
"global_load_dwordx4 a[140:143], %[vl], s[20:21] offset:-1024\n"
"v_mul_f32 v19, v19, v235\n"
"s_waitcnt lgkmcnt(0)\n"
"s_waitcnt vmcnt(12)\n"
"v_mfma_f32_16x16x32_bf16 v[80:83], a[92:95], a[16:19], v[80:83]\n"
"ds_read_b128 a[0:3], %[vx0] offset:3072\n"
"v_mfma_f32_16x16x32_bf16 v[84:87], a[92:95], a[20:23], v[84:87]\n"
"ds_read_b128 a[4:7], %[vx0] offset:22528\n"
"v_mfma_f32_16x16x32_bf16 v[88:91], a[92:95], a[24:27], v[88:91]\n"
"ds_read_b128 a[8:11], %[vx0] offset:41984\n"
"v_mfma_f32_16x16x32_bf16 v[92:95], a[92:95], a[28:31], v[92:95]\n"
"global_load_dwordx4 a[144:147], %[vl], s[20:21] offset:0\n"
"ds_read_b128 a[12:15], %[vx0] offset:61440\n"
"v_cvt_pk_bf16_f32 v240, v16, v17\n"
"v_cvt_pk_bf16_f32 v241, v18, v19\n"
"s_waitcnt vmcnt(12)\n"
"v_mfma_f32_16x16x32_bf16 v[108:111], a[96:99], a[28:31], v[108:111]\n"
"ds_write_b128 %[vhw], v[238:241] offset:0\n"
"v_mul_f32 v232, v4, v227\n"
"v_mul_f32 v233, v5, v227\n"
"v_mfma_f32_16x16x32_bf16 v[104:107], a[96:99], a[24:27], v[104:107]\n"
"v_mul_f32 v234, v6, v227\n"
"v_mul_f32 v235, v7, v227\n"
"v_mfma_f32_16x16x32_bf16 v[100:103], a[96:99], a[20:23], v[100:103]\n"
"v_exp_f32 v232, v232\n"
"v_mfma_f32_16x16x32_bf16 v[96:99], a[96:99], a[16:19], v[96:99]\n"
"global_load_dwordx4 a[148:151], %[vl], s[20:21] offset:1024\n"
"v_exp_f32 v233, v233\n"
"s_waitcnt vmcnt(12)\n"
"v_mfma_f32_16x16x32_bf16 v[112:115], a[100:103], a[16:19], v[112:115]\n"
"v_exp_f32 v234, v234\n"
"v_mfma_f32_16x16x32_bf16 v[116:119], a[100:103], a[20:23], v[116:119]\n"
"v_exp_f32 v235, v235\n"
"v_fma_f32 v232, v232, v226, v226\n"
"v_mfma_f32_16x16x32_bf16 v[120:123], a[100:103], a[24:27], v[120:123]\n"
"v_fma_f32 v233, v233, v226, v226\n"
"v_fma_f32 v234, v234, v226, v226\n"
"v_fma_f32 v235, v235, v226, v226\n"
"v_mfma_f32_16x16x32_bf16 v[124:127], a[100:103], a[28:31], v[124:127]\n"
"global_load_dwordx4 a[152:155], %[vl], s[20:21] offset:2048\n"
"s_waitcnt vmcnt(12)\n"
"v_mfma_f32_16x16x32_bf16 v[140:143], a[104:107], a[28:31], v[140:143]\n"
"v_rcp_f32 v232, v232\n"
"v_rcp_f32 v233, v233\n"
"v_mfma_f32_16x16x32_bf16 v[136:139], a[104:107], a[24:27], v[136:139]\n"
"v_rcp_f32 v234, v234\n"
"v_mfma_f32_16x16x32_bf16 v[132:135], a[104:107], a[20:23], v[132:135]\n"
"v_rcp_f32 v235, v235\n"
"v_mul_f32 v4, v4, v232\n"
"v_mfma_f32_16x16x32_bf16 v[128:131], a[104:107], a[16:19], v[128:131]\n"
"global_load_dwordx4 a[156:159], %[vl], s[20:21] offset:3072\n"
"v_mul_f32 v5, v5, v233\n"
"s_waitcnt lgkmcnt(1)\n"
"s_waitcnt vmcnt(12)\n"
"v_mfma_f32_16x16x32_bf16 v[80:83], a[108:111], a[0:3], v[80:83]\n"
"ds_read_b128 a[16:19], %[vx0] offset:4096\n"
"v_mfma_f32_16x16x32_bf16 v[84:87], a[108:111], a[4:7], v[84:87]\n"
"ds_read_b128 a[20:23], %[vx0] offset:23552\n"
"v_mfma_f32_16x16x32_bf16 v[88:91], a[108:111], a[8:11], v[88:91]\n"
"ds_read_b128 a[24:27], %[vx0] offset:43008\n"
"v_mfma_f32_16x16x32_bf16 v[92:95], a[108:111], a[12:15], v[92:95]\n"
"s_add_u32 s20, s20, 8192\n"
"s_addc_u32 s21, s21, 0\n"
"global_load_dwordx4 a[160:163], %[vl], s[20:21] offset:-4096\n"
"ds_read_b128 a[28:31], %[vx0] offset:62464\n"
"v_mul_f32 v6, v6, v234\n"
"v_mul_f32 v7, v7, v235\n"
"s_waitcnt vmcnt(12)\n"
"v_mfma_f32_16x16x32_bf16 v[108:111], a[112:115], a[12:15], v[108:111]\n"
"v_cvt_pk_bf16_f32 v242, v4, v5\n"
"v_cvt_pk_bf16_f32 v243, v6, v7\n"
"v_mul_f32 v232, v20, v227\n"
"v_mfma_f32_16x16x32_bf16 v[104:107], a[112:115], a[8:11], v[104:107]\n"
"v_mul_f32 v233, v21, v227\n"
"v_mul_f32 v234, v22, v227\n"
"v_mfma_f32_16x16x32_bf16 v[100:103], a[112:115], a[4:7], v[100:103]\n"
"v_mul_f32 v235, v23, v227\n"
"v_exp_f32 v232, v232\n"
"v_mfma_f32_16x16x32_bf16 v[96:99], a[112:115], a[0:3], v[96:99]\n"
"global_load_dwordx4 a[164:167], %[vl], s[20:21] offset:-3072\n"
"s_waitcnt vmcnt(12)\n"
"v_mfma_f32_16x16x32_bf16 v[112:115], a[116:119], a[0:3], v[112:115]\n"
"v_exp_f32 v233, v233\n"
"v_exp_f32 v234, v234\n"
"v_mfma_f32_16x16x32_bf16 v[116:119], a[116:119], a[4:7], v[116:119]\n"
"v_exp_f32 v235, v235\n"
"v_mfma_f32_16x16x32_bf16 v[120:123], a[116:119], a[8:11], v[120:123]\n"
"v_fma_f32 v232, v232, v226, v226\n"
"v_fma_f32 v233, v233, v226, v226\n"
"v_fma_f32 v234, v234, v226, v226\n"
"v_mfma_f32_16x16x32_bf16 v[124:127], a[116:119], a[12:15], v[124:127]\n"
"global_load_dwordx4 a[168:171], %[vl], s[20:21] offset:-2048\n"
"v_fma_f32 v235, v235, v226, v226\n"
"s_waitcnt vmcnt(12)\n"
"v_mfma_f32_16x16x32_bf16 v[140:143], a[120:123], a[12:15], v[140:143]\n"
"v_rcp_f32 v232, v232\n"
"v_mfma_f32_16x16x32_bf16 v[136:139], a[120:123], a[8:11], v[136:139]\n"
"v_rcp_f32 v233, v233\n"
"v_mfma_f32_16x16x32_bf16 v[132:135], a[120:123], a[4:7], v[132:135]\n"
"v_rcp_f32 v234, v234\n"
"v_rcp_f32 v235, v235\n"
"v_mfma_f32_16x16x32_bf16 v[128:131], a[120:123], a[0:3], v[128:131]\n"
"global_load_dwordx4 a[172:175], %[vl], s[20:21] offset:-1024\n"
"v_mul_f32 v20, v20, v232\n"
"s_waitcnt lgkmcnt(0)\n"
"s_waitcnt vmcnt(12)\n"
"v_mfma_f32_16x16x32_bf16 v[80:83], a[124:127], a[16:19], v[80:83]\n"
"ds_read_b128 a[0:3], %[vx0] offset:5120\n"
"v_mfma_f32_16x16x32_bf16 v[84:87], a[124:127], a[20:23], v[84:87]\n"
"ds_read_b128 a[4:7], %[vx0] offset:24576\n"
"v_mfma_f32_16x16x32_bf16 v[88:91], a[124:127], a[24:27], v[88:91]\n"
"ds_read_b128 a[8:11], %[vx0] offset:44032\n"
"v_mfma_f32_16x16x32_bf16 v[92:95], a[124:127], a[28:31], v[92:95]\n"
"global_load_dwordx4 a[176:179], %[vl], s[20:21] offset:0\n"
"ds_read_b128 a[12:15], %[vx0] offset:63488\n"
"v_mul_f32 v21, v21, v233\n"
"v_mul_f32 v22, v22, v234\n"
"s_waitcnt vmcnt(12)\n"
"v_mfma_f32_16x16x32_bf16 v[108:111], a[128:131], a[28:31], v[108:111]\n"
"v_mul_f32 v23, v23, v235\n"
"v_cvt_pk_bf16_f32 v244, v20, v21\n"
"v_cvt_pk_bf16_f32 v245, v22, v23\n"
"v_mfma_f32_16x16x32_bf16 v[104:107], a[128:131], a[24:27], v[104:107]\n"
"ds_write_b128 %[vhw], v[242:245] offset:1024\n"
"v_mul_f32 v232, v8, v229\n"
"v_mfma_f32_16x16x32_bf16 v[100:103], a[128:131], a[20:23], v[100:103]\n"
"v_mul_f32 v233, v9, v229\n"
"v_mul_f32 v234, v10, v229\n"
"v_mul_f32 v235, v11, v229\n"
"v_mfma_f32_16x16x32_bf16 v[96:99], a[128:131], a[16:19], v[96:99]\n"
"global_load_dwordx4 a[180:183], %[vl], s[20:21] offset:1024\n"
"s_waitcnt vmcnt(12)\n"
"v_mfma_f32_16x16x32_bf16 v[112:115], a[132:135], a[16:19], v[112:115]\n"
"v_exp_f32 v232, v232\n"
"v_exp_f32 v233, v233\n"
"v_mfma_f32_16x16x32_bf16 v[116:119], a[132:135], a[20:23], v[116:119]\n"
"v_exp_f32 v234, v234\n"
"v_mfma_f32_16x16x32_bf16 v[120:123], a[132:135], a[24:27], v[120:123]\n"
"v_exp_f32 v235, v235\n"
"v_fma_f32 v232, v232, v228, v228\n"
"v_mfma_f32_16x16x32_bf16 v[124:127], a[132:135], a[28:31], v[124:127]\n"
"global_load_dwordx4 a[184:187], %[vl], s[20:21] offset:2048\n"
"v_fma_f32 v233, v233, v228, v228\n"
"s_waitcnt vmcnt(12)\n"
"v_mfma_f32_16x16x32_bf16 v[140:143], a[136:139], a[28:31], v[140:143]\n"
"v_fma_f32 v234, v234, v228, v228\n"
"v_fma_f32 v235, v235, v228, v228\n"
"v_mfma_f32_16x16x32_bf16 v[136:139], a[136:139], a[24:27], v[136:139]\n"
"v_rcp_f32 v232, v232\n"
"v_mfma_f32_16x16x32_bf16 v[132:135], a[136:139], a[20:23], v[132:135]\n"
"v_rcp_f32 v233, v233\n"
"v_rcp_f32 v234, v234\n"
"v_mfma_f32_16x16x32_bf16 v[128:131], a[136:139], a[16:19], v[128:131]\n"
"global_load_dwordx4 a[188:191], %[vl], s[20:21] offset:3072\n"
"s_waitcnt lgkmcnt(1)\n"
"s_waitcnt vmcnt(12)\n"
"v_mfma_f32_16x16x32_bf16 v[80:83], a[140:143], a[0:3], v[80:83]\n"
"ds_read_b128 a[16:19], %[vx0] offset:6144\n"
"v_mfma_f32_16x16x32_bf16 v[84:87], a[140:143], a[4:7], v[84:87]\n"
"ds_read_b128 a[20:23], %[vx0] offset:25600\n"
"v_mfma_f32_16x16x32_bf16 v[88:91], a[140:143], a[8:11], v[88:91]\n"
"ds_read_b128 a[24:27], %[vx0] offset:45056\n"
"v_mfma_f32_16x16x32_bf16 v[92:95], a[140:143], a[12:15], v[92:95]\n"
"s_add_u32 s20, s20, 8192\n"
"s_addc_u32 s21, s21, 0\n"
"global_load_dwordx4 a[64:67], %[vl], s[20:21] offset:-4096\n"
"ds_read_b128 a[28:31], %[vx0] offset:64512\n"
"v_rcp_f32 v235, v235\n"
"s_waitcnt vmcnt(12)\n"
"v_mfma_f32_16x16x32_bf16 v[108:111], a[144:147], a[12:15], v[108:111]\n"
"v_mul_f32 v8, v8, v232\n"
"v_mul_f32 v9, v9, v233\n"
"v_mul_f32 v10, v10, v234\n"
"v_mfma_f32_16x16x32_bf16 v[104:107], a[144:147], a[8:11], v[104:107]\n"
"v_mul_f32 v11, v11, v235\n"
"v_cvt_pk_bf16_f32 v238, v8, v9\n"
"v_mfma_f32_16x16x32_bf16 v[100:103], a[144:147], a[4:7], v[100:103]\n"
"v_cvt_pk_bf16_f32 v239, v10, v11\n"
"v_mul_f32 v232, v24, v229\n"
"v_mul_f32 v233, v25, v229\n"
"v_mfma_f32_16x16x32_bf16 v[96:99], a[144:147], a[0:3], v[96:99]\n"
"global_load_dwordx4 a[68:71], %[vl], s[20:21] offset:-3072\n"
"v_mul_f32 v234, v26, v229\n"
"s_waitcnt vmcnt(12)\n"
"v_mfma_f32_16x16x32_bf16 v[112:115], a[148:151], a[0:3], v[112:115]\n"
"v_mul_f32 v235, v27, v229\n"
"v_exp_f32 v232, v232\n"
"v_mfma_f32_16x16x32_bf16 v[116:119], a[148:151], a[4:7], v[116:119]\n"
"v_exp_f32 v233, v233\n"
"v_mfma_f32_16x16x32_bf16 v[120:123], a[148:151], a[8:11], v[120:123]\n"
"v_exp_f32 v234, v234\n"
"v_mfma_f32_16x16x32_bf16 v[124:127], a[148:151], a[12:15], v[124:127]\n"
"global_load_dwordx4 a[72:75], %[vl], s[20:21] offset:-2048\n"
"v_exp_f32 v235, v235\n"
"s_waitcnt vmcnt(12)\n"
"v_mfma_f32_16x16x32_bf16 v[140:143], a[152:155], a[12:15], v[140:143]\n"
"v_fma_f32 v232, v232, v228, v228\n"
"v_fma_f32 v233, v233, v228, v228\n"
"v_fma_f32 v234, v234, v228, v228\n"
"v_mfma_f32_16x16x32_bf16 v[136:139], a[152:155], a[8:11], v[136:139]\n"
"v_fma_f32 v235, v235, v228, v228\n"
"v_mfma_f32_16x16x32_bf16 v[132:135], a[152:155], a[4:7], v[132:135]\n"
"v_rcp_f32 v232, v232\n"
"v_rcp_f32 v233, v233\n"
"v_mfma_f32_16x16x32_bf16 v[128:131], a[152:155], a[0:3], v[128:131]\n"
"global_load_dwordx4 a[76:79], %[vl], s[20:21] offset:-1024\n"
"s_waitcnt lgkmcnt(0)\n"
"s_waitcnt vmcnt(12)\n"
"v_mfma_f32_16x16x32_bf16 v[80:83], a[156:159], a[16:19], v[80:83]\n"
"ds_read_b128 a[0:3], %[vx0] offset:7168\n"
"v_mfma_f32_16x16x32_bf16 v[84:87], a[156:159], a[20:23], v[84:87]\n"
"ds_read_b128 a[4:7], %[vx0] offset:26624\n"
"v_mfma_f32_16x16x32_bf16 v[88:91], a[156:159], a[24:27], v[88:91]\n"
"ds_read_b128 a[8:11], %[vx0] offset:46080\n"
"v_mfma_f32_16x16x32_bf16 v[92:95], a[156:159], a[28:31], v[92:95]\n"
"global_load_dwordx4 a[80:83], %[vl], s[20:21] offset:0\n"
"ds_read_b128 a[12:15], v246 offset:0\n"
"v_rcp_f32 v234, v234\n"
"s_waitcnt vmcnt(12)\n"
"v_mfma_f32_16x16x32_bf16 v[108:111], a[160:163], a[28:31], v[108:111]\n"
"v_rcp_f32 v235, v235\n"
"v_mul_f32 v24, v24, v232\n"
"v_mfma_f32_16x16x32_bf16 v[104:107], a[160:163], a[24:27], v[104:107]\n"
"v_mul_f32 v25, v25, v233\n"
"v_mul_f32 v26, v26, v234\n"
"v_mfma_f32_16x16x32_bf16 v[100:103], a[160:163], a[20:23], v[100:103]\n"
"v_mul_f32 v27, v27, v235\n"
"v_cvt_pk_bf16_f32 v240, v24, v25\n"
"v_cvt_pk_bf16_f32 v241, v26, v27\n"
"v_mfma_f32_16x16x32_bf16 v[96:99], a[160:163], a[16:19], v[96:99]\n"
"global_load_dwordx4 a[84:87], %[vl], s[20:21] offset:1024\n"
"ds_write_b128 %[vhw], v[238:241] offset:2048\n"
"s_waitcnt vmcnt(12)\n"
"v_mfma_f32_16x16x32_bf16 v[112:115], a[164:167], a[16:19], v[112:115]\n"
"v_mul_f32 v232, v12, v231\n"
"v_mul_f32 v233, v13, v231\n"
"v_mul_f32 v234, v14, v231\n"
"v_mfma_f32_16x16x32_bf16 v[116:119], a[164:167], a[20:23], v[116:119]\n"
"v_mul_f32 v235, v15, v231\n"
"v_mfma_f32_16x16x32_bf16 v[120:123], a[164:167], a[24:27], v[120:123]\n"
"v_exp_f32 v232, v232\n"
"v_exp_f32 v233, v233\n"
"v_mfma_f32_16x16x32_bf16 v[124:127], a[164:167], a[28:31], v[124:127]\n"
"global_load_dwordx4 a[88:91], %[vl], s[20:21] offset:2048\n"
"s_waitcnt vmcnt(12)\n"
"v_mfma_f32_16x16x32_bf16 v[140:143], a[168:171], a[28:31], v[140:143]\n"
"v_exp_f32 v234, v234\n"
"v_exp_f32 v235, v235\n"
"v_mfma_f32_16x16x32_bf16 v[136:139], a[168:171], a[24:27], v[136:139]\n"
"v_fma_f32 v232, v232, v230, v230\n"
"v_fma_f32 v233, v233, v230, v230\n"
"v_mfma_f32_16x16x32_bf16 v[132:135], a[168:171], a[20:23], v[132:135]\n"
"v_fma_f32 v234, v234, v230, v230\n"
"v_fma_f32 v235, v235, v230, v230\n"
"v_mfma_f32_16x16x32_bf16 v[128:131], a[168:171], a[16:19], v[128:131]\n"
"global_load_dwordx4 a[92:95], %[vl], s[20:21] offset:3072\n"
"v_rcp_f32 v232, v232\n"
"s_waitcnt lgkmcnt(1)\n"
"s_waitcnt vmcnt(12)\n"
"v_mfma_f32_16x16x32_bf16 v[80:83], a[172:175], a[0:3], v[80:83]\n"
"ds_read_b128 a[16:19], %[vx0] offset:8192\n"
"v_mfma_f32_16x16x32_bf16 v[84:87], a[172:175], a[4:7], v[84:87]\n"
"ds_read_b128 a[20:23], %[vx0] offset:27648\n"
"v_mfma_f32_16x16x32_bf16 v[88:91], a[172:175], a[8:11], v[88:91]\n"
"ds_read_b128 a[24:27], %[vx0] offset:47104\n"
"v_mfma_f32_16x16x32_bf16 v[92:95], a[172:175], a[12:15], v[92:95]\n"
"s_add_u32 s20, s20, 8192\n"
"s_addc_u32 s21, s21, 0\n"
"global_load_dwordx4 a[96:99], %[vl], s[20:21] offset:-4096\n"
"ds_read_b128 a[28:31], v246 offset:1024\n"
"v_rcp_f32 v233, v233\n"
"s_waitcnt vmcnt(12)\n"
"v_mfma_f32_16x16x32_bf16 v[108:111], a[176:179], a[12:15], v[108:111]\n"
"v_rcp_f32 v234, v234\n"
"v_mfma_f32_16x16x32_bf16 v[104:107], a[176:179], a[8:11], v[104:107]\n"
"v_rcp_f32 v235, v235\n"
"v_mul_f32 v12, v12, v232\n"
"v_mfma_f32_16x16x32_bf16 v[100:103], a[176:179], a[4:7], v[100:103]\n"
"v_mul_f32 v13, v13, v233\n"
"v_mul_f32 v14, v14, v234\n"
"v_mul_f32 v15, v15, v235\n"
"v_mfma_f32_16x16x32_bf16 v[96:99], a[176:179], a[0:3], v[96:99]\n"
"global_load_dwordx4 a[100:103], %[vl], s[20:21] offset:-3072\n"
"v_cvt_pk_bf16_f32 v242, v12, v13\n"
"s_waitcnt vmcnt(12)\n"
"v_mfma_f32_16x16x32_bf16 v[112:115], a[180:183], a[0:3], v[112:115]\n"
"v_cvt_pk_bf16_f32 v243, v14, v15\n"
"v_mul_f32 v232, v28, v231\n"
"v_mul_f32 v233, v29, v231\n"
"v_mfma_f32_16x16x32_bf16 v[116:119], a[180:183], a[4:7], v[116:119]\n"
"v_mul_f32 v234, v30, v231\n"
"v_mul_f32 v235, v31, v231\n"
"v_mfma_f32_16x16x32_bf16 v[120:123], a[180:183], a[8:11], v[120:123]\n"
"v_exp_f32 v232, v232\n"
"v_mfma_f32_16x16x32_bf16 v[124:127], a[180:183], a[12:15], v[124:127]\n"
"global_load_dwordx4 a[104:107], %[vl], s[20:21] offset:-2048\n"
"v_exp_f32 v233, v233\n"
"s_waitcnt vmcnt(12)\n"
"v_mfma_f32_16x16x32_bf16 v[140:143], a[184:187], a[12:15], v[140:143]\n"
"v_exp_f32 v234, v234\n"
"v_mfma_f32_16x16x32_bf16 v[136:139], a[184:187], a[8:11], v[136:139]\n"
"v_exp_f32 v235, v235\n"
"v_fma_f32 v232, v232, v230, v230\n"
"v_mfma_f32_16x16x32_bf16 v[132:135], a[184:187], a[4:7], v[132:135]\n"
"v_fma_f32 v233, v233, v230, v230\n"
"v_fma_f32 v234, v234, v230, v230\n"
"v_fma_f32 v235, v235, v230, v230\n"
"v_mfma_f32_16x16x32_bf16 v[128:131], a[184:187], a[0:3], v[128:131]\n"
"global_load_dwordx4 a[108:111], %[vl], s[20:21] offset:-1024\n"
"s_waitcnt lgkmcnt(0)\n"
"s_waitcnt vmcnt(12)\n"
"v_mfma_f32_16x16x32_bf16 v[80:83], a[188:191], a[16:19], v[80:83]\n"
"ds_read_b128 a[0:3], %[vx0] offset:9216\n"
"v_mfma_f32_16x16x32_bf16 v[84:87], a[188:191], a[20:23], v[84:87]\n"
"ds_read_b128 a[4:7], %[vx0] offset:28672\n"
"v_mfma_f32_16x16x32_bf16 v[88:91], a[188:191], a[24:27], v[88:91]\n"
"ds_read_b128 a[8:11], %[vx0] offset:48128\n"
"v_mfma_f32_16x16x32_bf16 v[92:95], a[188:191], a[28:31], v[92:95]\n"
"global_load_dwordx4 a[112:115], %[vl], s[20:21] offset:0\n"
"ds_read_b128 a[12:15], v246 offset:2048\n"
"v_rcp_f32 v232, v232\n"
"s_waitcnt vmcnt(12)\n"
"v_mfma_f32_16x16x32_bf16 v[108:111], a[64:67], a[28:31], v[108:111]\n"
"v_rcp_f32 v233, v233\n"
"v_mfma_f32_16x16x32_bf16 v[104:107], a[64:67], a[24:27], v[104:107]\n"
"v_rcp_f32 v234, v234\n"
"v_mfma_f32_16x16x32_bf16 v[100:103], a[64:67], a[20:23], v[100:103]\n"
"v_rcp_f32 v235, v235\n"
"v_mul_f32 v28, v28, v232\n"
"v_mul_f32 v29, v29, v233\n"
"v_mfma_f32_16x16x32_bf16 v[96:99], a[64:67], a[16:19], v[96:99]\n"
"global_load_dwordx4 a[116:119], %[vl], s[20:21] offset:1024\n"
"v_mul_f32 v30, v30, v234\n"
"s_waitcnt vmcnt(12)\n"
"v_mfma_f32_16x16x32_bf16 v[112:115], a[68:71], a[16:19], v[112:115]\n"
"v_mul_f32 v31, v31, v235\n"
"v_cvt_pk_bf16_f32 v244, v28, v29\n"
"v_cvt_pk_bf16_f32 v245, v30, v31\n"
"v_mfma_f32_16x16x32_bf16 v[116:119], a[68:71], a[20:23], v[116:119]\n"
"ds_write_b128 %[vhw], v[242:245] offset:3072\n"
"v_mul_f32 v232, v32, v225\n"
"v_mfma_f32_16x16x32_bf16 v[120:123], a[68:71], a[24:27], v[120:123]\n"
"v_mul_f32 v233, v33, v225\n"
"v_mul_f32 v234, v34, v225\n"
"v_mul_f32 v235, v35, v225\n"
"v_mfma_f32_16x16x32_bf16 v[124:127], a[68:71], a[28:31], v[124:127]\n"
"global_load_dwordx4 a[120:123], %[vl], s[20:21] offset:2048\n"
"s_waitcnt vmcnt(12)\n"
"v_mfma_f32_16x16x32_bf16 v[140:143], a[72:75], a[28:31], v[140:143]\n"
"v_exp_f32 v232, v232\n"
"v_exp_f32 v233, v233\n"
"v_mfma_f32_16x16x32_bf16 v[136:139], a[72:75], a[24:27], v[136:139]\n"
"v_exp_f32 v234, v234\n"
"v_mfma_f32_16x16x32_bf16 v[132:135], a[72:75], a[20:23], v[132:135]\n"
"v_exp_f32 v235, v235\n"
"v_fma_f32 v232, v232, v224, v224\n"
"v_mfma_f32_16x16x32_bf16 v[128:131], a[72:75], a[16:19], v[128:131]\n"
"global_load_dwordx4 a[124:127], %[vl], s[20:21] offset:3072\n"
"v_fma_f32 v233, v233, v224, v224\n"
"s_waitcnt lgkmcnt(1)\n"
"s_waitcnt vmcnt(12)\n"
"v_mfma_f32_16x16x32_bf16 v[80:83], a[76:79], a[0:3], v[80:83]\n"
"ds_read_b128 a[16:19], %[vx0] offset:10240\n"
"v_mfma_f32_16x16x32_bf16 v[84:87], a[76:79], a[4:7], v[84:87]\n"
"ds_read_b128 a[20:23], %[vx0] offset:29696\n"
"v_mfma_f32_16x16x32_bf16 v[88:91], a[76:79], a[8:11], v[88:91]\n"
"ds_read_b128 a[24:27], %[vx0] offset:49152\n"
"v_mfma_f32_16x16x32_bf16 v[92:95], a[76:79], a[12:15], v[92:95]\n"
"s_add_u32 s20, s20, 8192\n"
"s_addc_u32 s21, s21, 0\n"
"global_load_dwordx4 a[128:131], %[vl], s[20:21] offset:-4096\n"
"ds_read_b128 a[28:31], v246 offset:3072\n"
"v_fma_f32 v234, v234, v224, v224\n"
"v_fma_f32 v235, v235, v224, v224\n"
"s_waitcnt vmcnt(12)\n"
"v_mfma_f32_16x16x32_bf16 v[108:111], a[80:83], a[12:15], v[108:111]\n"
"v_rcp_f32 v232, v232\n"
"v_mfma_f32_16x16x32_bf16 v[104:107], a[80:83], a[8:11], v[104:107]\n"
"v_rcp_f32 v233, v233\n"
"v_mfma_f32_16x16x32_bf16 v[100:103], a[80:83], a[4:7], v[100:103]\n"
"v_rcp_f32 v234, v234\n"
"v_rcp_f32 v235, v235\n"
"v_mfma_f32_16x16x32_bf16 v[96:99], a[80:83], a[0:3], v[96:99]\n"
"global_load_dwordx4 a[132:135], %[vl], s[20:21] offset:-3072\n"
"v_mul_f32 v32, v32, v232\n"
"s_waitcnt vmcnt(12)\n"
"v_mfma_f32_16x16x32_bf16 v[112:115], a[84:87], a[0:3], v[112:115]\n"
"v_mul_f32 v33, v33, v233\n"
"v_mul_f32 v34, v34, v234\n"
"v_mul_f32 v35, v35, v235\n"
"v_mfma_f32_16x16x32_bf16 v[116:119], a[84:87], a[4:7], v[116:119]\n"
"v_cvt_pk_bf16_f32 v238, v32, v33\n"
"v_cvt_pk_bf16_f32 v239, v34, v35\n"
"v_mfma_f32_16x16x32_bf16 v[120:123], a[84:87], a[8:11], v[120:123]\n"
"v_mul_f32 v232, v48, v225\n"
"v_mul_f32 v233, v49, v225\n"
"v_mul_f32 v234, v50, v225\n"
"v_mfma_f32_16x16x32_bf16 v[124:127], a[84:87], a[12:15], v[124:127]\n"
"global_load_dwordx4 a[136:139], %[vl], s[20:21] offset:-2048\n"
"v_mul_f32 v235, v51, v225\n"
"s_waitcnt vmcnt(12)\n"
"v_mfma_f32_16x16x32_bf16 v[140:143], a[88:91], a[12:15], v[140:143]\n"
"v_exp_f32 v232, v232\n"
"v_mfma_f32_16x16x32_bf16 v[136:139], a[88:91], a[8:11], v[136:139]\n"
"v_exp_f32 v233, v233\n"
"v_mfma_f32_16x16x32_bf16 v[132:135], a[88:91], a[4:7], v[132:135]\n"
"v_exp_f32 v234, v234\n"
"v_exp_f32 v235, v235\n"
"v_mfma_f32_16x16x32_bf16 v[128:131], a[88:91], a[0:3], v[128:131]\n"
"global_load_dwordx4 a[140:143], %[vl], s[20:21] offset:-1024\n"
"v_fma_f32 v232, v232, v224, v224\n"
"s_waitcnt lgkmcnt(0)\n"
"s_waitcnt vmcnt(12)\n"
"v_mfma_f32_16x16x32_bf16 v[80:83], a[92:95], a[16:19], v[80:83]\n"
"ds_read_b128 a[0:3], %[vx0] offset:11264\n"
"v_mfma_f32_16x16x32_bf16 v[84:87], a[92:95], a[20:23], v[84:87]\n"
"ds_read_b128 a[4:7], %[vx0] offset:30720\n"
"v_mfma_f32_16x16x32_bf16 v[88:91], a[92:95], a[24:27], v[88:91]\n"
"ds_read_b128 a[8:11], %[vx0] offset:50176\n"
"v_mfma_f32_16x16x32_bf16 v[92:95], a[92:95], a[28:31], v[92:95]\n"
"global_load_dwordx4 a[144:147], %[vl], s[20:21] offset:0\n"
"ds_read_b128 a[12:15], v246 offset:4096\n"
"v_fma_f32 v233, v233, v224, v224\n"
"v_fma_f32 v234, v234, v224, v224\n"
"s_waitcnt vmcnt(12)\n"
"v_mfma_f32_16x16x32_bf16 v[108:111], a[96:99], a[28:31], v[108:111]\n"
"v_fma_f32 v235, v235, v224, v224\n"
"v_rcp_f32 v232, v232\n"
"v_mfma_f32_16x16x32_bf16 v[104:107], a[96:99], a[24:27], v[104:107]\n"
"v_rcp_f32 v233, v233\n"
"v_mfma_f32_16x16x32_bf16 v[100:103], a[96:99], a[20:23], v[100:103]\n"
"v_rcp_f32 v234, v234\n"
"v_mfma_f32_16x16x32_bf16 v[96:99], a[96:99], a[16:19], v[96:99]\n"
"global_load_dwordx4 a[148:151], %[vl], s[20:21] offset:1024\n"
"v_rcp_f32 v235, v235\n"
"s_waitcnt vmcnt(12)\n"
"v_mfma_f32_16x16x32_bf16 v[112:115], a[100:103], a[16:19], v[112:115]\n"
"v_mul_f32 v48, v48, v232\n"
"v_mul_f32 v49, v49, v233\n"
"v_mul_f32 v50, v50, v234\n"
"v_mfma_f32_16x16x32_bf16 v[116:119], a[100:103], a[20:23], v[116:119]\n"
"v_mul_f32 v51, v51, v235\n"
"v_cvt_pk_bf16_f32 v240, v48, v49\n"
"v_mfma_f32_16x16x32_bf16 v[120:123], a[100:103], a[24:27], v[120:123]\n"
"v_cvt_pk_bf16_f32 v241, v50, v51\n"
"ds_write_b128 %[vhw], v[238:241] offset:4096\n"
"v_mul_f32 v232, v36, v227\n"
"v_mfma_f32_16x16x32_bf16 v[124:127], a[100:103], a[28:31], v[124:127]\n"
"global_load_dwordx4 a[152:155], %[vl], s[20:21] offset:2048\n"
"v_mul_f32 v233, v37, v227\n"
"s_waitcnt vmcnt(12)\n"
"v_mfma_f32_16x16x32_bf16 v[140:143], a[104:107], a[28:31], v[140:143]\n"
"v_mul_f32 v234, v38, v227\n"
"v_mul_f32 v235, v39, v227\n"
"v_mfma_f32_16x16x32_bf16 v[136:139], a[104:107], a[24:27], v[136:139]\n"
"v_exp_f32 v232, v232\n"
"v_mfma_f32_16x16x32_bf16 v[132:135], a[104:107], a[20:23], v[132:135]\n"
"v_exp_f32 v233, v233\n"
"v_exp_f32 v234, v234\n"
"v_mfma_f32_16x16x32_bf16 v[128:131], a[104:107], a[16:19], v[128:131]\n"
"global_load_dwordx4 a[156:159], %[vl], s[20:21] offset:3072\n"
"s_waitcnt lgkmcnt(1)\n"
"s_waitcnt vmcnt(12)\n"
"v_mfma_f32_16x16x32_bf16 v[80:83], a[108:111], a[0:3], v[80:83]\n"
"ds_read_b128 a[16:19], %[vx0] offset:12288\n"
"v_mfma_f32_16x16x32_bf16 v[84:87], a[108:111], a[4:7], v[84:87]\n"
"ds_read_b128 a[20:23], %[vx0] offset:31744\n"
"v_mfma_f32_16x16x32_bf16 v[88:91], a[108:111], a[8:11], v[88:91]\n"
"ds_read_b128 a[24:27], %[vx0] offset:51200\n"
"v_mfma_f32_16x16x32_bf16 v[92:95], a[108:111], a[12:15], v[92:95]\n"
"s_add_u32 s20, s20, 8192\n"
"s_addc_u32 s21, s21, 0\n"
"global_load_dwordx4 a[160:163], %[vl], s[20:21] offset:-4096\n"
"ds_read_b128 a[28:31], v246 offset:5120\n"
"v_exp_f32 v235, v235\n"
"s_waitcnt vmcnt(12)\n"
"v_mfma_f32_16x16x32_bf16 v[108:111], a[112:115], a[12:15], v[108:111]\n"
"v_fma_f32 v232, v232, v226, v226\n"
"v_fma_f32 v233, v233, v226, v226\n"
"v_fma_f32 v234, v234, v226, v226\n"
"v_mfma_f32_16x16x32_bf16 v[104:107], a[112:115], a[8:11], v[104:107]\n"
"v_fma_f32 v235, v235, v226, v226\n"
"v_mfma_f32_16x16x32_bf16 v[100:103], a[112:115], a[4:7], v[100:103]\n"
"v_rcp_f32 v232, v232\n"
"v_rcp_f32 v233, v233\n"
"v_mfma_f32_16x16x32_bf16 v[96:99], a[112:115], a[0:3], v[96:99]\n"
"global_load_dwordx4 a[164:167], %[vl], s[20:21] offset:-3072\n"
"s_waitcnt vmcnt(12)\n"
"v_mfma_f32_16x16x32_bf16 v[112:115], a[116:119], a[0:3], v[112:115]\n"
"v_rcp_f32 v234, v234\n"
"v_rcp_f32 v235, v235\n"
"v_mfma_f32_16x16x32_bf16 v[116:119], a[116:119], a[4:7], v[116:119]\n"
"v_mul_f32 v36, v36, v232\n"
"v_mul_f32 v37, v37, v233\n"
"v_mfma_f32_16x16x32_bf16 v[120:123], a[116:119], a[8:11], v[120:123]\n"
"v_mul_f32 v38, v38, v234\n"
"v_mul_f32 v39, v39, v235\n"
"v_cvt_pk_bf16_f32 v242, v36, v37\n"
"v_mfma_f32_16x16x32_bf16 v[124:127], a[116:119], a[12:15], v[124:127]\n"
"global_load_dwordx4 a[168:171], %[vl], s[20:21] offset:-2048\n"
"v_cvt_pk_bf16_f32 v243, v38, v39\n"
"s_waitcnt vmcnt(12)\n"
"v_mfma_f32_16x16x32_bf16 v[140:143], a[120:123], a[12:15], v[140:143]\n"
"v_mul_f32 v232, v52, v227\n"
"v_mul_f32 v233, v53, v227\n"
"v_mul_f32 v234, v54, v227\n"
"v_mfma_f32_16x16x32_bf16 v[136:139], a[120:123], a[8:11], v[136:139]\n"
"v_mul_f32 v235, v55, v227\n"
"v_mfma_f32_16x16x32_bf16 v[132:135], a[120:123], a[4:7], v[132:135]\n"
"v_exp_f32 v232, v232\n"
"v_exp_f32 v233, v233\n"
"v_mfma_f32_16x16x32_bf16 v[128:131], a[120:123], a[0:3], v[128:131]\n"
"global_load_dwordx4 a[172:175], %[vl], s[20:21] offset:-1024\n"
"s_waitcnt lgkmcnt(0)\n"
"s_waitcnt vmcnt(12)\n"
"v_mfma_f32_16x16x32_bf16 v[80:83], a[124:127], a[16:19], v[80:83]\n"
"ds_read_b128 a[0:3], %[vx0] offset:13312\n"
"v_mfma_f32_16x16x32_bf16 v[84:87], a[124:127], a[20:23], v[84:87]\n"
"ds_read_b128 a[4:7], %[vx0] offset:32768\n"
"v_mfma_f32_16x16x32_bf16 v[88:91], a[124:127], a[24:27], v[88:91]\n"
"ds_read_b128 a[8:11], %[vx0] offset:52224\n"
"v_mfma_f32_16x16x32_bf16 v[92:95], a[124:127], a[28:31], v[92:95]\n"
"global_load_dwordx4 a[176:179], %[vl], s[20:21] offset:0\n"
"ds_read_b128 a[12:15], v246 offset:6144\n"
"v_exp_f32 v234, v234\n"
"s_waitcnt vmcnt(12)\n"
"v_mfma_f32_16x16x32_bf16 v[108:111], a[128:131], a[28:31], v[108:111]\n"
"v_exp_f32 v235, v235\n"
"v_fma_f32 v232, v232, v226, v226\n"
"v_mfma_f32_16x16x32_bf16 v[104:107], a[128:131], a[24:27], v[104:107]\n"
"v_fma_f32 v233, v233, v226, v226\n"
"v_fma_f32 v234, v234, v226, v226\n"
"v_mfma_f32_16x16x32_bf16 v[100:103], a[128:131], a[20:23], v[100:103]\n"
"v_fma_f32 v235, v235, v226, v226\n"
"v_rcp_f32 v232, v232\n"
"v_mfma_f32_16x16x32_bf16 v[96:99], a[128:131], a[16:19], v[96:99]\n"
"global_load_dwordx4 a[180:183], %[vl], s[20:21] offset:1024\n"
"s_waitcnt vmcnt(12)\n"
"v_mfma_f32_16x16x32_bf16 v[112:115], a[132:135], a[16:19], v[112:115]\n"
"v_rcp_f32 v233, v233\n"
"v_rcp_f32 v234, v234\n"
"v_mfma_f32_16x16x32_bf16 v[116:119], a[132:135], a[20:23], v[116:119]\n"
"v_rcp_f32 v235, v235\n"
"v_mfma_f32_16x16x32_bf16 v[120:123], a[132:135], a[24:27], v[120:123]\n"
"v_mul_f32 v52, v52, v232\n"
"v_mul_f32 v53, v53, v233\n"
"v_mul_f32 v54, v54, v234\n"
"v_mfma_f32_16x16x32_bf16 v[124:127], a[132:135], a[28:31], v[124:127]\n"
"global_load_dwordx4 a[184:187], %[vl], s[20:21] offset:2048\n"
"v_mul_f32 v55, v55, v235\n"
"s_waitcnt vmcnt(12)\n"
"v_mfma_f32_16x16x32_bf16 v[140:143], a[136:139], a[28:31], v[140:143]\n"
"v_cvt_pk_bf16_f32 v244, v52, v53\n"
"v_cvt_pk_bf16_f32 v245, v54, v55\n"
"ds_write_b128 %[vhw], v[242:245] offset:5120\n"
"v_mfma_f32_16x16x32_bf16 v[136:139], a[136:139], a[24:27], v[136:139]\n"
"v_mul_f32 v232, v40, v229\n"
"v_mul_f32 v233, v41, v229\n"
"v_mfma_f32_16x16x32_bf16 v[132:135], a[136:139], a[20:23], v[132:135]\n"
"v_mul_f32 v234, v42, v229\n"
"v_mul_f32 v235, v43, v229\n"
"v_mfma_f32_16x16x32_bf16 v[128:131], a[136:139], a[16:19], v[128:131]\n"
"global_load_dwordx4 a[188:191], %[vl], s[20:21] offset:3072\n"
"v_exp_f32 v232, v232\n"
"s_waitcnt lgkmcnt(1)\n"
"s_waitcnt vmcnt(12)\n"
"v_mfma_f32_16x16x32_bf16 v[80:83], a[140:143], a[0:3], v[80:83]\n"
"ds_read_b128 a[16:19], %[vx0] offset:14336\n"
"v_mfma_f32_16x16x32_bf16 v[84:87], a[140:143], a[4:7], v[84:87]\n"
"ds_read_b128 a[20:23], %[vx0] offset:33792\n"
"v_mfma_f32_16x16x32_bf16 v[88:91], a[140:143], a[8:11], v[88:91]\n"
"ds_read_b128 a[24:27], %[vx0] offset:53248\n"
"v_mfma_f32_16x16x32_bf16 v[92:95], a[140:143], a[12:15], v[92:95]\n"
"s_add_u32 s20, s20, 8192\n"
"s_addc_u32 s21, s21, 0\n"
"global_load_dwordx4 a[64:67], %[vl], s[20:21] offset:-4096\n"
"ds_read_b128 a[28:31], v246 offset:7168\n"
"v_exp_f32 v233, v233\n"
"s_waitcnt vmcnt(12)\n"
"v_mfma_f32_16x16x32_bf16 v[108:111], a[144:147], a[12:15], v[108:111]\n"
"v_exp_f32 v234, v234\n"
"v_mfma_f32_16x16x32_bf16 v[104:107], a[144:147], a[8:11], v[104:107]\n"
"v_exp_f32 v235, v235\n"
"v_fma_f32 v232, v232, v228, v228\n"
"v_mfma_f32_16x16x32_bf16 v[100:103], a[144:147], a[4:7], v[100:103]\n"
"v_fma_f32 v233, v233, v228, v228\n"
"v_fma_f32 v234, v234, v228, v228\n"
"v_fma_f32 v235, v235, v228, v228\n"
"v_mfma_f32_16x16x32_bf16 v[96:99], a[144:147], a[0:3], v[96:99]\n"
"global_load_dwordx4 a[68:71], %[vl], s[20:21] offset:-3072\n"
"s_waitcnt vmcnt(12)\n"
"v_mfma_f32_16x16x32_bf16 v[112:115], a[148:151], a[0:3], v[112:115]\n"
"v_rcp_f32 v232, v232\n"
"v_rcp_f32 v233, v233\n"
"v_mfma_f32_16x16x32_bf16 v[116:119], a[148:151], a[4:7], v[116:119]\n"
"v_rcp_f32 v234, v234\n"
"v_mfma_f32_16x16x32_bf16 v[120:123], a[148:151], a[8:11], v[120:123]\n"
"v_rcp_f32 v235, v235\n"
"v_mul_f32 v40, v40, v232\n"
"v_mfma_f32_16x16x32_bf16 v[124:127], a[148:151], a[12:15], v[124:127]\n"
"global_load_dwordx4 a[72:75], %[vl], s[20:21] offset:-2048\n"
"v_mul_f32 v41, v41, v233\n"
"s_waitcnt vmcnt(12)\n"
"v_mfma_f32_16x16x32_bf16 v[140:143], a[152:155], a[12:15], v[140:143]\n"
"v_mul_f32 v42, v42, v234\n"
"v_mul_f32 v43, v43, v235\n"
"v_cvt_pk_bf16_f32 v238, v40, v41\n"
"v_mfma_f32_16x16x32_bf16 v[136:139], a[152:155], a[8:11], v[136:139]\n"
"v_cvt_pk_bf16_f32 v239, v42, v43\n"
"v_mul_f32 v232, v56, v229\n"
"v_mfma_f32_16x16x32_bf16 v[132:135], a[152:155], a[4:7], v[132:135]\n"
"v_mul_f32 v233, v57, v229\n"
"v_mul_f32 v234, v58, v229\n"
"v_mul_f32 v235, v59, v229\n"
"v_mfma_f32_16x16x32_bf16 v[128:131], a[152:155], a[0:3], v[128:131]\n"
"global_load_dwordx4 a[76:79], %[vl], s[20:21] offset:-1024\n"
"s_waitcnt lgkmcnt(0)\n"
"s_waitcnt vmcnt(12)\n"
"v_mfma_f32_16x16x32_bf16 v[80:83], a[156:159], a[16:19], v[80:83]\n"
"ds_read_b128 a[0:3], %[vx0] offset:15360\n"
"v_mfma_f32_16x16x32_bf16 v[84:87], a[156:159], a[20:23], v[84:87]\n"
"ds_read_b128 a[4:7], %[vx0] offset:34816\n"
"v_mfma_f32_16x16x32_bf16 v[88:91], a[156:159], a[24:27], v[88:91]\n"
"ds_read_b128 a[8:11], %[vx0] offset:54272\n"
"v_mfma_f32_16x16x32_bf16 v[92:95], a[156:159], a[28:31], v[92:95]\n"
"global_load_dwordx4 a[80:83], %[vl], s[20:21] offset:0\n"
"ds_read_b128 a[12:15], v246 offset:8192\n"
"v_exp_f32 v232, v232\n"
"s_waitcnt vmcnt(12)\n"
"v_mfma_f32_16x16x32_bf16 v[108:111], a[160:163], a[28:31], v[108:111]\n"
"v_exp_f32 v233, v233\n"
"v_mfma_f32_16x16x32_bf16 v[104:107], a[160:163], a[24:27], v[104:107]\n"
"v_exp_f32 v234, v234\n"
"v_mfma_f32_16x16x32_bf16 v[100:103], a[160:163], a[20:23], v[100:103]\n"
"v_exp_f32 v235, v235\n"
"v_fma_f32 v232, v232, v228, v228\n"
"v_fma_f32 v233, v233, v228, v228\n"
"v_mfma_f32_16x16x32_bf16 v[96:99], a[160:163], a[16:19], v[96:99]\n"
"global_load_dwordx4 a[84:87], %[vl], s[20:21] offset:1024\n"
"v_fma_f32 v234, v234, v228, v228\n"
"s_waitcnt vmcnt(12)\n"
"v_mfma_f32_16x16x32_bf16 v[112:115], a[164:167], a[16:19], v[112:115]\n"
"v_fma_f32 v235, v235, v228, v228\n"
"v_rcp_f32 v232, v232\n"
"v_mfma_f32_16x16x32_bf16 v[116:119], a[164:167], a[20:23], v[116:119]\n"
"v_rcp_f32 v233, v233\n"
"v_mfma_f32_16x16x32_bf16 v[120:123], a[164:167], a[24:27], v[120:123]\n"
"v_rcp_f32 v234, v234\n"
"v_mfma_f32_16x16x32_bf16 v[124:127], a[164:167], a[28:31], v[124:127]\n"
"global_load_dwordx4 a[88:91], %[vl], s[20:21] offset:2048\n"
"v_rcp_f32 v235, v235\n"
"s_waitcnt vmcnt(12)\n"
"v_mfma_f32_16x16x32_bf16 v[140:143], a[168:171], a[28:31], v[140:143]\n"
"v_mul_f32 v56, v56, v232\n"
"v_mul_f32 v57, v57, v233\n"
"v_mul_f32 v58, v58, v234\n"
"v_mfma_f32_16x16x32_bf16 v[136:139], a[168:171], a[24:27], v[136:139]\n"
"v_mul_f32 v59, v59, v235\n"
"v_cvt_pk_bf16_f32 v240, v56, v57\n"
"v_mfma_f32_16x16x32_bf16 v[132:135], a[168:171], a[20:23], v[132:135]\n"
"v_cvt_pk_bf16_f32 v241, v58, v59\n"
"ds_write_b128 %[vhw], v[238:241] offset:6144\n"
"v_mul_f32 v232, v44, v231\n"
"v_mfma_f32_16x16x32_bf16 v[128:131], a[168:171], a[16:19], v[128:131]\n"
"global_load_dwordx4 a[92:95], %[vl], s[20:21] offset:3072\n"
"v_mul_f32 v233, v45, v231\n"
"s_waitcnt lgkmcnt(1)\n"
"s_waitcnt vmcnt(12)\n"
"v_mfma_f32_16x16x32_bf16 v[80:83], a[172:175], a[0:3], v[80:83]\n"
"ds_read_b128 a[16:19], %[vx0] offset:16384\n"
"v_mfma_f32_16x16x32_bf16 v[84:87], a[172:175], a[4:7], v[84:87]\n"
"ds_read_b128 a[20:23], %[vx0] offset:35840\n"
"v_mfma_f32_16x16x32_bf16 v[88:91], a[172:175], a[8:11], v[88:91]\n"
"ds_read_b128 a[24:27], %[vx0] offset:55296\n"
"v_mfma_f32_16x16x32_bf16 v[92:95], a[172:175], a[12:15], v[92:95]\n"
"s_add_u32 s20, s20, 8192\n"
"s_addc_u32 s21, s21, 0\n"
"global_load_dwordx4 a[96:99], %[vl], s[20:21] offset:-4096\n"
"ds_read_b128 a[28:31], v246 offset:9216\n"
"v_mul_f32 v234, v46, v231\n"
"v_mul_f32 v235, v47, v231\n"
"s_waitcnt vmcnt(12)\n"
"v_mfma_f32_16x16x32_bf16 v[108:111], a[176:179], a[12:15], v[108:111]\n"
"v_exp_f32 v232, v232\n"
"v_mfma_f32_16x16x32_bf16 v[104:107], a[176:179], a[8:11], v[104:107]\n"
"v_exp_f32 v233, v233\n"
"v_mfma_f32_16x16x32_bf16 v[100:103], a[176:179], a[4:7], v[100:103]\n"
"v_exp_f32 v234, v234\n"
"v_exp_f32 v235, v235\n"
"v_mfma_f32_16x16x32_bf16 v[96:99], a[176:179], a[0:3], v[96:99]\n"
"global_load_dwordx4 a[100:103], %[vl], s[20:21] offset:-3072\n"
"v_fma_f32 v232, v232, v230, v230\n"
"s_waitcnt vmcnt(12)\n"
"v_mfma_f32_16x16x32_bf16 v[112:115], a[180:183], a[0:3], v[112:115]\n"
"v_fma_f32 v233, v233, v230, v230\n"
"v_fma_f32 v234, v234, v230, v230\n"
"v_fma_f32 v235, v235, v230, v230\n"
"v_mfma_f32_16x16x32_bf16 v[116:119], a[180:183], a[4:7], v[116:119]\n"
"v_rcp_f32 v232, v232\n"
"v_mfma_f32_16x16x32_bf16 v[120:123], a[180:183], a[8:11], v[120:123]\n"
"v_rcp_f32 v233, v233\n"
"v_mfma_f32_16x16x32_bf16 v[124:127], a[180:183], a[12:15], v[124:127]\n"
"global_load_dwordx4 a[104:107], %[vl], s[20:21] offset:-2048\n"
"v_rcp_f32 v234, v234\n"
"s_waitcnt vmcnt(12)\n"
"v_mfma_f32_16x16x32_bf16 v[140:143], a[184:187], a[12:15], v[140:143]\n"
"v_rcp_f32 v235, v235\n"
"v_mul_f32 v44, v44, v232\n"
"v_mfma_f32_16x16x32_bf16 v[136:139], a[184:187], a[8:11], v[136:139]\n"
"v_mul_f32 v45, v45, v233\n"
"v_mul_f32 v46, v46, v234\n"
"v_mfma_f32_16x16x32_bf16 v[132:135], a[184:187], a[4:7], v[132:135]\n"
"v_mul_f32 v47, v47, v235\n"
"v_cvt_pk_bf16_f32 v242, v44, v45\n"
"v_cvt_pk_bf16_f32 v243, v46, v47\n"
"v_mfma_f32_16x16x32_bf16 v[128:131], a[184:187], a[0:3], v[128:131]\n"
"global_load_dwordx4 a[108:111], %[vl], s[20:21] offset:-1024\n"
"v_mul_f32 v232, v60, v231\n"
"s_waitcnt lgkmcnt(0)\n"
"s_waitcnt vmcnt(12)\n"
"v_mfma_f32_16x16x32_bf16 v[80:83], a[188:191], a[16:19], v[80:83]\n"
"ds_read_b128 a[0:3], %[vx0] offset:17408\n"
"v_mfma_f32_16x16x32_bf16 v[84:87], a[188:191], a[20:23], v[84:87]\n"
"ds_read_b128 a[4:7], %[vx0] offset:36864\n"
"v_mfma_f32_16x16x32_bf16 v[88:91], a[188:191], a[24:27], v[88:91]\n"
"ds_read_b128 a[8:11], %[vx0] offset:56320\n"
"v_mfma_f32_16x16x32_bf16 v[92:95], a[188:191], a[28:31], v[92:95]\n"
"global_load_dwordx4 a[112:115], %[vl], s[20:21] offset:0\n"
"ds_read_b128 a[12:15], v246 offset:10240\n"
"v_mul_f32 v233, v61, v231\n"
"v_mul_f32 v234, v62, v231\n"
"s_waitcnt vmcnt(12)\n"
"v_mfma_f32_16x16x32_bf16 v[108:111], a[64:67], a[28:31], v[108:111]\n"
"v_mul_f32 v235, v63, v231\n"
"v_exp_f32 v232, v232\n"
"v_mfma_f32_16x16x32_bf16 v[104:107], a[64:67], a[24:27], v[104:107]\n"
"v_exp_f32 v233, v233\n"
"v_mfma_f32_16x16x32_bf16 v[100:103], a[64:67], a[20:23], v[100:103]\n"
"v_exp_f32 v234, v234\n"
"v_mfma_f32_16x16x32_bf16 v[96:99], a[64:67], a[16:19], v[96:99]\n"
"global_load_dwordx4 a[116:119], %[vl], s[20:21] offset:1024\n"
"v_exp_f32 v235, v235\n"
"s_waitcnt vmcnt(12)\n"
"v_mfma_f32_16x16x32_bf16 v[112:115], a[68:71], a[16:19], v[112:115]\n"
"v_fma_f32 v232, v232, v230, v230\n"
"v_fma_f32 v233, v233, v230, v230\n"
"v_fma_f32 v234, v234, v230, v230\n"
"v_mfma_f32_16x16x32_bf16 v[116:119], a[68:71], a[20:23], v[116:119]\n"
"v_fma_f32 v235, v235, v230, v230\n"
"v_mfma_f32_16x16x32_bf16 v[120:123], a[68:71], a[24:27], v[120:123]\n"
"v_rcp_f32 v232, v232\n"
"v_rcp_f32 v233, v233\n"
"v_mfma_f32_16x16x32_bf16 v[124:127], a[68:71], a[28:31], v[124:127]\n"
"global_load_dwordx4 a[120:123], %[vl], s[20:21] offset:2048\n"
"s_waitcnt vmcnt(12)\n"
"v_mfma_f32_16x16x32_bf16 v[140:143], a[72:75], a[28:31], v[140:143]\n"
"v_rcp_f32 v234, v234\n"
"v_rcp_f32 v235, v235\n"
"v_mfma_f32_16x16x32_bf16 v[136:139], a[72:75], a[24:27], v[136:139]\n"
"v_mul_f32 v60, v60, v232\n"
"v_mul_f32 v61, v61, v233\n"
"v_mfma_f32_16x16x32_bf16 v[132:135], a[72:75], a[20:23], v[132:135]\n"
"v_mul_f32 v62, v62, v234\n"
"v_mul_f32 v63, v63, v235\n"
"v_cvt_pk_bf16_f32 v244, v60, v61\n"
"v_mfma_f32_16x16x32_bf16 v[128:131], a[72:75], a[16:19], v[128:131]\n"
"global_load_dwordx4 a[124:127], %[vl], s[20:21] offset:3072\n"
"v_cvt_pk_bf16_f32 v245, v62, v63\n"
"s_waitcnt lgkmcnt(0)\n"
"s_waitcnt vmcnt(12)\n"
"v_mfma_f32_16x16x32_bf16 v[80:83], a[76:79], a[0:3], v[80:83]\n"
"ds_read_b128 a[16:19], %[vx0] offset:18432\n"
"v_mfma_f32_16x16x32_bf16 v[84:87], a[76:79], a[4:7], v[84:87]\n"
"ds_read_b128 a[20:23], %[vx0] offset:37888\n"
"v_mfma_f32_16x16x32_bf16 v[88:91], a[76:79], a[8:11], v[88:91]\n"
"ds_read_b128 a[24:27], %[vx0] offset:57344\n"
"v_mfma_f32_16x16x32_bf16 v[92:95], a[76:79], a[12:15], v[92:95]\n"
"s_add_u32 s20, s20, 8192\n"
"s_addc_u32 s21, s21, 0\n"
"global_load_dwordx4 a[128:131], %[vl], s[20:21] offset:-4096\n"
"ds_read_b128 a[28:31], v246 offset:11264\n"
"ds_write_b128 %[vhw], v[242:245] offset:7168\n"
"v_mul_f32 v232, v64, v225\n"
"s_waitcnt vmcnt(12)\n"
"v_mfma_f32_16x16x32_bf16 v[108:111], a[80:83], a[12:15], v[108:111]\n"
"v_mul_f32 v233, v65, v225\n"
"v_mul_f32 v234, v66, v225\n"
"v_mul_f32 v235, v67, v225\n"
"v_mfma_f32_16x16x32_bf16 v[104:107], a[80:83], a[8:11], v[104:107]\n"
"v_exp_f32 v232, v232\n"
"v_mfma_f32_16x16x32_bf16 v[100:103], a[80:83], a[4:7], v[100:103]\n"
"v_exp_f32 v233, v233\n"
"v_mfma_f32_16x16x32_bf16 v[96:99], a[80:83], a[0:3], v[96:99]\n"
"global_load_dwordx4 a[132:135], %[vl], s[20:21] offset:-3072\n"
"v_exp_f32 v234, v234\n"
"s_waitcnt vmcnt(12)\n"
"v_mfma_f32_16x16x32_bf16 v[112:115], a[84:87], a[0:3], v[112:115]\n"
"v_exp_f32 v235, v235\n"
"v_fma_f32 v232, v232, v224, v224\n"
"v_mfma_f32_16x16x32_bf16 v[116:119], a[84:87], a[4:7], v[116:119]\n"
"v_fma_f32 v233, v233, v224, v224\n"
"v_fma_f32 v234, v234, v224, v224\n"
"v_mfma_f32_16x16x32_bf16 v[120:123], a[84:87], a[8:11], v[120:123]\n"
"v_fma_f32 v235, v235, v224, v224\n"
"v_rcp_f32 v232, v232\n"
"v_mfma_f32_16x16x32_bf16 v[124:127], a[84:87], a[12:15], v[124:127]\n"
"global_load_dwordx4 a[136:139], %[vl], s[20:21] offset:-2048\n"
"s_waitcnt vmcnt(12)\n"
"v_mfma_f32_16x16x32_bf16 v[140:143], a[88:91], a[12:15], v[140:143]\n"
"v_rcp_f32 v233, v233\n"
"v_rcp_f32 v234, v234\n"
"v_mfma_f32_16x16x32_bf16 v[136:139], a[88:91], a[8:11], v[136:139]\n"
"v_rcp_f32 v235, v235\n"
"v_mfma_f32_16x16x32_bf16 v[132:135], a[88:91], a[4:7], v[132:135]\n"
"v_mul_f32 v64, v64, v232\n"
"v_mul_f32 v65, v65, v233\n"
"v_mul_f32 v66, v66, v234\n"
"v_mfma_f32_16x16x32_bf16 v[128:131], a[88:91], a[0:3], v[128:131]\n"
"global_load_dwordx4 a[140:143], %[vl], s[20:21] offset:-1024\n"
"v_mul_f32 v67, v67, v235\n"
"s_waitcnt lgkmcnt(1)\n"
"s_waitcnt vmcnt(12)\n"
"v_mfma_f32_16x16x32_bf16 v[80:83], a[92:95], a[16:19], v[80:83]\n"
"v_cvt_pk_bf16_f32 v238, v64, v65\n"
"v_cvt_pk_bf16_f32 v239, v66, v67\n"
"ds_write_b64 %[vh4], v[238:239] offset:0\n"
"v_mfma_f32_16x16x32_bf16 v[84:87], a[92:95], a[20:23], v[84:87]\n"
"v_mul_f32 v232, v68, v227\n"
"v_mul_f32 v233, v69, v227\n"
"v_mfma_f32_16x16x32_bf16 v[88:91], a[92:95], a[24:27], v[88:91]\n"
"v_mul_f32 v234, v70, v227\n"
"v_mul_f32 v235, v71, v227\n"
"v_mfma_f32_16x16x32_bf16 v[92:95], a[92:95], a[28:31], v[92:95]\n"
"global_load_dwordx4 a[144:147], %[vl], s[20:21] offset:0\n"
"v_exp_f32 v232, v232\n"
"s_waitcnt vmcnt(12)\n"
"v_mfma_f32_16x16x32_bf16 v[108:111], a[96:99], a[28:31], v[108:111]\n"
"s_waitcnt lgkmcnt(0)\n"
"s_barrier\n"
"ds_read_b128 a[0:3], v246 offset:12288\n"
"v_mfma_f32_16x16x32_bf16 v[104:107], a[96:99], a[24:27], v[104:107]\n"
"ds_read_b128 a[4:7], v246 offset:13312\n"
"v_mfma_f32_16x16x32_bf16 v[100:103], a[96:99], a[20:23], v[100:103]\n"
"ds_read_b128 a[8:11], v246 offset:14336\n"
"v_mfma_f32_16x16x32_bf16 v[96:99], a[96:99], a[16:19], v[96:99]\n"
"global_load_dwordx4 a[148:151], %[vl], s[20:21] offset:1024\n"
"ds_read_b128 a[12:15], v246 offset:15360\n"
"v_exp_f32 v233, v233\n"
"s_waitcnt vmcnt(12)\n"
"v_mfma_f32_16x16x32_bf16 v[112:115], a[100:103], a[16:19], v[112:115]\n"
"v_exp_f32 v234, v234\n"
"v_mfma_f32_16x16x32_bf16 v[116:119], a[100:103], a[20:23], v[116:119]\n"
"v_exp_f32 v235, v235\n"
"v_fma_f32 v232, v232, v226, v226\n"
"v_mfma_f32_16x16x32_bf16 v[120:123], a[100:103], a[24:27], v[120:123]\n"
"v_fma_f32 v233, v233, v226, v226\n"
"v_fma_f32 v234, v234, v226, v226\n"
"v_fma_f32 v235, v235, v226, v226\n"
"v_mfma_f32_16x16x32_bf16 v[124:127], a[100:103], a[28:31], v[124:127]\n"
"global_load_dwordx4 a[152:155], %[vl], s[20:21] offset:2048\n"
"s_waitcnt vmcnt(12)\n"
"v_mfma_f32_16x16x32_bf16 v[140:143], a[104:107], a[28:31], v[140:143]\n"
"v_rcp_f32 v232, v232\n"
"v_rcp_f32 v233, v233\n"
"v_mfma_f32_16x16x32_bf16 v[136:139], a[104:107], a[24:27], v[136:139]\n"
"v_rcp_f32 v234, v234\n"
"v_mfma_f32_16x16x32_bf16 v[132:135], a[104:107], a[20:23], v[132:135]\n"
"v_rcp_f32 v235, v235\n"
"v_mul_f32 v68, v68, v232\n"
"v_mfma_f32_16x16x32_bf16 v[128:131], a[104:107], a[16:19], v[128:131]\n"
"global_load_dwordx4 a[156:159], %[vl], s[20:21] offset:3072\n"
"v_mul_f32 v69, v69, v233\n"
"s_waitcnt lgkmcnt(0)\n"
"s_waitcnt vmcnt(12)\n"
"v_mfma_f32_16x16x32_bf16 a[32:35], a[108:111], a[0:3], 0\n"
"ds_read_b128 a[16:19], v246 offset:20480\n"
"v_mfma_f32_16x16x32_bf16 a[36:39], a[108:111], a[4:7], 0\n"
"ds_read_b128 a[20:23], v246 offset:21504\n"
"v_mfma_f32_16x16x32_bf16 a[40:43], a[108:111], a[8:11], 0\n"
"ds_read_b128 a[24:27], v246 offset:22528\n"
"v_mfma_f32_16x16x32_bf16 a[44:47], a[108:111], a[12:15], 0\n"
"s_add_u32 s20, s20, 8192\n"
"s_addc_u32 s21, s21, 0\n"
"global_load_dwordx4 a[160:163], %[vl], s[20:21] offset:-4096\n"
"ds_read_b128 a[28:31], v246 offset:23552\n"
"v_mul_f32 v70, v70, v234\n"
"v_mul_f32 v71, v71, v235\n"
"s_waitcnt vmcnt(12)\n"
"v_mfma_f32_16x16x32_bf16 a[60:63], a[112:115], a[12:15], 0\n"
"v_cvt_pk_bf16_f32 v242, v68, v69\n"
"v_cvt_pk_bf16_f32 v243, v70, v71\n"
"ds_write_b64 %[vh4], v[242:243] offset:1024\n"
"v_mfma_f32_16x16x32_bf16 a[56:59], a[112:115], a[8:11], 0\n"
"v_mul_f32 v232, v72, v229\n"
"v_mul_f32 v233, v73, v229\n"
"v_mfma_f32_16x16x32_bf16 a[52:55], a[112:115], a[4:7], 0\n"
"global_load_dwordx4 a[164:167], %[vl], s[20:21] offset:-3072\n"
"v_mul_f32 v234, v74, v229\n"
"v_mul_f32 v235, v75, v229\n"
"v_mfma_f32_16x16x32_bf16 a[48:51], a[112:115], a[0:3], 0\n"
"v_exp_f32 v232, v232\n"
"s_waitcnt vmcnt(12)\n"
"v_mfma_f32_16x16x32_bf16 v[0:3], a[116:119], a[0:3], 0\n"
"v_exp_f32 v233, v233\n"
"v_mfma_f32_16x16x32_bf16 v[4:7], a[116:119], a[4:7], 0\n"
"global_load_dwordx4 a[168:171], %[vl], s[20:21] offset:-2048\n"
"v_exp_f32 v234, v234\n"
"v_mfma_f32_16x16x32_bf16 v[8:11], a[116:119], a[8:11], 0\n"
"v_exp_f32 v235, v235\n"
"v_fma_f32 v232, v232, v228, v228\n"
"v_mfma_f32_16x16x32_bf16 v[12:15], a[116:119], a[12:15], 0\n"
"v_fma_f32 v233, v233, v228, v228\n"
"v_fma_f32 v234, v234, v228, v228\n"
"s_waitcnt vmcnt(12)\n"
"v_mfma_f32_16x16x32_bf16 v[28:31], a[120:123], a[12:15], 0\n"
"global_load_dwordx4 a[172:175], %[vl], s[20:21] offset:-1024\n"
"v_fma_f32 v235, v235, v228, v228\n"
"v_mfma_f32_16x16x32_bf16 v[24:27], a[120:123], a[8:11], 0\n"
"v_rcp_f32 v232, v232\n"
"v_mfma_f32_16x16x32_bf16 v[20:23], a[120:123], a[4:7], 0\n"
"v_rcp_f32 v233, v233\n"
"v_rcp_f32 v234, v234\n"
"v_mfma_f32_16x16x32_bf16 v[16:19], a[120:123], a[0:3], 0\n"
"global_load_dwordx4 a[176:179], %[vl], s[20:21] offset:0\n"
"s_waitcnt vmcnt(13)\n"
"v_mfma_f32_16x16x32_bf16 v[144:147], a[124:127], a[0:3], 0\n"
"v_rcp_f32 v235, v235\n"
"v_mul_f32 v72, v72, v232\n"
"v_mul_f32 v73, v73, v233\n"
"v_mfma_f32_16x16x32_bf16 v[148:151], a[124:127], a[4:7], 0\n"
"v_mul_f32 v74, v74, v234\n"
"v_mul_f32 v75, v75, v235\n"
"v_mfma_f32_16x16x32_bf16 v[152:155], a[124:127], a[8:11], 0\n"
"global_load_dwordx4 a[180:183], %[vl], s[20:21] offset:1024\n"
"v_cvt_pk_bf16_f32 v238, v72, v73\n"
"v_cvt_pk_bf16_f32 v239, v74, v75\n"
"v_mfma_f32_16x16x32_bf16 v[156:159], a[124:127], a[12:15], 0\n"
"ds_write_b64 %[vh4], v[238:239] offset:2048\n"
"v_mul_f32 v232, v76, v231\n"
"s_waitcnt vmcnt(13)\n"
"v_mfma_f32_16x16x32_bf16 v[172:175], a[128:131], a[12:15], 0\n"
"v_mul_f32 v233, v77, v231\n"
"v_mul_f32 v234, v78, v231\n"
"v_mul_f32 v235, v79, v231\n"
"v_mfma_f32_16x16x32_bf16 v[168:171], a[128:131], a[8:11], 0\n"
"global_load_dwordx4 a[184:187], %[vl], s[20:21] offset:2048\n"
"v_mfma_f32_16x16x32_bf16 v[164:167], a[128:131], a[4:7], 0\n"
"v_exp_f32 v232, v232\n"
"v_exp_f32 v233, v233\n"
"v_mfma_f32_16x16x32_bf16 v[160:163], a[128:131], a[0:3], 0\n"
"v_exp_f32 v234, v234\n"
"s_waitcnt vmcnt(13)\n"
"v_mfma_f32_16x16x32_bf16 v[176:179], a[132:135], a[0:3], 0\n"
"v_exp_f32 v235, v235\n"
"v_fma_f32 v232, v232, v230, v230\n"
"v_mfma_f32_16x16x32_bf16 v[180:183], a[132:135], a[4:7], 0\n"
"global_load_dwordx4 a[188:191], %[vl], s[20:21] offset:3072\n"
"v_fma_f32 v233, v233, v230, v230\n"
"v_mfma_f32_16x16x32_bf16 v[184:187], a[132:135], a[8:11], 0\n"
"v_fma_f32 v234, v234, v230, v230\n"
"v_fma_f32 v235, v235, v230, v230\n"
"v_mfma_f32_16x16x32_bf16 v[188:191], a[132:135], a[12:15], 0\n"
"v_rcp_f32 v232, v232\n"
"s_waitcnt vmcnt(13)\n"
"v_mfma_f32_16x16x32_bf16 v[204:207], a[136:139], a[12:15], 0\n"
"s_add_u32 s20, s20, 8192\n"
"s_addc_u32 s21, s21, 0\n"
"global_load_dwordx4 a[64:67], %[vl], s[20:21] offset:-4096\n"
"v_rcp_f32 v233, v233\n"
"v_mfma_f32_16x16x32_bf16 v[200:203], a[136:139], a[8:11], 0\n"
"v_rcp_f32 v234, v234\n"
"v_mfma_f32_16x16x32_bf16 v[196:199], a[136:139], a[4:7], 0\n"
"v_rcp_f32 v235, v235\n"
"v_mul_f32 v76, v76, v232\n"
"v_mul_f32 v77, v77, v233\n"
"v_mfma_f32_16x16x32_bf16 v[192:195], a[136:139], a[0:3], 0\n"
"global_load_dwordx4 a[68:71], %[vl], s[20:21] offset:-3072\n"
"v_mul_f32 v78, v78, v234\n"
"s_waitcnt vmcnt(14)\n"
"v_mfma_f32_16x16x32_bf16 v[208:211], a[140:143], a[0:3], 0\n"
"v_mul_f32 v79, v79, v235\n"
"v_cvt_pk_bf16_f32 v242, v76, v77\n"
"v_cvt_pk_bf16_f32 v243, v78, v79\n"
"v_mfma_f32_16x16x32_bf16 v[212:215], a[140:143], a[4:7], 0\n"
"ds_write_b64 %[vh4], v[242:243] offset:3072\n"
"v_mul_f32 v232, v80, v225\n"
"v_mfma_f32_16x16x32_bf16 v[216:219], a[140:143], a[8:11], 0\n"
"global_load_dwordx4 a[72:75], %[vl], s[20:21] offset:-2048\n"
"v_mul_f32 v233, v81, v225\n"
"v_mul_f32 v234, v82, v225\n"
"v_mfma_f32_16x16x32_bf16 v[220:223], a[140:143], a[12:15], 0\n"
"v_mul_f32 v235, v83, v225\n"
"s_waitcnt lgkmcnt(3)\n"
"s_waitcnt vmcnt(14)\n"
"v_mfma_f32_16x16x32_bf16 a[32:35], a[144:147], a[16:19], a[32:35]\n"
"ds_read_b128 a[0:3], v246 offset:28672\n"
"v_mfma_f32_16x16x32_bf16 a[36:39], a[144:147], a[20:23], a[36:39]\n"
"global_load_dwordx4 a[76:79], %[vl], s[20:21] offset:-1024\n"
"ds_read_b128 a[4:7], v246 offset:29696\n"
"v_mfma_f32_16x16x32_bf16 a[40:43], a[144:147], a[24:27], a[40:43]\n"
"ds_read_b128 a[8:11], v246 offset:30720\n"
"v_mfma_f32_16x16x32_bf16 a[44:47], a[144:147], a[28:31], a[44:47]\n"
"ds_read_b128 a[12:15], v246 offset:31744\n"
"v_exp_f32 v232, v232\n"
"s_waitcnt vmcnt(14)\n"
"v_mfma_f32_16x16x32_bf16 a[60:63], a[148:151], a[28:31], a[60:63]\n"
"global_load_dwordx4 a[80:83], %[vl], s[20:21] offset:0\n"
"v_exp_f32 v233, v233\n"
"v_mfma_f32_16x16x32_bf16 a[56:59], a[148:151], a[24:27], a[56:59]\n"
"v_exp_f32 v234, v234\n"
"v_mfma_f32_16x16x32_bf16 a[52:55], a[148:151], a[20:23], a[52:55]\n"
"v_exp_f32 v235, v235\n"
"v_fma_f32 v232, v232, v224, v224\n"
"v_fma_f32 v233, v233, v224, v224\n"
"v_mfma_f32_16x16x32_bf16 a[48:51], a[148:151], a[16:19], a[48:51]\n"
"global_load_dwordx4 a[84:87], %[vl], s[20:21] offset:1024\n"
"v_fma_f32 v234, v234, v224, v224\n"
"s_waitcnt vmcnt(15)\n"
"v_mfma_f32_16x16x32_bf16 v[0:3], a[152:155], a[16:19], v[0:3]\n"
"v_fma_f32 v235, v235, v224, v224\n"
"v_rcp_f32 v232, v232\n"
"v_mfma_f32_16x16x32_bf16 v[4:7], a[152:155], a[20:23], v[4:7]\n"
"v_rcp_f32 v233, v233\n"
"v_mfma_f32_16x16x32_bf16 v[8:11], a[152:155], a[24:27], v[8:11]\n"
"global_load_dwordx4 a[88:91], %[vl], s[20:21] offset:2048\n"
"v_rcp_f32 v234, v234\n"
"v_mfma_f32_16x16x32_bf16 v[12:15], a[152:155], a[28:31], v[12:15]\n"
"v_rcp_f32 v235, v235\n"
"s_waitcnt vmcnt(15)\n"
"v_mfma_f32_16x16x32_bf16 v[28:31], a[156:159], a[28:31], v[28:31]\n"
"v_mul_f32 v80, v80, v232\n"
"v_mul_f32 v81, v81, v233\n"
"v_mul_f32 v82, v82, v234\n"
"v_mfma_f32_16x16x32_bf16 v[24:27], a[156:159], a[24:27], v[24:27]\n"
"v_mul_f32 v83, v83, v235\n"
"v_cvt_pk_bf16_f32 v238, v80, v81\n"
"v_mfma_f32_16x16x32_bf16 v[20:23], a[156:159], a[20:23], v[20:23]\n"
"global_load_dwordx4 a[92:95], %[vl], s[20:21] offset:3072\n"
"v_cvt_pk_bf16_f32 v239, v82, v83\n"
"v_mul_f32 v232, v96, v225\n"
"v_mfma_f32_16x16x32_bf16 v[16:19], a[156:159], a[16:19], v[16:19]\n"
"v_mul_f32 v233, v97, v225\n"
"v_mul_f32 v234, v98, v225\n"
"s_waitcnt vmcnt(15)\n"
"v_mfma_f32_16x16x32_bf16 v[144:147], a[160:163], a[16:19], v[144:147]\n"
"v_mul_f32 v235, v99, v225\n"
"v_exp_f32 v232, v232\n"
"v_mfma_f32_16x16x32_bf16 v[148:151], a[160:163], a[20:23], v[148:151]\n"
"s_add_u32 s20, s20, 8192\n"
"s_addc_u32 s21, s21, 0\n"
"global_load_dwordx4 a[96:99], %[vl], s[20:21] offset:-4096\n"
"v_mfma_f32_16x16x32_bf16 v[152:155], a[160:163], a[24:27], v[152:155]\n"
"v_exp_f32 v233, v233\n"
"v_exp_f32 v234, v234\n"
"v_mfma_f32_16x16x32_bf16 v[156:159], a[160:163], a[28:31], v[156:159]\n"
"v_exp_f32 v235, v235\n"
"s_waitcnt vmcnt(15)\n"
"v_mfma_f32_16x16x32_bf16 v[172:175], a[164:167], a[28:31], v[172:175]\n"
"global_load_dwordx4 a[100:103], %[vl], s[20:21] offset:-3072\n"
"v_fma_f32 v232, v232, v224, v224\n"
"v_fma_f32 v233, v233, v224, v224\n"
"v_mfma_f32_16x16x32_bf16 v[168:171], a[164:167], a[24:27], v[168:171]\n"
"v_fma_f32 v234, v234, v224, v224\n"
"v_fma_f32 v235, v235, v224, v224\n"
"v_mfma_f32_16x16x32_bf16 v[164:167], a[164:167], a[20:23], v[164:167]\n"
"v_rcp_f32 v232, v232\n"
"v_mfma_f32_16x16x32_bf16 v[160:163], a[164:167], a[16:19], v[160:163]\n"
"global_load_dwordx4 a[104:107], %[vl], s[20:21] offset:-2048\n"
"v_rcp_f32 v233, v233\n"
"s_waitcnt vmcnt(16)\n"
"v_mfma_f32_16x16x32_bf16 v[176:179], a[168:171], a[16:19], v[176:179]\n"
"v_rcp_f32 v234, v234\n"
"v_mfma_f32_16x16x32_bf16 v[180:183], a[168:171], a[20:23], v[180:183]\n"
"v_rcp_f32 v235, v235\n"
"v_mul_f32 v96, v96, v232\n"
"v_mfma_f32_16x16x32_bf16 v[184:187], a[168:171], a[24:27], v[184:187]\n"
"global_load_dwordx4 a[108:111], %[vl], s[20:21] offset:-1024\n"
"v_mul_f32 v97, v97, v233\n"
"v_mul_f32 v98, v98, v234\n"
"v_mfma_f32_16x16x32_bf16 v[188:191], a[168:171], a[28:31], v[188:191]\n"
"v_mul_f32 v99, v99, v235\n"
"v_cvt_pk_bf16_f32 v240, v96, v97\n"
"s_waitcnt vmcnt(16)\n"
"v_mfma_f32_16x16x32_bf16 v[204:207], a[172:175], a[28:31], v[204:207]\n"
"v_cvt_pk_bf16_f32 v241, v98, v99\n"
"ds_write_b128 %[vhw], v[238:241] offset:40960\n"
"v_mul_f32 v232, v84, v227\n"
"v_mfma_f32_16x16x32_bf16 v[200:203], a[172:175], a[24:27], v[200:203]\n"
"global_load_dwordx4 a[112:115], %[vl], s[20:21] offset:0\n"
"v_mul_f32 v233, v85, v227\n"
"v_mfma_f32_16x16x32_bf16 v[196:199], a[172:175], a[20:23], v[196:199]\n"
"v_mul_f32 v234, v86, v227\n"
"v_mul_f32 v235, v87, v227\n"
"v_mfma_f32_16x16x32_bf16 v[192:195], a[172:175], a[16:19], v[192:195]\n"
"v_exp_f32 v232, v232\n"
"s_waitcnt vmcnt(16)\n"
"v_mfma_f32_16x16x32_bf16 v[208:211], a[176:179], a[16:19], v[208:211]\n"
"global_load_dwordx4 a[116:119], %[vl], s[20:21] offset:1024\n"
"v_exp_f32 v233, v233\n"
"v_mfma_f32_16x16x32_bf16 v[212:215], a[176:179], a[20:23], v[212:215]\n"
"v_exp_f32 v234, v234\n"
"v_mfma_f32_16x16x32_bf16 v[216:219], a[176:179], a[24:27], v[216:219]\n"
"v_exp_f32 v235, v235\n"
"v_fma_f32 v232, v232, v226, v226\n"
"v_fma_f32 v233, v233, v226, v226\n"
"v_mfma_f32_16x16x32_bf16 v[220:223], a[176:179], a[28:31], v[220:223]\n"
"global_load_dwordx4 a[120:123], %[vl], s[20:21] offset:2048\n"
"v_fma_f32 v234, v234, v226, v226\n"
"s_waitcnt lgkmcnt(1)\n"
"s_waitcnt vmcnt(17)\n"
"v_mfma_f32_16x16x32_bf16 a[32:35], a[180:183], a[0:3], a[32:35]\n"
"ds_read_b128 a[16:19], v246 offset:36864\n"
"v_mfma_f32_16x16x32_bf16 a[36:39], a[180:183], a[4:7], a[36:39]\n"
"ds_read_b128 a[20:23], v246 offset:37888\n"
"v_mfma_f32_16x16x32_bf16 a[40:43], a[180:183], a[8:11], a[40:43]\n"
"ds_read_b128 a[24:27], v246 offset:38912\n"
"v_mfma_f32_16x16x32_bf16 a[44:47], a[180:183], a[12:15], a[44:47]\n"
"global_load_dwordx4 a[124:127], %[vl], s[20:21] offset:3072\n"
"ds_read_b128 a[28:31], v246 offset:39936\n"
"v_fma_f32 v235, v235, v226, v226\n"
"s_waitcnt vmcnt(17)\n"
"v_mfma_f32_16x16x32_bf16 a[60:63], a[184:187], a[12:15], a[60:63]\n"
"v_rcp_f32 v232, v232\n"
"v_rcp_f32 v233, v233\n"
"v_mfma_f32_16x16x32_bf16 a[56:59], a[184:187], a[8:11], a[56:59]\n"
"v_rcp_f32 v234, v234\n"
"v_mfma_f32_16x16x32_bf16 a[52:55], a[184:187], a[4:7], a[52:55]\n"
"s_add_u32 s20, s20, 8192\n"
"s_addc_u32 s21, s21, 0\n"
"global_load_dwordx4 a[128:131], %[vl], s[20:21] offset:-4096\n"
"v_rcp_f32 v235, v235\n"
"v_mfma_f32_16x16x32_bf16 a[48:51], a[184:187], a[0:3], a[48:51]\n"
"v_mul_f32 v84, v84, v232\n"
"v_mul_f32 v85, v85, v233\n"
"s_waitcnt vmcnt(17)\n"
"v_mfma_f32_16x16x32_bf16 v[0:3], a[188:191], a[0:3], v[0:3]\n"
"v_mul_f32 v86, v86, v234\n"
"v_mul_f32 v87, v87, v235\n"
"v_cvt_pk_bf16_f32 v242, v84, v85\n"
"v_mfma_f32_16x16x32_bf16 v[4:7], a[188:191], a[4:7], v[4:7]\n"
"global_load_dwordx4 a[132:135], %[vl], s[20:21] offset:-3072\n"
"v_cvt_pk_bf16_f32 v243, v86, v87\n"
"v_mfma_f32_16x16x32_bf16 v[8:11], a[188:191], a[8:11], v[8:11]\n"
"v_mul_f32 v232, v100, v227\n"
"v_mul_f32 v233, v101, v227\n"
"v_mul_f32 v234, v102, v227\n"
"v_mfma_f32_16x16x32_bf16 v[12:15], a[188:191], a[12:15], v[12:15]\n"
"v_mul_f32 v235, v103, v227\n"
"s_waitcnt vmcnt(17)\n"
"v_mfma_f32_16x16x32_bf16 v[28:31], a[64:67], a[12:15], v[28:31]\n"
"global_load_dwordx4 a[136:139], %[vl], s[20:21] offset:-2048\n"
"v_exp_f32 v232, v232\n"
"v_mfma_f32_16x16x32_bf16 v[24:27], a[64:67], a[8:11], v[24:27]\n"
"v_exp_f32 v233, v233\n"
"v_mfma_f32_16x16x32_bf16 v[20:23], a[64:67], a[4:7], v[20:23]\n"
"v_exp_f32 v234, v234\n"
"v_exp_f32 v235, v235\n"
"v_mfma_f32_16x16x32_bf16 v[16:19], a[64:67], a[0:3], v[16:19]\n"
"global_load_dwordx4 a[140:143], %[vl], s[20:21] offset:-1024\n"
"v_fma_f32 v232, v232, v226, v226\n"
"s_waitcnt vmcnt(18)\n"
"v_mfma_f32_16x16x32_bf16 v[144:147], a[68:71], a[0:3], v[144:147]\n"
"v_fma_f32 v233, v233, v226, v226\n"
"v_fma_f32 v234, v234, v226, v226\n"
"v_fma_f32 v235, v235, v226, v226\n"
"v_mfma_f32_16x16x32_bf16 v[148:151], a[68:71], a[4:7], v[148:151]\n"
"v_rcp_f32 v232, v232\n"
"v_mfma_f32_16x16x32_bf16 v[152:155], a[68:71], a[8:11], v[152:155]\n"
"global_load_dwordx4 a[144:147], %[vl], s[20:21] offset:0\n"
"v_rcp_f32 v233, v233\n"
"v_mfma_f32_16x16x32_bf16 v[156:159], a[68:71], a[12:15], v[156:159]\n"
"v_rcp_f32 v234, v234\n"
"s_waitcnt vmcnt(18)\n"
"v_mfma_f32_16x16x32_bf16 v[172:175], a[72:75], a[12:15], v[172:175]\n"
"v_rcp_f32 v235, v235\n"
"v_mul_f32 v100, v100, v232\n"
"v_mfma_f32_16x16x32_bf16 v[168:171], a[72:75], a[8:11], v[168:171]\n"
"global_load_dwordx4 a[148:151], %[vl], s[20:21] offset:1024\n"
"v_mul_f32 v101, v101, v233\n"
"v_mfma_f32_16x16x32_bf16 v[164:167], a[72:75], a[4:7], v[164:167]\n"
"v_mul_f32 v102, v102, v234\n"
"v_mul_f32 v103, v103, v235\n"
"v_cvt_pk_bf16_f32 v244, v100, v101\n"
"v_mfma_f32_16x16x32_bf16 v[160:163], a[72:75], a[0:3], v[160:163]\n"
"v_cvt_pk_bf16_f32 v245, v102, v103\n"
"ds_write_b128 %[vhw], v[242:245] offset:41984\n"
"s_waitcnt vmcnt(18)\n"
"v_mfma_f32_16x16x32_bf16 v[176:179], a[76:79], a[0:3], v[176:179]\n"
"v_mul_f32 v232, v88, v229\n"
"v_mul_f32 v233, v89, v229\n"
"v_mul_f32 v234, v90, v229\n"
"v_mfma_f32_16x16x32_bf16 v[180:183], a[76:79], a[4:7], v[180:183]\n"
"global_load_dwordx4 a[152:155], %[vl], s[20:21] offset:2048\n"
"v_mul_f32 v235, v91, v229\n"
"v_mfma_f32_16x16x32_bf16 v[184:187], a[76:79], a[8:11], v[184:187]\n"
"v_exp_f32 v232, v232\n"
"v_mfma_f32_16x16x32_bf16 v[188:191], a[76:79], a[12:15], v[188:191]\n"
"v_exp_f32 v233, v233\n"
"s_waitcnt vmcnt(18)\n"
"v_mfma_f32_16x16x32_bf16 v[204:207], a[80:83], a[12:15], v[204:207]\n"
"global_load_dwordx4 a[156:159], %[vl], s[20:21] offset:3072\n"
"v_exp_f32 v234, v234\n"
"v_mfma_f32_16x16x32_bf16 v[200:203], a[80:83], a[8:11], v[200:203]\n"
"v_exp_f32 v235, v235\n"
"v_fma_f32 v232, v232, v228, v228\n"
"v_mfma_f32_16x16x32_bf16 v[196:199], a[80:83], a[4:7], v[196:199]\n"
"v_fma_f32 v233, v233, v228, v228\n"
"v_fma_f32 v234, v234, v228, v228\n"
"v_fma_f32 v235, v235, v228, v228\n"
"v_mfma_f32_16x16x32_bf16 v[192:195], a[80:83], a[0:3], v[192:195]\n"
"s_add_u32 s20, s20, 8192\n"
"s_addc_u32 s21, s21, 0\n"
"global_load_dwordx4 a[160:163], %[vl], s[20:21] offset:-4096\n"
"s_waitcnt vmcnt(19)\n"
"v_mfma_f32_16x16x32_bf16 v[208:211], a[84:87], a[0:3], v[208:211]\n"
"v_rcp_f32 v232, v232\n"
"v_rcp_f32 v233, v233\n"
"v_mfma_f32_16x16x32_bf16 v[212:215], a[84:87], a[4:7], v[212:215]\n"
"v_rcp_f32 v234, v234\n"
"v_mfma_f32_16x16x32_bf16 v[216:219], a[84:87], a[8:11], v[216:219]\n"
"global_load_dwordx4 a[164:167], %[vl], s[20:21] offset:-3072\n"
"v_rcp_f32 v235, v235\n"
"v_mfma_f32_16x16x32_bf16 v[220:223], a[84:87], a[12:15], v[220:223]\n"
"v_mul_f32 v88, v88, v232\n"
"v_mul_f32 v89, v89, v233\n"
"s_waitcnt lgkmcnt(1)\n"
"s_waitcnt vmcnt(19)\n"
"v_mfma_f32_16x16x32_bf16 a[32:35], a[88:91], a[16:19], a[32:35]\n"
"v_mul_f32 v90, v90, v234\n"
"v_mul_f32 v91, v91, v235\n"
"v_cvt_pk_bf16_f32 v238, v88, v89\n"
"v_mfma_f32_16x16x32_bf16 a[36:39], a[88:91], a[20:23], a[36:39]\n"
"global_load_dwordx4 a[168:171], %[vl], s[20:21] offset:-2048\n"
"v_cvt_pk_bf16_f32 v239, v90, v91\n"
"v_mfma_f32_16x16x32_bf16 a[40:43], a[88:91], a[24:27], a[40:43]\n"
"v_mul_f32 v232, v104, v229\n"
"v_mul_f32 v233, v105, v229\n"
"v_mul_f32 v234, v106, v229\n"
"v_mfma_f32_16x16x32_bf16 a[44:47], a[88:91], a[28:31], a[44:47]\n"
"v_mul_f32 v235, v107, v229\n"
"s_waitcnt vmcnt(19)\n"
"v_mfma_f32_16x16x32_bf16 a[60:63], a[92:95], a[28:31], a[60:63]\n"
"global_load_dwordx4 a[172:175], %[vl], s[20:21] offset:-1024\n"
"v_exp_f32 v232, v232\n"
"v_mfma_f32_16x16x32_bf16 a[56:59], a[92:95], a[24:27], a[56:59]\n"
"v_exp_f32 v233, v233\n"
"v_mfma_f32_16x16x32_bf16 a[52:55], a[92:95], a[20:23], a[52:55]\n"
"v_exp_f32 v234, v234\n"
"v_exp_f32 v235, v235\n"
"v_mfma_f32_16x16x32_bf16 a[48:51], a[92:95], a[16:19], a[48:51]\n"
"global_load_dwordx4 a[176:179], %[vl], s[20:21] offset:0\n"
"v_fma_f32 v232, v232, v228, v228\n"
"s_waitcnt vmcnt(20)\n"
"v_mfma_f32_16x16x32_bf16 v[0:3], a[96:99], a[16:19], v[0:3]\n"
"v_fma_f32 v233, v233, v228, v228\n"
"v_fma_f32 v234, v234, v228, v228\n"
"v_fma_f32 v235, v235, v228, v228\n"
"v_mfma_f32_16x16x32_bf16 v[4:7], a[96:99], a[20:23], v[4:7]\n"
"v_rcp_f32 v232, v232\n"
"v_mfma_f32_16x16x32_bf16 v[8:11], a[96:99], a[24:27], v[8:11]\n"
"global_load_dwordx4 a[180:183], %[vl], s[20:21] offset:1024\n"
"v_rcp_f32 v233, v233\n"
"v_mfma_f32_16x16x32_bf16 v[12:15], a[96:99], a[28:31], v[12:15]\n"
"v_rcp_f32 v234, v234\n"
"s_waitcnt vmcnt(20)\n"
"v_mfma_f32_16x16x32_bf16 v[28:31], a[100:103], a[28:31], v[28:31]\n"
"v_rcp_f32 v235, v235\n"
"v_mul_f32 v104, v104, v232\n"
"v_mfma_f32_16x16x32_bf16 v[24:27], a[100:103], a[24:27], v[24:27]\n"
"v_mul_f32 v105, v105, v233\n"
"v_mul_f32 v106, v106, v234\n"
"v_mfma_f32_16x16x32_bf16 v[20:23], a[100:103], a[20:23], v[20:23]\n"
"global_load_dwordx4 a[184:187], %[vl], s[20:21] offset:2048\n"
"v_mul_f32 v107, v107, v235\n"
"v_cvt_pk_bf16_f32 v240, v104, v105\n"
"v_mfma_f32_16x16x32_bf16 v[16:19], a[100:103], a[16:19], v[16:19]\n"
"v_cvt_pk_bf16_f32 v241, v106, v107\n"
"ds_write_b128 %[vhw], v[238:241] offset:43008\n"
"s_waitcnt vmcnt(20)\n"
"v_mfma_f32_16x16x32_bf16 v[144:147], a[104:107], a[16:19], v[144:147]\n"
"v_mul_f32 v232, v92, v231\n"
"v_mul_f32 v233, v93, v231\n"
"v_mul_f32 v234, v94, v231\n"
"v_mfma_f32_16x16x32_bf16 v[148:151], a[104:107], a[20:23], v[148:151]\n"
"global_load_dwordx4 a[188:191], %[vl], s[20:21] offset:3072\n"
"v_mul_f32 v235, v95, v231\n"
"v_mfma_f32_16x16x32_bf16 v[152:155], a[104:107], a[24:27], v[152:155]\n"
"v_exp_f32 v232, v232\n"
"v_mfma_f32_16x16x32_bf16 v[156:159], a[104:107], a[28:31], v[156:159]\n"
"v_exp_f32 v233, v233\n"
"s_waitcnt vmcnt(20)\n"
"v_mfma_f32_16x16x32_bf16 v[172:175], a[108:111], a[28:31], v[172:175]\n"
"s_add_u32 s20, s20, 8192\n"
"s_addc_u32 s21, s21, 0\n"
"global_load_dwordx4 a[64:67], %[vl], s[20:21] offset:-4096\n"
"v_exp_f32 v234, v234\n"
"v_mfma_f32_16x16x32_bf16 v[168:171], a[108:111], a[24:27], v[168:171]\n"
"v_exp_f32 v235, v235\n"
"v_fma_f32 v232, v232, v230, v230\n"
"v_mfma_f32_16x16x32_bf16 v[164:167], a[108:111], a[20:23], v[164:167]\n"
"v_fma_f32 v233, v233, v230, v230\n"
"v_fma_f32 v234, v234, v230, v230\n"
"v_fma_f32 v235, v235, v230, v230\n"
"v_mfma_f32_16x16x32_bf16 v[160:163], a[108:111], a[16:19], v[160:163]\n"
"global_load_dwordx4 a[68:71], %[vl], s[20:21] offset:-3072\n"
"s_waitcnt vmcnt(21)\n"
"v_mfma_f32_16x16x32_bf16 v[176:179], a[112:115], a[16:19], v[176:179]\n"
"s_waitcnt lgkmcnt(0)\n"
"s_barrier\n"
"ds_read_b128 a[0:3], v246 offset:16384\n"
"v_mfma_f32_16x16x32_bf16 v[180:183], a[112:115], a[20:23], v[180:183]\n"
"ds_read_b128 a[4:7], v246 offset:17408\n"
"v_mfma_f32_16x16x32_bf16 v[184:187], a[112:115], a[24:27], v[184:187]\n"
"global_load_dwordx4 a[72:75], %[vl], s[20:21] offset:-2048\n"
"ds_read_b128 a[8:11], v246 offset:18432\n"
"v_mfma_f32_16x16x32_bf16 v[188:191], a[112:115], a[28:31], v[188:191]\n"
"ds_read_b128 a[12:15], v246 offset:19456\n"
"v_rcp_f32 v232, v232\n"
"s_waitcnt vmcnt(21)\n"
"v_mfma_f32_16x16x32_bf16 v[204:207], a[116:119], a[28:31], v[204:207]\n"
"v_rcp_f32 v233, v233\n"
"v_rcp_f32 v234, v234\n"
"v_mfma_f32_16x16x32_bf16 v[200:203], a[116:119], a[24:27], v[200:203]\n"
"global_load_dwordx4 a[76:79], %[vl], s[20:21] offset:-1024\n"
"v_mfma_f32_16x16x32_bf16 v[196:199], a[116:119], a[20:23], v[196:199]\n"
"v_rcp_f32 v235, v235\n"
"v_mul_f32 v92, v92, v232\n"
"v_mul_f32 v93, v93, v233\n"
"v_mfma_f32_16x16x32_bf16 v[192:195], a[116:119], a[16:19], v[192:195]\n"
"v_mul_f32 v94, v94, v234\n"
"v_mul_f32 v95, v95, v235\n"
"s_waitcnt vmcnt(21)\n"
"v_mfma_f32_16x16x32_bf16 v[208:211], a[120:123], a[16:19], v[208:211]\n"
"global_load_dwordx4 a[80:83], %[vl], s[20:21] offset:0\n"
"v_cvt_pk_bf16_f32 v242, v92, v93\n"
"v_cvt_pk_bf16_f32 v243, v94, v95\n"
"v_mfma_f32_16x16x32_bf16 v[212:215], a[120:123], a[20:23], v[212:215]\n"
"v_mul_f32 v232, v108, v231\n"
"v_mul_f32 v233, v109, v231\n"
"v_mfma_f32_16x16x32_bf16 v[216:219], a[120:123], a[24:27], v[216:219]\n"
"v_mul_f32 v234, v110, v231\n"
"v_mul_f32 v235, v111, v231\n"
"v_mfma_f32_16x16x32_bf16 v[220:223], a[120:123], a[28:31], v[220:223]\n"
"global_load_dwordx4 a[84:87], %[vl], s[20:21] offset:1024\n"
"v_exp_f32 v232, v232\n"
"s_waitcnt lgkmcnt(0)\n"
"s_waitcnt vmcnt(22)\n"
"v_mfma_f32_16x16x32_bf16 a[32:35], a[124:127], a[0:3], a[32:35]\n"
"ds_read_b128 a[16:19], v246 offset:24576\n"
"v_mfma_f32_16x16x32_bf16 a[36:39], a[124:127], a[4:7], a[36:39]\n"
"ds_read_b128 a[20:23], v246 offset:25600\n"
"v_mfma_f32_16x16x32_bf16 a[40:43], a[124:127], a[8:11], a[40:43]\n"
"ds_read_b128 a[24:27], v246 offset:26624\n"
"v_mfma_f32_16x16x32_bf16 a[44:47], a[124:127], a[12:15], a[44:47]\n"
"global_load_dwordx4 a[88:91], %[vl], s[20:21] offset:2048\n"
"ds_read_b128 a[28:31], v246 offset:27648\n"
"v_exp_f32 v233, v233\n"
"s_waitcnt vmcnt(22)\n"
"v_mfma_f32_16x16x32_bf16 a[60:63], a[128:131], a[12:15], a[60:63]\n"
"v_exp_f32 v234, v234\n"
"v_mfma_f32_16x16x32_bf16 a[56:59], a[128:131], a[8:11], a[56:59]\n"
"v_exp_f32 v235, v235\n"
"v_fma_f32 v232, v232, v230, v230\n"
"v_mfma_f32_16x16x32_bf16 a[52:55], a[128:131], a[4:7], a[52:55]\n"
"global_load_dwordx4 a[92:95], %[vl], s[20:21] offset:3072\n"
"v_fma_f32 v233, v233, v230, v230\n"
"v_fma_f32 v234, v234, v230, v230\n"
"v_mfma_f32_16x16x32_bf16 a[48:51], a[128:131], a[0:3], a[48:51]\n"
"v_fma_f32 v235, v235, v230, v230\n"
"s_waitcnt vmcnt(22)\n"
"v_mfma_f32_16x16x32_bf16 v[0:3], a[132:135], a[0:3], v[0:3]\n"
"v_rcp_f32 v232, v232\n"
"v_rcp_f32 v233, v233\n"
"v_mfma_f32_16x16x32_bf16 v[4:7], a[132:135], a[4:7], v[4:7]\n"
"s_add_u32 s20, s20, 8192\n"
"s_addc_u32 s21, s21, 0\n"
"global_load_dwordx4 a[96:99], %[vl], s[20:21] offset:-4096\n"
"v_mfma_f32_16x16x32_bf16 v[8:11], a[132:135], a[8:11], v[8:11]\n"
"v_rcp_f32 v234, v234\n"
"v_rcp_f32 v235, v235\n"
"v_mfma_f32_16x16x32_bf16 v[12:15], a[132:135], a[12:15], v[12:15]\n"
"v_mul_f32 v108, v108, v232\n"
"v_mul_f32 v109, v109, v233\n"
"s_waitcnt vmcnt(22)\n"
"v_mfma_f32_16x16x32_bf16 v[28:31], a[136:139], a[12:15], v[28:31]\n"
"global_load_dwordx4 a[100:103], %[vl], s[20:21] offset:-3072\n"
"v_mul_f32 v110, v110, v234\n"
"v_mul_f32 v111, v111, v235\n"
"v_mfma_f32_16x16x32_bf16 v[24:27], a[136:139], a[8:11], v[24:27]\n"
"v_cvt_pk_bf16_f32 v244, v108, v109\n"
"v_cvt_pk_bf16_f32 v245, v110, v111\n"
"v_mfma_f32_16x16x32_bf16 v[20:23], a[136:139], a[4:7], v[20:23]\n"
"ds_write_b128 %[vhw], v[242:245] offset:44032\n"
"v_mul_f32 v232, v112, v225\n"
"v_mul_f32 v233, v113, v225\n"
"v_mfma_f32_16x16x32_bf16 v[16:19], a[136:139], a[0:3], v[16:19]\n"
"global_load_dwordx4 a[104:107], %[vl], s[20:21] offset:-2048\n"
"v_mul_f32 v234, v114, v225\n"
"s_waitcnt vmcnt(23)\n"
"v_mfma_f32_16x16x32_bf16 v[144:147], a[140:143], a[0:3], v[144:147]\n"
"v_mul_f32 v235, v115, v225\n"
"v_exp_f32 v232, v232\n"
"v_mfma_f32_16x16x32_bf16 v[148:151], a[140:143], a[4:7], v[148:151]\n"
"v_exp_f32 v233, v233\n"
"v_mfma_f32_16x16x32_bf16 v[152:155], a[140:143], a[8:11], v[152:155]\n"
"global_load_dwordx4 a[108:111], %[vl], s[20:21] offset:-1024\n"
"v_exp_f32 v234, v234\n"
"v_mfma_f32_16x16x32_bf16 v[156:159], a[140:143], a[12:15], v[156:159]\n"
"v_exp_f32 v235, v235\n"
"s_waitcnt vmcnt(23)\n"
"v_mfma_f32_16x16x32_bf16 v[172:175], a[144:147], a[12:15], v[172:175]\n"
"v_fma_f32 v232, v232, v224, v224\n"
"v_fma_f32 v233, v233, v224, v224\n"
"v_fma_f32 v234, v234, v224, v224\n"
"v_mfma_f32_16x16x32_bf16 v[168:171], a[144:147], a[8:11], v[168:171]\n"
"global_load_dwordx4 a[112:115], %[vl], s[20:21] offset:0\n"
"v_fma_f32 v235, v235, v224, v224\n"
"v_mfma_f32_16x16x32_bf16 v[164:167], a[144:147], a[4:7], v[164:167]\n"
"v_rcp_f32 v232, v232\n"
"v_mfma_f32_16x16x32_bf16 v[160:163], a[144:147], a[0:3], v[160:163]\n"
"v_rcp_f32 v233, v233\n"
"s_waitcnt vmcnt(23)\n"
"v_mfma_f32_16x16x32_bf16 v[176:179], a[148:151], a[0:3], v[176:179]\n"
"v_rcp_f32 v234, v234\n"
"v_rcp_f32 v235, v235\n"
"v_mfma_f32_16x16x32_bf16 v[180:183], a[148:151], a[4:7], v[180:183]\n"
"global_load_dwordx4 a[116:119], %[vl], s[20:21] offset:1024\n"
"v_mul_f32 v112, v112, v232\n"
"v_mfma_f32_16x16x32_bf16 v[184:187], a[148:151], a[8:11], v[184:187]\n"
"v_mul_f32 v113, v113, v233\n"
"v_mul_f32 v114, v114, v234\n"
"v_mul_f32 v115, v115, v235\n"
"v_mfma_f32_16x16x32_bf16 v[188:191], a[148:151], a[12:15], v[188:191]\n"
"v_cvt_pk_bf16_f32 v238, v112, v113\n"
"v_cvt_pk_bf16_f32 v239, v114, v115\n"
"s_waitcnt vmcnt(23)\n"
"v_mfma_f32_16x16x32_bf16 v[204:207], a[152:155], a[12:15], v[204:207]\n"
"global_load_dwordx4 a[120:123], %[vl], s[20:21] offset:2048\n"
"v_mul_f32 v232, v128, v225\n"
"v_mul_f32 v233, v129, v225\n"
"v_mfma_f32_16x16x32_bf16 v[200:203], a[152:155], a[8:11], v[200:203]\n"
"v_mul_f32 v234, v130, v225\n"
"v_mul_f32 v235, v131, v225\n"
"v_mfma_f32_16x16x32_bf16 v[196:199], a[152:155], a[4:7], v[196:199]\n"
"v_exp_f32 v232, v232\n"
"v_mfma_f32_16x16x32_bf16 v[192:195], a[152:155], a[0:3], v[192:195]\n"
"global_load_dwordx4 a[124:127], %[vl], s[20:21] offset:3072\n"
"v_exp_f32 v233, v233\n"
"s_waitcnt vmcnt(24)\n"
"v_mfma_f32_16x16x32_bf16 v[208:211], a[156:159], a[0:3], v[208:211]\n"
"v_exp_f32 v234, v234\n"
"v_mfma_f32_16x16x32_bf16 v[212:215], a[156:159], a[4:7], v[212:215]\n"
"v_exp_f32 v235, v235\n"
"v_fma_f32 v232, v232, v224, v224\n"
"v_mfma_f32_16x16x32_bf16 v[216:219], a[156:159], a[8:11], v[216:219]\n"
"s_add_u32 s20, s20, 8192\n"
"s_addc_u32 s21, s21, 0\n"
"global_load_dwordx4 a[128:131], %[vl], s[20:21] offset:-4096\n"
"v_fma_f32 v233, v233, v224, v224\n"
"v_fma_f32 v234, v234, v224, v224\n"
"v_mfma_f32_16x16x32_bf16 v[220:223], a[156:159], a[12:15], v[220:223]\n"
"v_fma_f32 v235, v235, v224, v224\n"
"s_waitcnt lgkmcnt(1)\n"
"s_waitcnt vmcnt(24)\n"
"v_mfma_f32_16x16x32_bf16 a[32:35], a[160:163], a[16:19], a[32:35]\n"
"ds_read_b128 a[0:3], v246 offset:32768\n"
"v_mfma_f32_16x16x32_bf16 a[36:39], a[160:163], a[20:23], a[36:39]\n"
"global_load_dwordx4 a[132:135], %[vl], s[20:21] offset:-3072\n"
"ds_read_b128 a[4:7], v246 offset:33792\n"
"v_mfma_f32_16x16x32_bf16 a[40:43], a[160:163], a[24:27], a[40:43]\n"
"ds_read_b128 a[8:11], v246 offset:34816\n"
"v_mfma_f32_16x16x32_bf16 a[44:47], a[160:163], a[28:31], a[44:47]\n"
"ds_read_b128 a[12:15], v246 offset:35840\n"
"v_rcp_f32 v232, v232\n"
"s_waitcnt vmcnt(24)\n"
"v_mfma_f32_16x16x32_bf16 a[60:63], a[164:167], a[28:31], a[60:63]\n"
"global_load_dwordx4 a[136:139], %[vl], s[20:21] offset:-2048\n"
"v_rcp_f32 v233, v233\n"
"v_mfma_f32_16x16x32_bf16 a[56:59], a[164:167], a[24:27], a[56:59]\n"
"v_rcp_f32 v234, v234\n"
"v_mfma_f32_16x16x32_bf16 a[52:55], a[164:167], a[20:23], a[52:55]\n"
"v_rcp_f32 v235, v235\n"
"v_mul_f32 v128, v128, v232\n"
"v_mul_f32 v129, v129, v233\n"
"v_mfma_f32_16x16x32_bf16 a[48:51], a[164:167], a[16:19], a[48:51]\n"
"global_load_dwordx4 a[140:143], %[vl], s[20:21] offset:-1024\n"
"v_mul_f32 v130, v130, v234\n"
"s_waitcnt vmcnt(25)\n"
"v_mfma_f32_16x16x32_bf16 v[0:3], a[168:171], a[16:19], v[0:3]\n"
"v_mul_f32 v131, v131, v235\n"
"v_cvt_pk_bf16_f32 v240, v128, v129\n"
"v_cvt_pk_bf16_f32 v241, v130, v131\n"
"v_mfma_f32_16x16x32_bf16 v[4:7], a[168:171], a[20:23], v[4:7]\n"
"ds_write_b128 %[vhw], v[238:241] offset:45056\n"
"v_mul_f32 v232, v116, v227\n"
"v_mfma_f32_16x16x32_bf16 v[8:11], a[168:171], a[24:27], v[8:11]\n"
"global_load_dwordx4 a[144:147], %[vl], s[20:21] offset:0\n"
"v_mul_f32 v233, v117, v227\n"
"v_mul_f32 v234, v118, v227\n"
"v_mfma_f32_16x16x32_bf16 v[12:15], a[168:171], a[28:31], v[12:15]\n"
"v_mul_f32 v235, v119, v227\n"
"s_waitcnt vmcnt(25)\n"
"v_mfma_f32_16x16x32_bf16 v[28:31], a[172:175], a[28:31], v[28:31]\n"
"v_exp_f32 v232, v232\n"
"v_exp_f32 v233, v233\n"
"v_mfma_f32_16x16x32_bf16 v[24:27], a[172:175], a[24:27], v[24:27]\n"
"v_exp_f32 v234, v234\n"
"v_mfma_f32_16x16x32_bf16 v[20:23], a[172:175], a[20:23], v[20:23]\n"
"global_load_dwordx4 a[148:151], %[vl], s[20:21] offset:1024\n"
"v_exp_f32 v235, v235\n"
"v_mfma_f32_16x16x32_bf16 v[16:19], a[172:175], a[16:19], v[16:19]\n"
"v_fma_f32 v232, v232, v226, v226\n"
"v_fma_f32 v233, v233, v226, v226\n"
"s_waitcnt vmcnt(25)\n"
"v_mfma_f32_16x16x32_bf16 v[144:147], a[176:179], a[16:19], v[144:147]\n"
"v_fma_f32 v234, v234, v226, v226\n"
"v_fma_f32 v235, v235, v226, v226\n"
"v_mfma_f32_16x16x32_bf16 v[148:151], a[176:179], a[20:23], v[148:151]\n"
"global_load_dwordx4 a[152:155], %[vl], s[20:21] offset:2048\n"
"v_rcp_f32 v232, v232\n"
"v_mfma_f32_16x16x32_bf16 v[152:155], a[176:179], a[24:27], v[152:155]\n"
"v_rcp_f32 v233, v233\n"
"v_mfma_f32_16x16x32_bf16 v[156:159], a[176:179], a[28:31], v[156:159]\n"
"v_rcp_f32 v234, v234\n"
"s_waitcnt vmcnt(25)\n"
"v_mfma_f32_16x16x32_bf16 v[172:175], a[180:183], a[28:31], v[172:175]\n"
"global_load_dwordx4 a[156:159], %[vl], s[20:21] offset:3072\n"
"v_rcp_f32 v235, v235\n"
"v_mul_f32 v116, v116, v232\n"
"v_mfma_f32_16x16x32_bf16 v[168:171], a[180:183], a[24:27], v[168:171]\n"
"v_mul_f32 v117, v117, v233\n"
"v_mul_f32 v118, v118, v234\n"
"v_mfma_f32_16x16x32_bf16 v[164:167], a[180:183], a[20:23], v[164:167]\n"
"v_mul_f32 v119, v119, v235\n"
"v_cvt_pk_bf16_f32 v242, v116, v117\n"
"v_cvt_pk_bf16_f32 v243, v118, v119\n"
"v_mfma_f32_16x16x32_bf16 v[160:163], a[180:183], a[16:19], v[160:163]\n"
"s_add_u32 s20, s20, 8192\n"
"s_addc_u32 s21, s21, 0\n"
"global_load_dwordx4 a[160:163], %[vl], s[20:21] offset:-4096\n"
"v_mul_f32 v232, v132, v227\n"
"s_waitcnt vmcnt(26)\n"
"v_mfma_f32_16x16x32_bf16 v[176:179], a[184:187], a[16:19], v[176:179]\n"
"v_mul_f32 v233, v133, v227\n"
"v_mul_f32 v234, v134, v227\n"
"v_mul_f32 v235, v135, v227\n"
"v_mfma_f32_16x16x32_bf16 v[180:183], a[184:187], a[20:23], v[180:183]\n"
"v_exp_f32 v232, v232\n"
"v_mfma_f32_16x16x32_bf16 v[184:187], a[184:187], a[24:27], v[184:187]\n"
"global_load_dwordx4 a[164:167], %[vl], s[20:21] offset:-3072\n"
"v_exp_f32 v233, v233\n"
"v_mfma_f32_16x16x32_bf16 v[188:191], a[184:187], a[28:31], v[188:191]\n"
"v_exp_f32 v234, v234\n"
"s_waitcnt vmcnt(26)\n"
"v_mfma_f32_16x16x32_bf16 v[204:207], a[188:191], a[28:31], v[204:207]\n"
"v_exp_f32 v235, v235\n"
"v_fma_f32 v232, v232, v226, v226\n"
"v_mfma_f32_16x16x32_bf16 v[200:203], a[188:191], a[24:27], v[200:203]\n"
"global_load_dwordx4 a[168:171], %[vl], s[20:21] offset:-2048\n"
"v_fma_f32 v233, v233, v226, v226\n"
"v_mfma_f32_16x16x32_bf16 v[196:199], a[188:191], a[20:23], v[196:199]\n"
"v_fma_f32 v234, v234, v226, v226\n"
"v_fma_f32 v235, v235, v226, v226\n"
"v_mfma_f32_16x16x32_bf16 v[192:195], a[188:191], a[16:19], v[192:195]\n"
"v_rcp_f32 v232, v232\n"
"s_waitcnt vmcnt(26)\n"
"v_mfma_f32_16x16x32_bf16 v[208:211], a[64:67], a[16:19], v[208:211]\n"
"global_load_dwordx4 a[172:175], %[vl], s[20:21] offset:-1024\n"
"v_rcp_f32 v233, v233\n"
"v_mfma_f32_16x16x32_bf16 v[212:215], a[64:67], a[20:23], v[212:215]\n"
"v_rcp_f32 v234, v234\n"
"v_mfma_f32_16x16x32_bf16 v[216:219], a[64:67], a[24:27], v[216:219]\n"
"v_rcp_f32 v235, v235\n"
"v_mul_f32 v132, v132, v232\n"
"v_mul_f32 v133, v133, v233\n"
"v_mfma_f32_16x16x32_bf16 v[220:223], a[64:67], a[28:31], v[220:223]\n"
"global_load_dwordx4 a[176:179], %[vl], s[20:21] offset:0\n"
"v_mul_f32 v134, v134, v234\n"
"s_waitcnt lgkmcnt(1)\n"
"s_waitcnt vmcnt(27)\n"
"v_mfma_f32_16x16x32_bf16 a[32:35], a[68:71], a[0:3], a[32:35]\n"
"ds_read_b128 a[16:19], v246 offset:40960\n"
"v_mfma_f32_16x16x32_bf16 a[36:39], a[68:71], a[4:7], a[36:39]\n"
"ds_read_b128 a[20:23], v246 offset:41984\n"
"v_mfma_f32_16x16x32_bf16 a[40:43], a[68:71], a[8:11], a[40:43]\n"
"ds_read_b128 a[24:27], v246 offset:43008\n"
"v_mfma_f32_16x16x32_bf16 a[44:47], a[68:71], a[12:15], a[44:47]\n"
"global_load_dwordx4 a[180:183], %[vl], s[20:21] offset:1024\n"
"ds_read_b128 a[28:31], v246 offset:44032\n"
"v_mul_f32 v135, v135, v235\n"
"v_cvt_pk_bf16_f32 v244, v132, v133\n"
"s_waitcnt vmcnt(27)\n"
"v_mfma_f32_16x16x32_bf16 a[60:63], a[72:75], a[12:15], a[60:63]\n"
"v_cvt_pk_bf16_f32 v245, v134, v135\n"
"ds_write_b128 %[vhw], v[242:245] offset:46080\n"
"v_mul_f32 v232, v120, v229\n"
"v_mfma_f32_16x16x32_bf16 a[56:59], a[72:75], a[8:11], a[56:59]\n"
"v_mul_f32 v233, v121, v229\n"
"v_mul_f32 v234, v122, v229\n"
"v_mfma_f32_16x16x32_bf16 a[52:55], a[72:75], a[4:7], a[52:55]\n"
"global_load_dwordx4 a[184:187], %[vl], s[20:21] offset:2048\n"
"v_mul_f32 v235, v123, v229\n"
"v_mfma_f32_16x16x32_bf16 a[48:51], a[72:75], a[0:3], a[48:51]\n"
"v_exp_f32 v232, v232\n"
"s_waitcnt vmcnt(27)\n"
"v_mfma_f32_16x16x32_bf16 v[0:3], a[76:79], a[0:3], v[0:3]\n"
"v_exp_f32 v233, v233\n"
"v_exp_f32 v234, v234\n"
"v_mfma_f32_16x16x32_bf16 v[4:7], a[76:79], a[4:7], v[4:7]\n"
"global_load_dwordx4 a[188:191], %[vl], s[20:21] offset:3072\n"
"v_mfma_f32_16x16x32_bf16 v[8:11], a[76:79], a[8:11], v[8:11]\n"
"v_exp_f32 v235, v235\n"
"v_fma_f32 v232, v232, v228, v228\n"
"v_fma_f32 v233, v233, v228, v228\n"
"v_mfma_f32_16x16x32_bf16 v[12:15], a[76:79], a[12:15], v[12:15]\n"
"v_fma_f32 v234, v234, v228, v228\n"
"v_fma_f32 v235, v235, v228, v228\n"
"s_waitcnt vmcnt(27)\n"
"v_mfma_f32_16x16x32_bf16 v[28:31], a[80:83], a[12:15], v[28:31]\n"
"s_add_u32 s20, s20, 8192\n"
"s_addc_u32 s21, s21, 0\n"
"global_load_dwordx4 a[64:67], %[vl], s[20:21] offset:-4096\n"
"v_rcp_f32 v232, v232\n"
"v_mfma_f32_16x16x32_bf16 v[24:27], a[80:83], a[8:11], v[24:27]\n"
"v_rcp_f32 v233, v233\n"
"v_mfma_f32_16x16x32_bf16 v[20:23], a[80:83], a[4:7], v[20:23]\n"
"v_rcp_f32 v234, v234\n"
"v_mfma_f32_16x16x32_bf16 v[16:19], a[80:83], a[0:3], v[16:19]\n"
"global_load_dwordx4 a[68:71], %[vl], s[20:21] offset:-3072\n"
"v_rcp_f32 v235, v235\n"
"s_waitcnt vmcnt(28)\n"
"v_mfma_f32_16x16x32_bf16 v[144:147], a[84:87], a[0:3], v[144:147]\n"
"v_mul_f32 v120, v120, v232\n"
"v_mul_f32 v121, v121, v233\n"
"v_mul_f32 v122, v122, v234\n"
"v_mfma_f32_16x16x32_bf16 v[148:151], a[84:87], a[4:7], v[148:151]\n"
"v_mul_f32 v123, v123, v235\n"
"v_cvt_pk_bf16_f32 v238, v120, v121\n"
"v_mfma_f32_16x16x32_bf16 v[152:155], a[84:87], a[8:11], v[152:155]\n"
"global_load_dwordx4 a[72:75], %[vl], s[20:21] offset:-2048\n"
"v_cvt_pk_bf16_f32 v239, v122, v123\n"
"v_mul_f32 v232, v136, v229\n"
"v_mfma_f32_16x16x32_bf16 v[156:159], a[84:87], a[12:15], v[156:159]\n"
"v_mul_f32 v233, v137, v229\n"
"v_mul_f32 v234, v138, v229\n"
"s_waitcnt vmcnt(28)\n"
"v_mfma_f32_16x16x32_bf16 v[172:175], a[88:91], a[12:15], v[172:175]\n"
"v_mul_f32 v235, v139, v229\n"
"v_exp_f32 v232, v232\n"
"v_mfma_f32_16x16x32_bf16 v[168:171], a[88:91], a[8:11], v[168:171]\n"
"global_load_dwordx4 a[76:79], %[vl], s[20:21] offset:-1024\n"
"v_mfma_f32_16x16x32_bf16 v[164:167], a[88:91], a[4:7], v[164:167]\n"
"v_exp_f32 v233, v233\n"
"v_exp_f32 v234, v234\n"
"v_mfma_f32_16x16x32_bf16 v[160:163], a[88:91], a[0:3], v[160:163]\n"
"v_exp_f32 v235, v235\n"
"s_waitcnt vmcnt(28)\n"
"v_mfma_f32_16x16x32_bf16 v[176:179], a[92:95], a[0:3], v[176:179]\n"
"v_fma_f32 v232, v232, v228, v228\n"
"v_fma_f32 v233, v233, v228, v228\n"
"v_fma_f32 v234, v234, v228, v228\n"
"v_mfma_f32_16x16x32_bf16 v[180:183], a[92:95], a[4:7], v[180:183]\n"
"global_load_dwordx4 a[80:83], %[vl], s[20:21] offset:0\n"
"v_fma_f32 v235, v235, v228, v228\n"
"v_mfma_f32_16x16x32_bf16 v[184:187], a[92:95], a[8:11], v[184:187]\n"
"v_rcp_f32 v232, v232\n"
"v_mfma_f32_16x16x32_bf16 v[188:191], a[92:95], a[12:15], v[188:191]\n"
"v_rcp_f32 v233, v233\n"
"s_waitcnt vmcnt(28)\n"
"v_mfma_f32_16x16x32_bf16 v[204:207], a[96:99], a[12:15], v[204:207]\n"
"global_load_dwordx4 a[84:87], %[vl], s[20:21] offset:1024\n"
"v_rcp_f32 v234, v234\n"
"v_mfma_f32_16x16x32_bf16 v[200:203], a[96:99], a[8:11], v[200:203]\n"
"v_rcp_f32 v235, v235\n"
"v_mul_f32 v136, v136, v232\n"
"v_mfma_f32_16x16x32_bf16 v[196:199], a[96:99], a[4:7], v[196:199]\n"
"v_mul_f32 v137, v137, v233\n"
"v_mul_f32 v138, v138, v234\n"
"v_mul_f32 v139, v139, v235\n"
"v_mfma_f32_16x16x32_bf16 v[192:195], a[96:99], a[0:3], v[192:195]\n"
"global_load_dwordx4 a[88:91], %[vl], s[20:21] offset:2048\n"
"v_cvt_pk_bf16_f32 v240, v136, v137\n"
"s_waitcnt vmcnt(29)\n"
"v_mfma_f32_16x16x32_bf16 v[208:211], a[100:103], a[0:3], v[208:211]\n"
"v_cvt_pk_bf16_f32 v241, v138, v139\n"
"ds_write_b128 %[vhw], v[238:241] offset:47104\n"
"v_mul_f32 v232, v124, v231\n"
"v_mfma_f32_16x16x32_bf16 v[212:215], a[100:103], a[4:7], v[212:215]\n"
"v_mul_f32 v233, v125, v231\n"
"v_mul_f32 v234, v126, v231\n"
"v_mfma_f32_16x16x32_bf16 v[216:219], a[100:103], a[8:11], v[216:219]\n"
"global_load_dwordx4 a[92:95], %[vl], s[20:21] offset:3072\n"
"v_mul_f32 v235, v127, v231\n"
"v_mfma_f32_16x16x32_bf16 v[220:223], a[100:103], a[12:15], v[220:223]\n"
"v_exp_f32 v232, v232\n"
"s_waitcnt lgkmcnt(2)\n"
"s_waitcnt vmcnt(29)\n"
"v_mfma_f32_16x16x32_bf16 a[32:35], a[104:107], a[16:19], a[32:35]\n"
"v_exp_f32 v233, v233\n"
"v_exp_f32 v234, v234\n"
"v_mfma_f32_16x16x32_bf16 a[36:39], a[104:107], a[20:23], a[36:39]\n"
"s_add_u32 s20, s20, 8192\n"
"s_addc_u32 s21, s21, 0\n"
"global_load_dwordx4 a[96:99], %[vl], s[20:21] offset:-4096\n"
"v_mfma_f32_16x16x32_bf16 a[40:43], a[104:107], a[24:27], a[40:43]\n"
"v_exp_f32 v235, v235\n"
"v_fma_f32 v232, v232, v230, v230\n"
"v_fma_f32 v233, v233, v230, v230\n"
"v_mfma_f32_16x16x32_bf16 a[44:47], a[104:107], a[28:31], a[44:47]\n"
"v_fma_f32 v234, v234, v230, v230\n"
"v_fma_f32 v235, v235, v230, v230\n"
"s_waitcnt vmcnt(29)\n"
"v_mfma_f32_16x16x32_bf16 a[60:63], a[108:111], a[28:31], a[60:63]\n"
"global_load_dwordx4 a[100:103], %[vl], s[20:21] offset:-3072\n"
"v_rcp_f32 v232, v232\n"
"v_mfma_f32_16x16x32_bf16 a[56:59], a[108:111], a[24:27], a[56:59]\n"
"v_rcp_f32 v233, v233\n"
"v_mfma_f32_16x16x32_bf16 a[52:55], a[108:111], a[20:23], a[52:55]\n"
"v_rcp_f32 v234, v234\n"
"v_mfma_f32_16x16x32_bf16 a[48:51], a[108:111], a[16:19], a[48:51]\n"
"global_load_dwordx4 a[104:107], %[vl], s[20:21] offset:-2048\n"
"v_rcp_f32 v235, v235\n"
"s_waitcnt vmcnt(30)\n"
"v_mfma_f32_16x16x32_bf16 v[0:3], a[112:115], a[16:19], v[0:3]\n"
"v_mul_f32 v124, v124, v232\n"
"v_mul_f32 v125, v125, v233\n"
"v_mul_f32 v126, v126, v234\n"
"v_mfma_f32_16x16x32_bf16 v[4:7], a[112:115], a[20:23], v[4:7]\n"
"v_mul_f32 v127, v127, v235\n"
"v_cvt_pk_bf16_f32 v242, v124, v125\n"
"v_mfma_f32_16x16x32_bf16 v[8:11], a[112:115], a[24:27], v[8:11]\n"
"global_load_dwordx4 a[108:111], %[vl], s[20:21] offset:-1024\n"
"v_cvt_pk_bf16_f32 v243, v126, v127\n"
"v_mul_f32 v232, v140, v231\n"
"v_mfma_f32_16x16x32_bf16 v[12:15], a[112:115], a[28:31], v[12:15]\n"
"v_mul_f32 v233, v141, v231\n"
"v_mul_f32 v234, v142, v231\n"
"s_waitcnt vmcnt(30)\n"
"v_mfma_f32_16x16x32_bf16 v[28:31], a[116:119], a[28:31], v[28:31]\n"
"v_mul_f32 v235, v143, v231\n"
"v_exp_f32 v232, v232\n"
"v_mfma_f32_16x16x32_bf16 v[24:27], a[116:119], a[24:27], v[24:27]\n"
"v_exp_f32 v233, v233\n"
"v_mfma_f32_16x16x32_bf16 v[20:23], a[116:119], a[20:23], v[20:23]\n"
"global_load_dwordx4 a[112:115], %[vl], s[20:21] offset:0\n"
"v_exp_f32 v234, v234\n"
"v_mfma_f32_16x16x32_bf16 v[16:19], a[116:119], a[16:19], v[16:19]\n"
"v_exp_f32 v235, v235\n"
"s_waitcnt vmcnt(30)\n"
"v_mfma_f32_16x16x32_bf16 v[144:147], a[120:123], a[16:19], v[144:147]\n"
"v_fma_f32 v232, v232, v230, v230\n"
"v_fma_f32 v233, v233, v230, v230\n"
"v_fma_f32 v234, v234, v230, v230\n"
"v_mfma_f32_16x16x32_bf16 v[148:151], a[120:123], a[20:23], v[148:151]\n"
"global_load_dwordx4 a[116:119], %[vl], s[20:21] offset:1024\n"
"v_fma_f32 v235, v235, v230, v230\n"
"v_mfma_f32_16x16x32_bf16 v[152:155], a[120:123], a[24:27], v[152:155]\n"
"v_rcp_f32 v232, v232\n"
"v_mfma_f32_16x16x32_bf16 v[156:159], a[120:123], a[28:31], v[156:159]\n"
"v_rcp_f32 v233, v233\n"
"s_waitcnt vmcnt(30)\n"
"v_mfma_f32_16x16x32_bf16 v[172:175], a[124:127], a[28:31], v[172:175]\n"
"global_load_dwordx4 a[120:123], %[vl], s[20:21] offset:2048\n"
"v_rcp_f32 v234, v234\n"
"v_mfma_f32_16x16x32_bf16 v[168:171], a[124:127], a[24:27], v[168:171]\n"
"v_rcp_f32 v235, v235\n"
"v_mul_f32 v140, v140, v232\n"
"v_mfma_f32_16x16x32_bf16 v[164:167], a[124:127], a[20:23], v[164:167]\n"
"v_mul_f32 v141, v141, v233\n"
"v_mul_f32 v142, v142, v234\n"
"v_mul_f32 v143, v143, v235\n"
"v_mfma_f32_16x16x32_bf16 v[160:163], a[124:127], a[16:19], v[160:163]\n"
"global_load_dwordx4 a[124:127], %[vl], s[20:21] offset:3072\n"
"v_cvt_pk_bf16_f32 v244, v140, v141\n"
"s_waitcnt vmcnt(31)\n"
"v_mfma_f32_16x16x32_bf16 v[176:179], a[128:131], a[16:19], v[176:179]\n"
"s_waitcnt lgkmcnt(0)\n"
"s_barrier\n"
"ds_read_b128 a[0:3], v246 offset:45056\n"
"v_mfma_f32_16x16x32_bf16 v[180:183], a[128:131], a[20:23], v[180:183]\n"
"ds_read_b128 a[4:7], v246 offset:46080\n"
"v_mfma_f32_16x16x32_bf16 v[184:187], a[128:131], a[24:27], v[184:187]\n"
"ds_read_b128 a[8:11], v246 offset:47104\n"
"v_mfma_f32_16x16x32_bf16 v[188:191], a[128:131], a[28:31], v[188:191]\n"
"s_add_u32 s20, s20, 8192\n"
"s_addc_u32 s21, s21, 0\n"
"global_load_dwordx4 a[128:131], %[vl], s[20:21] offset:-4096\n"
"ds_read_b128 a[12:15], v246 offset:48128\n"
"v_cvt_pk_bf16_f32 v245, v142, v143\n"
"ds_write_b128 %[vhw], v[242:245] offset:48128\n"
"s_waitcnt vmcnt(31)\n"
"v_mfma_f32_16x16x32_bf16 v[204:207], a[132:135], a[28:31], v[204:207]\n"
"v_mfma_f32_16x16x32_bf16 v[200:203], a[132:135], a[24:27], v[200:203]\n"
"v_mfma_f32_16x16x32_bf16 v[196:199], a[132:135], a[20:23], v[196:199]\n"
"v_mfma_f32_16x16x32_bf16 v[192:195], a[132:135], a[16:19], v[192:195]\n"
"global_load_dwordx4 a[132:135], %[vl], s[20:21] offset:-3072\n"
"s_waitcnt vmcnt(31)\n"
"v_mfma_f32_16x16x32_bf16 v[208:211], a[136:139], a[16:19], v[208:211]\n"
"v_mfma_f32_16x16x32_bf16 v[212:215], a[136:139], a[20:23], v[212:215]\n"
"v_mfma_f32_16x16x32_bf16 v[216:219], a[136:139], a[24:27], v[216:219]\n"
"v_mfma_f32_16x16x32_bf16 v[220:223], a[136:139], a[28:31], v[220:223]\n"
"global_load_dwordx4 a[136:139], %[vl], s[20:21] offset:-2048\n"
"s_waitcnt lgkmcnt(1)\n"
"s_waitcnt vmcnt(31)\n"
"v_mfma_f32_16x16x32_bf16 a[32:35], a[140:143], a[0:3], a[32:35]\n"
"ds_read_b128 a[16:19], v246 offset:49152\n"
"v_mfma_f32_16x16x32_bf16 a[36:39], a[140:143], a[4:7], a[36:39]\n"
"ds_read_b128 a[20:23], v246 offset:50176\n"
"v_mfma_f32_16x16x32_bf16 a[40:43], a[140:143], a[8:11], a[40:43]\n"
"ds_read_b128 a[24:27], v246 offset:51200\n"
"v_mfma_f32_16x16x32_bf16 a[44:47], a[140:143], a[12:15], a[44:47]\n"
"global_load_dwordx4 a[140:143], %[vl], s[20:21] offset:-1024\n"
"ds_read_b128 a[28:31], v246 offset:52224\n"
"v_lshrrev_b32 v224, 2, %[vl]\n"
"v_lshlrev_b32 v230, 4, %[vl]\n"
"s_waitcnt vmcnt(31)\n"
"v_mfma_f32_16x16x32_bf16 a[60:63], a[144:147], a[12:15], a[60:63]\n"
"v_and_b32 v224, 0xc0, v224\n"
"v_mov_b32 v231, 1\n"
"v_add_u32 v225, %[gs], v224\n"
"v_mfma_f32_16x16x32_bf16 a[56:59], a[144:147], a[8:11], a[56:59]\n"
"global_load_dwordx4 v[128:131], v225, %[gb] offset:0\n"
"global_load_dwordx4 v[132:135], v225, %[gb] offset:16\n"
"v_mfma_f32_16x16x32_bf16 a[52:55], a[144:147], a[4:7], a[52:55]\n"
"global_load_dwordx4 v[136:139], v225, %[gb] offset:32\n"
"global_load_dwordx4 v[140:143], v225, %[gb] offset:48\n"
"v_and_b32 v226, 0xf0, %[vl]\n"
"v_mfma_f32_16x16x32_bf16 a[48:51], a[144:147], a[0:3], a[48:51]\n"
"global_load_dwordx4 a[144:147], %[vl], s[20:21] offset:0\n"
"v_mul_u32_u24 v227, 72, v226\n"
"s_waitcnt vmcnt(35)\n"
"v_mfma_f32_16x16x32_bf16 v[208:211], a[148:151], a[0:3], v[208:211]\n"
"v_lshl_add_u32 v226, v224, 3, v226\n"
"v_lshrrev_b32 v228, 1, v224\n"
"v_add_u32 v226, %[xs], v226\n"
"v_mfma_f32_16x16x32_bf16 v[212:215], a[148:151], a[4:7], v[212:215]\n"
"v_add_u32 v227, v227, v228\n"
"v_add_u32 v229, 4096, %[vl]\n"
"s_mov_b64 s[36:37], %[pb]\n"
"s_and_b32 s37, s37, 0xffff\n"
"s_mov_b32 s39, 0x20000\n"
"s_xor_b32 s40, %[s], 1\n"
"s_mul_i32 s40, s40, 2359296\n"
"s_add_u32 s40, s40, %[so]\n"
"s_xor_b32 s41, %[s], 2\n"
"s_mul_i32 s41, s41, 2359296\n"
"s_add_u32 s41, s41, %[so]\n"
"s_xor_b32 s42, %[s], 3\n"
"s_mul_i32 s42, s42, 2359296\n"
"s_add_u32 s42, s42, %[so]\n"
"s_xor_b32 s43, %[s], 4\n"
"s_mul_i32 s43, s43, 2359296\n"
"s_add_u32 s43, s43, %[so]\n"
"s_xor_b32 s44, %[s], 5\n"
"s_mul_i32 s44, s44, 2359296\n"
"s_add_u32 s44, s44, %[so]\n"
"s_xor_b32 s45, %[s], 6\n"
"s_mul_i32 s45, s45, 2359296\n"
"s_add_u32 s45, s45, %[so]\n"
"s_xor_b32 s46, %[s], 7\n"
"s_mul_i32 s46, s46, 2359296\n"
"s_add_u32 s46, s46, %[so]\n"
"v_mfma_f32_16x16x32_bf16 v[216:219], a[148:151], a[8:11], v[216:219]\n"
"v_add_u32 v228, %[ol], %[vl]\n"
"v_lshl_add_u32 v225, %[w], 10, v228\n"
"v_mfma_f32_16x16x32_bf16 v[220:223], a[148:151], a[12:15], v[220:223]\n"
"global_load_dwordx4 a[148:151], %[vl], s[20:21] offset:1024\n"
"s_waitcnt lgkmcnt(0)\n"
"s_waitcnt vmcnt(35)\n"
"v_mfma_f32_16x16x32_bf16 a[32:35], a[152:155], a[16:19], a[32:35]\n"
"s_barrier\n"
"ds_read_b128 a[0:3], v246 offset:53248\n"
"v_mfma_f32_16x16x32_bf16 a[36:39], a[152:155], a[20:23], a[36:39]\n"
"ds_read_b128 a[4:7], v246 offset:54272\n"
"v_mfma_f32_16x16x32_bf16 a[40:43], a[152:155], a[24:27], a[40:43]\n"
"ds_read_b128 a[8:11], v246 offset:55296\n"
"v_mfma_f32_16x16x32_bf16 a[44:47], a[152:155], a[28:31], a[44:47]\n"
"global_load_dwordx4 a[152:155], %[vl], s[20:21] offset:2048\n"
"ds_read_b128 a[12:15], v246 offset:56320\n"
"s_waitcnt vmcnt(35)\n"
"v_mfma_f32_16x16x32_bf16 a[60:63], a[156:159], a[28:31], a[60:63]\n"
"v_mfma_f32_16x16x32_bf16 a[56:59], a[156:159], a[24:27], a[56:59]\n"
"v_mfma_f32_16x16x32_bf16 a[52:55], a[156:159], a[20:23], a[52:55]\n"
"v_mfma_f32_16x16x32_bf16 a[48:51], a[156:159], a[16:19], a[48:51]\n"
"global_load_dwordx4 a[156:159], %[vl], s[20:21] offset:3072\n"
"s_waitcnt vmcnt(35)\n"
"v_mfma_f32_16x16x32_bf16 v[208:211], a[160:163], a[16:19], v[208:211]\n"
"v_mfma_f32_16x16x32_bf16 v[212:215], a[160:163], a[20:23], v[212:215]\n"
"v_mfma_f32_16x16x32_bf16 v[216:219], a[160:163], a[24:27], v[216:219]\n"
"v_mfma_f32_16x16x32_bf16 v[220:223], a[160:163], a[28:31], v[220:223]\n"
"s_add_u32 s20, s20, 8192\n"
"s_addc_u32 s21, s21, 0\n"
"global_load_dwordx4 a[160:163], %[vl], s[20:21] offset:-4096\n"
"s_waitcnt lgkmcnt(0)\n"
"s_waitcnt vmcnt(35)\n"
"v_mfma_f32_16x16x32_bf16 a[32:35], a[164:167], a[0:3], a[32:35]\n"
"ds_read_b128 a[16:19], v246 offset:61440\n"
"v_mfma_f32_16x16x32_bf16 a[36:39], a[164:167], a[4:7], a[36:39]\n"
"ds_read_b128 a[20:23], v246 offset:62464\n"
"v_mfma_f32_16x16x32_bf16 a[40:43], a[164:167], a[8:11], a[40:43]\n"
"ds_read_b128 a[24:27], v246 offset:63488\n"
"v_mfma_f32_16x16x32_bf16 a[44:47], a[164:167], a[12:15], a[44:47]\n"
"global_load_dwordx4 a[164:167], %[vl], s[20:21] offset:-3072\n"
"ds_read_b128 a[28:31], v246 offset:64512\n"
"s_waitcnt vmcnt(35)\n"
"v_mfma_f32_16x16x32_bf16 a[60:63], a[168:171], a[12:15], a[60:63]\n"
"v_mfma_f32_16x16x32_bf16 a[56:59], a[168:171], a[8:11], a[56:59]\n"
"v_mfma_f32_16x16x32_bf16 a[52:55], a[168:171], a[4:7], a[52:55]\n"
"v_mfma_f32_16x16x32_bf16 a[48:51], a[168:171], a[0:3], a[48:51]\n"
"global_load_dwordx4 a[168:171], %[vl], s[20:21] offset:-2048\n"
"s_waitcnt vmcnt(35)\n"
"v_mfma_f32_16x16x32_bf16 v[208:211], a[172:175], a[0:3], v[208:211]\n"
"v_mfma_f32_16x16x32_bf16 v[212:215], a[172:175], a[4:7], v[212:215]\n"
"v_mfma_f32_16x16x32_bf16 v[216:219], a[172:175], a[8:11], v[216:219]\n"
"v_mfma_f32_16x16x32_bf16 v[220:223], a[172:175], a[12:15], v[220:223]\n"
"global_load_dwordx4 a[172:175], %[vl], s[20:21] offset:-1024\n"
"s_waitcnt lgkmcnt(0)\n"
"s_waitcnt vmcnt(35)\n"
"v_mfma_f32_16x16x32_bf16 a[32:35], a[176:179], a[16:19], a[32:35]\n"
"ds_read_b128 a[0:3], v247 offset:4096\n"
"v_mfma_f32_16x16x32_bf16 a[36:39], a[176:179], a[20:23], a[36:39]\n"
"ds_read_b128 a[4:7], v247 offset:5120\n"
"v_mfma_f32_16x16x32_bf16 a[40:43], a[176:179], a[24:27], a[40:43]\n"
"ds_read_b128 a[8:11], v247 offset:6144\n"
"v_mfma_f32_16x16x32_bf16 a[44:47], a[176:179], a[28:31], a[44:47]\n"
"global_load_dwordx4 a[176:179], %[vl], s[20:21] offset:0\n"
"ds_read_b128 a[12:15], v247 offset:7168\n"
"s_waitcnt vmcnt(35)\n"
"v_mfma_f32_16x16x32_bf16 a[60:63], a[180:183], a[28:31], a[60:63]\n"
"v_mfma_f32_16x16x32_bf16 a[56:59], a[180:183], a[24:27], a[56:59]\n"
"v_mfma_f32_16x16x32_bf16 a[52:55], a[180:183], a[20:23], a[52:55]\n"
"v_mfma_f32_16x16x32_bf16 a[48:51], a[180:183], a[16:19], a[48:51]\n"
"global_load_dwordx4 a[180:183], %[vl], s[20:21] offset:1024\n"
"s_waitcnt vmcnt(35)\n"
"v_mfma_f32_16x16x32_bf16 v[208:211], a[184:187], a[16:19], v[208:211]\n"
"v_mfma_f32_16x16x32_bf16 v[212:215], a[184:187], a[20:23], v[212:215]\n"
"v_mfma_f32_16x16x32_bf16 v[216:219], a[184:187], a[24:27], v[216:219]\n"
"v_mfma_f32_16x16x32_bf16 v[220:223], a[184:187], a[28:31], v[220:223]\n"
"global_load_dwordx4 a[184:187], %[vl], s[20:21] offset:2048\n"
"s_waitcnt lgkmcnt(0)\n"
"s_waitcnt vmcnt(35)\n"
"v_mfma_f32_16x16x32_bf16 a[32:35], a[188:191], a[0:3], a[32:35]\n"
"ds_read_b128 a[16:19], v247 offset:12288\n"
"v_mfma_f32_16x16x32_bf16 a[36:39], a[188:191], a[4:7], a[36:39]\n"
"ds_read_b128 a[20:23], v247 offset:13312\n"
"v_mfma_f32_16x16x32_bf16 a[40:43], a[188:191], a[8:11], a[40:43]\n"
"ds_read_b128 a[24:27], v247 offset:14336\n"
"v_mfma_f32_16x16x32_bf16 a[44:47], a[188:191], a[12:15], a[44:47]\n"
"global_load_dwordx4 a[188:191], %[vl], s[20:21] offset:3072\n"
"ds_read_b128 a[28:31], v247 offset:15360\n"
"s_waitcnt vmcnt(35)\n"
"v_mfma_f32_16x16x32_bf16 a[60:63], a[64:67], a[12:15], a[60:63]\n"
"v_mfma_f32_16x16x32_bf16 a[56:59], a[64:67], a[8:11], a[56:59]\n"
"v_mfma_f32_16x16x32_bf16 a[52:55], a[64:67], a[4:7], a[52:55]\n"
"v_mfma_f32_16x16x32_bf16 a[48:51], a[64:67], a[0:3], a[48:51]\n"
"s_add_u32 s20, s20, 8192\n"
"s_addc_u32 s21, s21, 0\n"
"global_load_dwordx4 a[64:67], %[vl], s[20:21] offset:-4096\n"
"s_waitcnt vmcnt(35)\n"
"v_mfma_f32_16x16x32_bf16 v[208:211], a[68:71], a[0:3], v[208:211]\n"
"v_mfma_f32_16x16x32_bf16 v[212:215], a[68:71], a[4:7], v[212:215]\n"
"v_mfma_f32_16x16x32_bf16 v[216:219], a[68:71], a[8:11], v[216:219]\n"
"v_mfma_f32_16x16x32_bf16 v[220:223], a[68:71], a[12:15], v[220:223]\n"
"global_load_dwordx4 a[68:71], %[vl], s[20:21] offset:-3072\n"
"s_waitcnt lgkmcnt(0)\n"
"s_waitcnt vmcnt(35)\n"
"v_mfma_f32_16x16x32_bf16 a[32:35], a[72:75], a[16:19], a[32:35]\n"
"s_barrier\n"
"ds_read_b128 a[0:3], v246 offset:57344\n"
"v_mfma_f32_16x16x32_bf16 a[36:39], a[72:75], a[20:23], a[36:39]\n"
"ds_read_b128 a[4:7], v246 offset:58368\n"
"v_mfma_f32_16x16x32_bf16 a[40:43], a[72:75], a[24:27], a[40:43]\n"
"ds_read_b128 a[8:11], v246 offset:59392\n"
"v_mfma_f32_16x16x32_bf16 a[44:47], a[72:75], a[28:31], a[44:47]\n"
"global_load_dwordx4 a[72:75], %[vl], s[20:21] offset:-2048\n"
"ds_read_b128 a[12:15], v246 offset:60416\n"
"s_waitcnt vmcnt(35)\n"
"v_mfma_f32_16x16x32_bf16 a[60:63], a[76:79], a[28:31], a[60:63]\n"
"v_mfma_f32_16x16x32_bf16 a[56:59], a[76:79], a[24:27], a[56:59]\n"
"v_mfma_f32_16x16x32_bf16 a[52:55], a[76:79], a[20:23], a[52:55]\n"
"v_mfma_f32_16x16x32_bf16 a[48:51], a[76:79], a[16:19], a[48:51]\n"
"global_load_dwordx4 a[76:79], %[vl], s[20:21] offset:-1024\n"
"s_waitcnt vmcnt(35)\n"
"v_mfma_f32_16x16x32_bf16 v[208:211], a[80:83], a[16:19], v[208:211]\n"
"v_mfma_f32_16x16x32_bf16 v[212:215], a[80:83], a[20:23], v[212:215]\n"
"v_mfma_f32_16x16x32_bf16 v[216:219], a[80:83], a[24:27], v[216:219]\n"
"v_mfma_f32_16x16x32_bf16 v[220:223], a[80:83], a[28:31], v[220:223]\n"
"global_load_dwordx4 a[80:83], %[vl], s[20:21] offset:0\n"
"s_waitcnt lgkmcnt(0)\n"
"s_waitcnt vmcnt(35)\n"
"v_mfma_f32_16x16x32_bf16 a[32:35], a[84:87], a[0:3], a[32:35]\n"
"ds_read_b128 a[16:19], v247 offset:0\n"
"v_mfma_f32_16x16x32_bf16 a[36:39], a[84:87], a[4:7], a[36:39]\n"
"ds_read_b128 a[20:23], v247 offset:1024\n"
"v_mfma_f32_16x16x32_bf16 a[40:43], a[84:87], a[8:11], a[40:43]\n"
"ds_read_b128 a[24:27], v247 offset:2048\n"
"v_mfma_f32_16x16x32_bf16 a[44:47], a[84:87], a[12:15], a[44:47]\n"
"global_load_dwordx4 a[84:87], %[vl], s[20:21] offset:1024\n"
"ds_read_b128 a[28:31], v247 offset:3072\n"
"s_waitcnt vmcnt(35)\n"
"v_mfma_f32_16x16x32_bf16 a[60:63], a[88:91], a[12:15], a[60:63]\n"
"v_mfma_f32_16x16x32_bf16 a[56:59], a[88:91], a[8:11], a[56:59]\n"
"v_mfma_f32_16x16x32_bf16 a[52:55], a[88:91], a[4:7], a[52:55]\n"
"v_mfma_f32_16x16x32_bf16 a[48:51], a[88:91], a[0:3], a[48:51]\n"
"global_load_dwordx4 a[88:91], %[vl], s[20:21] offset:2048\n"
"s_waitcnt vmcnt(35)\n"
"v_mfma_f32_16x16x32_bf16 v[208:211], a[92:95], a[0:3], v[208:211]\n"
"v_mfma_f32_16x16x32_bf16 v[212:215], a[92:95], a[4:7], v[212:215]\n"
"v_mfma_f32_16x16x32_bf16 v[216:219], a[92:95], a[8:11], v[216:219]\n"
"v_mfma_f32_16x16x32_bf16 v[220:223], a[92:95], a[12:15], v[220:223]\n"
"global_load_dwordx4 a[92:95], %[vl], s[20:21] offset:3072\n"
"s_waitcnt lgkmcnt(0)\n"
"s_waitcnt vmcnt(35)\n"
"v_mfma_f32_16x16x32_bf16 a[32:35], a[96:99], a[16:19], a[32:35]\n"
"ds_read_b128 a[0:3], v247 offset:8192\n"
"v_mfma_f32_16x16x32_bf16 a[36:39], a[96:99], a[20:23], a[36:39]\n"
"ds_read_b128 a[4:7], v247 offset:9216\n"
"v_mfma_f32_16x16x32_bf16 a[40:43], a[96:99], a[24:27], a[40:43]\n"
"ds_read_b128 a[8:11], v247 offset:10240\n"
"v_mfma_f32_16x16x32_bf16 a[44:47], a[96:99], a[28:31], a[44:47]\n"
"s_add_u32 s20, s20, 8192\n"
"s_addc_u32 s21, s21, 0\n"
"global_load_dwordx4 a[96:99], %[vl], s[20:21] offset:-4096\n"
"ds_read_b128 a[12:15], v247 offset:11264\n"
"s_waitcnt vmcnt(35)\n"
"v_mfma_f32_16x16x32_bf16 a[60:63], a[100:103], a[28:31], a[60:63]\n"
"v_mfma_f32_16x16x32_bf16 a[56:59], a[100:103], a[24:27], a[56:59]\n"
"v_mfma_f32_16x16x32_bf16 a[52:55], a[100:103], a[20:23], a[52:55]\n"
"v_mfma_f32_16x16x32_bf16 a[48:51], a[100:103], a[16:19], a[48:51]\n"
"global_load_dwordx4 a[100:103], %[vl], s[20:21] offset:-3072\n"
"s_waitcnt vmcnt(35)\n"
"v_mfma_f32_16x16x32_bf16 v[208:211], a[104:107], a[16:19], v[208:211]\n"
"v_mfma_f32_16x16x32_bf16 v[212:215], a[104:107], a[20:23], v[212:215]\n"
"v_mfma_f32_16x16x32_bf16 v[216:219], a[104:107], a[24:27], v[216:219]\n"
"v_mfma_f32_16x16x32_bf16 v[220:223], a[104:107], a[28:31], v[220:223]\n"
"global_load_dwordx4 a[104:107], %[vl], s[20:21] offset:-2048\n"
"s_waitcnt lgkmcnt(0)\n"
"s_waitcnt vmcnt(35)\n"
"v_mfma_f32_16x16x32_bf16 a[32:35], a[108:111], a[0:3], a[32:35]\n"
"ds_read_b128 a[16:19], v247 offset:16384\n"
"v_mfma_f32_16x16x32_bf16 a[36:39], a[108:111], a[4:7], a[36:39]\n"
"ds_read_b128 a[20:23], v247 offset:17408\n"
"v_mfma_f32_16x16x32_bf16 a[40:43], a[108:111], a[8:11], a[40:43]\n"
"ds_read_b128 a[24:27], v247 offset:18432\n"
"v_mfma_f32_16x16x32_bf16 a[44:47], a[108:111], a[12:15], a[44:47]\n"
"global_load_dwordx4 a[108:111], %[vl], s[20:21] offset:-1024\n"
"ds_read_b128 a[28:31], v247 offset:19456\n"
"s_waitcnt vmcnt(35)\n"
"v_mfma_f32_16x16x32_bf16 a[60:63], a[112:115], a[12:15], a[60:63]\n"
"v_mfma_f32_16x16x32_bf16 a[56:59], a[112:115], a[8:11], a[56:59]\n"
"v_mfma_f32_16x16x32_bf16 a[52:55], a[112:115], a[4:7], a[52:55]\n"
"v_mfma_f32_16x16x32_bf16 a[48:51], a[112:115], a[0:3], a[48:51]\n"
"global_load_dwordx4 a[112:115], %[vl], s[20:21] offset:0\n"
"s_waitcnt vmcnt(35)\n"
"v_mfma_f32_16x16x32_bf16 v[208:211], a[116:119], a[0:3], v[208:211]\n"
"v_mfma_f32_16x16x32_bf16 v[212:215], a[116:119], a[4:7], v[212:215]\n"
"v_mfma_f32_16x16x32_bf16 v[216:219], a[116:119], a[8:11], v[216:219]\n"
"v_mfma_f32_16x16x32_bf16 v[220:223], a[116:119], a[12:15], v[220:223]\n"
"global_load_dwordx4 a[116:119], %[vl], s[20:21] offset:1024\n"
"s_waitcnt lgkmcnt(0)\n"
"s_waitcnt vmcnt(35)\n"
"v_mfma_f32_16x16x32_bf16 a[32:35], a[120:123], a[16:19], a[32:35]\n"
"ds_read_b128 a[0:3], v246 offset:45056\n"
"v_mfma_f32_16x16x32_bf16 a[36:39], a[120:123], a[20:23], a[36:39]\n"
"ds_read_b128 a[4:7], v246 offset:46080\n"
"v_mfma_f32_16x16x32_bf16 a[40:43], a[120:123], a[24:27], a[40:43]\n"
"ds_read_b128 a[8:11], v246 offset:47104\n"
"v_mfma_f32_16x16x32_bf16 a[44:47], a[120:123], a[28:31], a[44:47]\n"
"global_load_dwordx4 a[120:123], %[vl], s[20:21] offset:2048\n"
"ds_read_b128 a[12:15], v246 offset:48128\n"
"s_waitcnt vmcnt(35)\n"
"v_mfma_f32_16x16x32_bf16 a[60:63], a[124:127], a[28:31], a[60:63]\n"
"v_mfma_f32_16x16x32_bf16 a[56:59], a[124:127], a[24:27], a[56:59]\n"
"v_mfma_f32_16x16x32_bf16 a[52:55], a[124:127], a[20:23], a[52:55]\n"
"v_mfma_f32_16x16x32_bf16 a[48:51], a[124:127], a[16:19], a[48:51]\n"
"global_load_dwordx4 a[124:127], %[vl], s[20:21] offset:3072\n"
"s_waitcnt vmcnt(35)\n"
"v_mfma_f32_16x16x32_bf16 v[208:211], a[128:131], a[16:19], v[208:211]\n"
"v_mfma_f32_16x16x32_bf16 v[212:215], a[128:131], a[20:23], v[212:215]\n"
"v_mfma_f32_16x16x32_bf16 v[216:219], a[128:131], a[24:27], v[216:219]\n"
"v_mfma_f32_16x16x32_bf16 v[220:223], a[128:131], a[28:31], v[220:223]\n"
"s_add_u32 s20, s20, 8192\n"
"s_addc_u32 s21, s21, 0\n"
"global_load_dwordx4 a[128:131], %[vl], s[20:21] offset:-4096\n"
"s_waitcnt lgkmcnt(0)\n"
"s_waitcnt vmcnt(35)\n"
"v_mfma_f32_16x16x32_bf16 v[144:147], a[132:135], a[0:3], v[144:147]\n"
"ds_read_b128 a[16:19], v246 offset:49152\n"
"v_mfma_f32_16x16x32_bf16 v[148:151], a[132:135], a[4:7], v[148:151]\n"
"ds_read_b128 a[20:23], v246 offset:50176\n"
"v_mfma_f32_16x16x32_bf16 v[152:155], a[132:135], a[8:11], v[152:155]\n"
"ds_read_b128 a[24:27], v246 offset:51200\n"
"v_mfma_f32_16x16x32_bf16 v[156:159], a[132:135], a[12:15], v[156:159]\n"
"global_load_dwordx4 a[132:135], %[vl], s[20:21] offset:-3072\n"
"ds_read_b128 a[28:31], v246 offset:52224\n"
"s_mov_b32 s38, %[n0]\n"
"s_mov_b32 s24, 0\n"
"v_accvgpr_read_b32 v232, a32\n"
"v_accvgpr_read_b32 v233, a33\n"
"s_waitcnt vmcnt(35)\n"
"v_mfma_f32_16x16x32_bf16 v[172:175], a[136:139], a[12:15], v[172:175]\n"
"v_accvgpr_read_b32 v234, a34\n"
"v_accvgpr_read_b32 v235, a35\n"
"v_cvt_pk_bf16_f32 v238, v232, v233\n"
"v_mfma_f32_16x16x32_bf16 v[168:171], a[136:139], a[8:11], v[168:171]\n"
"v_cvt_pk_bf16_f32 v239, v234, v235\n"
"v_accvgpr_read_b32 v232, a48\n"
"v_mfma_f32_16x16x32_bf16 v[164:167], a[136:139], a[4:7], v[164:167]\n"
"v_accvgpr_read_b32 v233, a49\n"
"v_accvgpr_read_b32 v234, a50\n"
"v_accvgpr_read_b32 v235, a51\n"
"v_mfma_f32_16x16x32_bf16 v[160:163], a[136:139], a[0:3], v[160:163]\n"
"global_load_dwordx4 a[136:139], %[vl], s[20:21] offset:-2048\n"
"v_cvt_pk_bf16_f32 v240, v232, v233\n"
"s_waitcnt lgkmcnt(0)\n"
"s_waitcnt vmcnt(35)\n"
"v_mfma_f32_16x16x32_bf16 v[144:147], a[140:143], a[16:19], v[144:147]\n"
"ds_read_b128 a[0:3], v246 offset:53248\n"
"v_mfma_f32_16x16x32_bf16 v[148:151], a[140:143], a[20:23], v[148:151]\n"
"ds_read_b128 a[4:7], v246 offset:54272\n"
"v_mfma_f32_16x16x32_bf16 v[152:155], a[140:143], a[24:27], v[152:155]\n"
"ds_read_b128 a[8:11], v246 offset:55296\n"
"v_mfma_f32_16x16x32_bf16 v[156:159], a[140:143], a[28:31], v[156:159]\n"
"global_load_dwordx4 a[140:143], %[vl], s[20:21] offset:-1024\n"
"ds_read_b128 a[12:15], v246 offset:56320\n"
"v_cvt_pk_bf16_f32 v241, v234, v235\n"
"buffer_store_dwordx4 v[238:241], %[vl], s[36:39], s24 offen offset:0 sc1 ;S1\n"
"s_waitcnt vmcnt(32)\n"
"v_mfma_f32_16x16x32_bf16 v[172:175], a[144:147], a[28:31], v[172:175]\n"
"v_accvgpr_read_b32 v232, a36\n"
"v_accvgpr_read_b32 v233, a37\n"
"v_accvgpr_read_b32 v234, a38\n"
"v_mfma_f32_16x16x32_bf16 v[168:171], a[144:147], a[24:27], v[168:171]\n"
"v_accvgpr_read_b32 v235, a39\n"
"v_cvt_pk_bf16_f32 v242, v232, v233\n"
"v_mfma_f32_16x16x32_bf16 v[164:167], a[144:147], a[20:23], v[164:167]\n"
"v_cvt_pk_bf16_f32 v243, v234, v235\n"
"v_accvgpr_read_b32 v232, a52\n"
"v_accvgpr_read_b32 v233, a53\n"
"v_mfma_f32_16x16x32_bf16 v[160:163], a[144:147], a[16:19], v[160:163]\n"
"global_load_dwordx4 a[144:147], %[vl], s[20:21] offset:0\n"
"v_accvgpr_read_b32 v234, a54\n"
"s_waitcnt lgkmcnt(0)\n"
"s_waitcnt vmcnt(32)\n"
"v_mfma_f32_16x16x32_bf16 v[144:147], a[148:151], a[0:3], v[144:147]\n"
"ds_read_b128 a[16:19], v246 offset:61440\n"
"v_mfma_f32_16x16x32_bf16 v[148:151], a[148:151], a[4:7], v[148:151]\n"
"ds_read_b128 a[20:23], v246 offset:62464\n"
"v_mfma_f32_16x16x32_bf16 v[152:155], a[148:151], a[8:11], v[152:155]\n"
"ds_read_b128 a[24:27], v246 offset:63488\n"
"v_mfma_f32_16x16x32_bf16 v[156:159], a[148:151], a[12:15], v[156:159]\n"
"global_load_dwordx4 a[148:151], %[vl], s[20:21] offset:1024\n"
"ds_read_b128 a[28:31], v246 offset:64512\n"
"v_accvgpr_read_b32 v235, a55\n"
"v_cvt_pk_bf16_f32 v244, v232, v233\n"
"s_waitcnt vmcnt(32)\n"
"v_mfma_f32_16x16x32_bf16 v[172:175], a[152:155], a[12:15], v[172:175]\n"
"v_cvt_pk_bf16_f32 v245, v234, v235\n"
"buffer_store_dwordx4 v[242:245], %[vl], s[36:39], s24 offen offset:1024 sc1 ;S1\n"
"v_accvgpr_read_b32 v232, a40\n"
"v_mfma_f32_16x16x32_bf16 v[168:171], a[152:155], a[8:11], v[168:171]\n"
"v_accvgpr_read_b32 v233, a41\n"
"v_accvgpr_read_b32 v234, a42\n"
"v_mfma_f32_16x16x32_bf16 v[164:167], a[152:155], a[4:7], v[164:167]\n"
"v_accvgpr_read_b32 v235, a43\n"
"v_cvt_pk_bf16_f32 v238, v232, v233\n"
"v_cvt_pk_bf16_f32 v239, v234, v235\n"
"v_mfma_f32_16x16x32_bf16 v[160:163], a[152:155], a[0:3], v[160:163]\n"
"global_load_dwordx4 a[152:155], %[vl], s[20:21] offset:2048\n"
"v_accvgpr_read_b32 v232, a56\n"
"s_waitcnt lgkmcnt(0)\n"
"s_waitcnt vmcnt(33)\n"
"v_mfma_f32_16x16x32_bf16 v[144:147], a[156:159], a[16:19], v[144:147]\n"
"ds_read_b128 a[0:3], v247 offset:4096\n"
"v_mfma_f32_16x16x32_bf16 v[148:151], a[156:159], a[20:23], v[148:151]\n"
"ds_read_b128 a[4:7], v247 offset:5120\n"
"v_mfma_f32_16x16x32_bf16 v[152:155], a[156:159], a[24:27], v[152:155]\n"
"ds_read_b128 a[8:11], v247 offset:6144\n"
"v_mfma_f32_16x16x32_bf16 v[156:159], a[156:159], a[28:31], v[156:159]\n"
"global_load_dwordx4 a[156:159], %[vl], s[20:21] offset:3072\n"
"ds_read_b128 a[12:15], v247 offset:7168\n"
"v_accvgpr_read_b32 v233, a57\n"
"v_accvgpr_read_b32 v234, a58\n"
"s_waitcnt vmcnt(33)\n"
"v_mfma_f32_16x16x32_bf16 v[172:175], a[160:163], a[28:31], v[172:175]\n"
"v_accvgpr_read_b32 v235, a59\n"
"v_cvt_pk_bf16_f32 v240, v232, v233\n"
"v_cvt_pk_bf16_f32 v241, v234, v235\n"
"v_mfma_f32_16x16x32_bf16 v[168:171], a[160:163], a[24:27], v[168:171]\n"
"buffer_store_dwordx4 v[238:241], %[vl], s[36:39], s24 offen offset:2048 sc1 ;S1\n"
"v_accvgpr_read_b32 v232, a44\n"
"v_mfma_f32_16x16x32_bf16 v[164:167], a[160:163], a[20:23], v[164:167]\n"
"v_accvgpr_read_b32 v233, a45\n"
"v_accvgpr_read_b32 v234, a46\n"
"v_accvgpr_read_b32 v235, a47\n"
"v_mfma_f32_16x16x32_bf16 v[160:163], a[160:163], a[16:19], v[160:163]\n"
"s_add_u32 s20, s20, 8192\n"
"s_addc_u32 s21, s21, 0\n"
"global_load_dwordx4 a[160:163], %[vl], s[20:21] offset:-4096\n"
"v_cvt_pk_bf16_f32 v242, v232, v233\n"
"s_waitcnt lgkmcnt(0)\n"
"s_waitcnt vmcnt(34)\n"
"v_mfma_f32_16x16x32_bf16 v[144:147], a[164:167], a[0:3], v[144:147]\n"
"ds_read_b128 a[16:19], v247 offset:12288\n"
"v_mfma_f32_16x16x32_bf16 v[148:151], a[164:167], a[4:7], v[148:151]\n"
"ds_read_b128 a[20:23], v247 offset:13312\n"
"v_mfma_f32_16x16x32_bf16 v[152:155], a[164:167], a[8:11], v[152:155]\n"
"ds_read_b128 a[24:27], v247 offset:14336\n"
"v_mfma_f32_16x16x32_bf16 v[156:159], a[164:167], a[12:15], v[156:159]\n"
"global_load_dwordx4 a[164:167], %[vl], s[20:21] offset:-3072\n"
"ds_read_b128 a[28:31], v247 offset:15360\n"
"v_cvt_pk_bf16_f32 v243, v234, v235\n"
"v_accvgpr_read_b32 v232, a60\n"
"s_waitcnt vmcnt(34)\n"
"v_mfma_f32_16x16x32_bf16 v[172:175], a[168:171], a[12:15], v[172:175]\n"
"v_accvgpr_read_b32 v233, a61\n"
"v_accvgpr_read_b32 v234, a62\n"
"v_accvgpr_read_b32 v235, a63\n"
"v_mfma_f32_16x16x32_bf16 v[168:171], a[168:171], a[8:11], v[168:171]\n"
"v_cvt_pk_bf16_f32 v244, v232, v233\n"
"v_cvt_pk_bf16_f32 v245, v234, v235\n"
"v_mfma_f32_16x16x32_bf16 v[164:167], a[168:171], a[4:7], v[164:167]\n"
"buffer_store_dwordx4 v[242:245], %[vl], s[36:39], s24 offen offset:3072 sc1 ;S1\n"
"s_mov_b32 s38, 18432\n"
"s_mov_b32 s24, 16384\n"
"v_cvt_pk_bf16_f32 v238, v208, v209\n"
"v_cvt_pk_bf16_f32 v239, v210, v211\n"
"v_mfma_f32_16x16x32_bf16 v[160:163], a[168:171], a[0:3], v[160:163]\n"
"global_load_dwordx4 a[168:171], %[vl], s[20:21] offset:-2048\n"
"v_cvt_pk_bf16_f32 v240, v212, v213\n"
"s_waitcnt lgkmcnt(0)\n"
"s_waitcnt vmcnt(35)\n"
"v_mfma_f32_16x16x32_bf16 v[144:147], a[172:175], a[16:19], v[144:147]\n"
"ds_read_b128 a[0:3], v246 offset:57344\n"
"v_mfma_f32_16x16x32_bf16 v[148:151], a[172:175], a[20:23], v[148:151]\n"
"ds_read_b128 a[4:7], v246 offset:58368\n"
"v_mfma_f32_16x16x32_bf16 v[152:155], a[172:175], a[24:27], v[152:155]\n"
"ds_read_b128 a[8:11], v246 offset:59392\n"
"v_mfma_f32_16x16x32_bf16 v[156:159], a[172:175], a[28:31], v[156:159]\n"
"global_load_dwordx4 a[172:175], %[vl], s[20:21] offset:-1024\n"
"ds_read_b128 a[12:15], v246 offset:60416\n"
"v_cvt_pk_bf16_f32 v241, v214, v215\n"
"buffer_store_dwordx4 v[238:241], %[vl], s[36:39], s24 offen offset:0 sc1 ;S1\n"
"s_waitcnt vmcnt(36)\n"
"v_mfma_f32_16x16x32_bf16 v[172:175], a[176:179], a[28:31], v[172:175]\n"
"v_cvt_pk_bf16_f32 v242, v216, v217\n"
"v_cvt_pk_bf16_f32 v243, v218, v219\n"
"v_cvt_pk_bf16_f32 v244, v220, v221\n"
"v_mfma_f32_16x16x32_bf16 v[168:171], a[176:179], a[24:27], v[168:171]\n"
"v_cvt_pk_bf16_f32 v245, v222, v223\n"
"buffer_store_dwordx4 v[242:245], %[vl], s[36:39], s24 offen offset:1024 sc1 ;S1\n"
"v_mfma_f32_16x16x32_bf16 v[164:167], a[176:179], a[20:23], v[164:167]\n"
"v_mfma_f32_16x16x32_bf16 v[160:163], a[176:179], a[16:19], v[160:163]\n"
"global_load_dwordx4 a[176:179], %[vl], s[20:21] offset:0\n"
"s_waitcnt lgkmcnt(0)\n"
"s_waitcnt vmcnt(37)\n"
"v_mfma_f32_16x16x32_bf16 v[144:147], a[180:183], a[0:3], v[144:147]\n"
"ds_read_b128 a[16:19], v247 offset:0\n"
"v_mfma_f32_16x16x32_bf16 v[148:151], a[180:183], a[4:7], v[148:151]\n"
"ds_read_b128 a[20:23], v247 offset:1024\n"
"v_mfma_f32_16x16x32_bf16 v[152:155], a[180:183], a[8:11], v[152:155]\n"
"ds_read_b128 a[24:27], v247 offset:2048\n"
"v_mfma_f32_16x16x32_bf16 v[156:159], a[180:183], a[12:15], v[156:159]\n"
"global_load_dwordx4 a[180:183], %[vl], s[20:21] offset:1024\n"
"ds_read_b128 a[28:31], v247 offset:3072\n"
"s_waitcnt vmcnt(37)\n"
"v_mfma_f32_16x16x32_bf16 v[172:175], a[184:187], a[12:15], v[172:175]\n"
"v_mfma_f32_16x16x32_bf16 v[168:171], a[184:187], a[8:11], v[168:171]\n"
"v_mfma_f32_16x16x32_bf16 v[164:167], a[184:187], a[4:7], v[164:167]\n"
"v_mfma_f32_16x16x32_bf16 v[160:163], a[184:187], a[0:3], v[160:163]\n"
"global_load_dwordx4 a[184:187], %[vl], s[20:21] offset:2048\n"
"s_waitcnt lgkmcnt(0)\n"
"s_waitcnt vmcnt(37)\n"
"v_mfma_f32_16x16x32_bf16 v[144:147], a[188:191], a[16:19], v[144:147]\n"
"ds_read_b128 a[0:3], v247 offset:8192\n"
"v_mfma_f32_16x16x32_bf16 v[148:151], a[188:191], a[20:23], v[148:151]\n"
"ds_read_b128 a[4:7], v247 offset:9216\n"
"v_mfma_f32_16x16x32_bf16 v[152:155], a[188:191], a[24:27], v[152:155]\n"
"ds_read_b128 a[8:11], v247 offset:10240\n"
"v_mfma_f32_16x16x32_bf16 v[156:159], a[188:191], a[28:31], v[156:159]\n"
"global_load_dwordx4 a[188:191], %[vl], s[20:21] offset:3072\n"
"ds_read_b128 a[12:15], v247 offset:11264\n"
"s_waitcnt vmcnt(37)\n"
"v_mfma_f32_16x16x32_bf16 v[172:175], a[64:67], a[28:31], v[172:175]\n"
"v_mfma_f32_16x16x32_bf16 v[168:171], a[64:67], a[24:27], v[168:171]\n"
"v_mfma_f32_16x16x32_bf16 v[164:167], a[64:67], a[20:23], v[164:167]\n"
"v_mfma_f32_16x16x32_bf16 v[160:163], a[64:67], a[16:19], v[160:163]\n"
"s_add_u32 s20, s20, 8192\n"
"s_addc_u32 s21, s21, 0\n"
"global_load_dwordx4 a[64:67], %[vl], s[20:21] offset:-4096\n"
"s_waitcnt lgkmcnt(0)\n"
"s_waitcnt vmcnt(37)\n"
"v_mfma_f32_16x16x32_bf16 v[144:147], a[68:71], a[0:3], v[144:147]\n"
"ds_read_b128 a[16:19], v247 offset:16384\n"
"v_mfma_f32_16x16x32_bf16 v[148:151], a[68:71], a[4:7], v[148:151]\n"
"ds_read_b128 a[20:23], v247 offset:17408\n"
"v_mfma_f32_16x16x32_bf16 v[152:155], a[68:71], a[8:11], v[152:155]\n"
"ds_read_b128 a[24:27], v247 offset:18432\n"
"v_mfma_f32_16x16x32_bf16 v[156:159], a[68:71], a[12:15], v[156:159]\n"
"global_load_dwordx4 a[68:71], %[vl], s[20:21] offset:-3072\n"
"ds_read_b128 a[28:31], v247 offset:19456\n"
"s_waitcnt vmcnt(37)\n"
"v_mfma_f32_16x16x32_bf16 v[172:175], a[72:75], a[12:15], v[172:175]\n"
"v_mfma_f32_16x16x32_bf16 v[168:171], a[72:75], a[8:11], v[168:171]\n"
"v_mfma_f32_16x16x32_bf16 v[164:167], a[72:75], a[4:7], v[164:167]\n"
"v_mfma_f32_16x16x32_bf16 v[160:163], a[72:75], a[0:3], v[160:163]\n"
"global_load_dwordx4 a[72:75], %[vl], s[20:21] offset:-2048\n"
"s_waitcnt lgkmcnt(0)\n"
"s_waitcnt vmcnt(37)\n"
"v_mfma_f32_16x16x32_bf16 v[144:147], a[76:79], a[16:19], v[144:147]\n"
"ds_read_b128 a[0:3], v246 offset:45056\n"
"v_mfma_f32_16x16x32_bf16 v[148:151], a[76:79], a[20:23], v[148:151]\n"
"ds_read_b128 a[4:7], v246 offset:46080\n"
"v_mfma_f32_16x16x32_bf16 v[152:155], a[76:79], a[24:27], v[152:155]\n"
"ds_read_b128 a[8:11], v246 offset:47104\n"
"v_mfma_f32_16x16x32_bf16 v[156:159], a[76:79], a[28:31], v[156:159]\n"
"global_load_dwordx4 a[76:79], %[vl], s[20:21] offset:-1024\n"
"ds_read_b128 a[12:15], v246 offset:48128\n"
"s_waitcnt vmcnt(37)\n"
"v_mfma_f32_16x16x32_bf16 v[172:175], a[80:83], a[28:31], v[172:175]\n"
"v_mfma_f32_16x16x32_bf16 v[168:171], a[80:83], a[24:27], v[168:171]\n"
"v_mfma_f32_16x16x32_bf16 v[164:167], a[80:83], a[20:23], v[164:167]\n"
"v_mfma_f32_16x16x32_bf16 v[160:163], a[80:83], a[16:19], v[160:163]\n"
"global_load_dwordx4 a[80:83], %[vl], s[20:21] offset:0\n"
"s_waitcnt lgkmcnt(0)\n"
"s_waitcnt vmcnt(37)\n"
"v_mfma_f32_16x16x32_bf16 v[0:3], a[84:87], a[0:3], v[0:3]\n"
"ds_read_b128 a[16:19], v246 offset:49152\n"
"v_mfma_f32_16x16x32_bf16 v[4:7], a[84:87], a[4:7], v[4:7]\n"
"ds_read_b128 a[20:23], v246 offset:50176\n"
"v_mfma_f32_16x16x32_bf16 v[8:11], a[84:87], a[8:11], v[8:11]\n"
"ds_read_b128 a[24:27], v246 offset:51200\n"
"v_mfma_f32_16x16x32_bf16 v[12:15], a[84:87], a[12:15], v[12:15]\n"
"global_load_dwordx4 a[84:87], %[vl], s[20:21] offset:1024\n"
"ds_read_b128 a[28:31], v246 offset:52224\n"
"s_mov_b32 s38, %[n1]\n"
"s_mov_b32 s24, 8192\n"
"v_cvt_pk_bf16_f32 v238, v144, v145\n"
"v_cvt_pk_bf16_f32 v239, v146, v147\n"
"s_waitcnt vmcnt(37)\n"
"v_mfma_f32_16x16x32_bf16 v[28:31], a[88:91], a[12:15], v[28:31]\n"
"v_cvt_pk_bf16_f32 v240, v160, v161\n"
"v_cvt_pk_bf16_f32 v241, v162, v163\n"
"buffer_store_dwordx4 v[238:241], %[vl], s[36:39], s24 offen offset:0 sc1 ;S1\n"
"v_mfma_f32_16x16x32_bf16 v[24:27], a[88:91], a[8:11], v[24:27]\n"
"v_cvt_pk_bf16_f32 v242, v148, v149\n"
"v_cvt_pk_bf16_f32 v243, v150, v151\n"
"v_mfma_f32_16x16x32_bf16 v[20:23], a[88:91], a[4:7], v[20:23]\n"
"v_cvt_pk_bf16_f32 v244, v164, v165\n"
"v_cvt_pk_bf16_f32 v245, v166, v167\n"
"buffer_store_dwordx4 v[242:245], %[vl], s[36:39], s24 offen offset:1024 sc1 ;S1\n"
"v_mfma_f32_16x16x32_bf16 v[16:19], a[88:91], a[0:3], v[16:19]\n"
"global_load_dwordx4 a[88:91], %[vl], s[20:21] offset:2048\n"
"v_cvt_pk_bf16_f32 v238, v152, v153\n"
"s_waitcnt lgkmcnt(0)\n"
"s_waitcnt vmcnt(39)\n"
"v_mfma_f32_16x16x32_bf16 v[0:3], a[92:95], a[16:19], v[0:3]\n"
"ds_read_b128 a[0:3], v246 offset:53248\n"
"v_mfma_f32_16x16x32_bf16 v[4:7], a[92:95], a[20:23], v[4:7]\n"
"ds_read_b128 a[4:7], v246 offset:54272\n"
"v_mfma_f32_16x16x32_bf16 v[8:11], a[92:95], a[24:27], v[8:11]\n"
"ds_read_b128 a[8:11], v246 offset:55296\n"
"v_mfma_f32_16x16x32_bf16 v[12:15], a[92:95], a[28:31], v[12:15]\n"
"global_load_dwordx4 a[92:95], %[vl], s[20:21] offset:3072\n"
"ds_read_b128 a[12:15], v246 offset:56320\n"
"v_cvt_pk_bf16_f32 v239, v154, v155\n"
"v_cvt_pk_bf16_f32 v240, v168, v169\n"
"s_waitcnt vmcnt(39)\n"
"v_mfma_f32_16x16x32_bf16 v[28:31], a[96:99], a[28:31], v[28:31]\n"
"v_cvt_pk_bf16_f32 v241, v170, v171\n"
"buffer_store_dwordx4 v[238:241], %[vl], s[36:39], s24 offen offset:2048 sc1 ;S1\n"
"v_cvt_pk_bf16_f32 v242, v156, v157\n"
"v_mfma_f32_16x16x32_bf16 v[24:27], a[96:99], a[24:27], v[24:27]\n"
"v_cvt_pk_bf16_f32 v243, v158, v159\n"
"v_cvt_pk_bf16_f32 v244, v172, v173\n"
"v_mfma_f32_16x16x32_bf16 v[20:23], a[96:99], a[20:23], v[20:23]\n"
"v_cvt_pk_bf16_f32 v245, v174, v175\n"
"buffer_store_dwordx4 v[242:245], %[vl], s[36:39], s24 offen offset:3072 sc1 ;S1\n"
"v_mfma_f32_16x16x32_bf16 v[16:19], a[96:99], a[16:19], v[16:19]\n"
"s_add_u32 s20, s20, 8192\n"
"s_addc_u32 s21, s21, 0\n"
"global_load_dwordx4 a[96:99], %[vl], s[20:21] offset:-4096\n"
"s_waitcnt lgkmcnt(0)\n"
"s_waitcnt vmcnt(41)\n"
"v_mfma_f32_16x16x32_bf16 v[0:3], a[100:103], a[0:3], v[0:3]\n"
"ds_read_b128 a[16:19], v246 offset:61440\n"
"v_mfma_f32_16x16x32_bf16 v[4:7], a[100:103], a[4:7], v[4:7]\n"
"ds_read_b128 a[20:23], v246 offset:62464\n"
"v_mfma_f32_16x16x32_bf16 v[8:11], a[100:103], a[8:11], v[8:11]\n"
"ds_read_b128 a[24:27], v246 offset:63488\n"
"v_mfma_f32_16x16x32_bf16 v[12:15], a[100:103], a[12:15], v[12:15]\n"
"global_load_dwordx4 a[100:103], %[vl], s[20:21] offset:-3072\n"
"ds_read_b128 a[28:31], v246 offset:64512\n"
"s_waitcnt vmcnt(41)\n"
"v_mfma_f32_16x16x32_bf16 v[28:31], a[104:107], a[12:15], v[28:31]\n"
"v_mfma_f32_16x16x32_bf16 v[24:27], a[104:107], a[8:11], v[24:27]\n"
"v_mfma_f32_16x16x32_bf16 v[20:23], a[104:107], a[4:7], v[20:23]\n"
"v_mfma_f32_16x16x32_bf16 v[16:19], a[104:107], a[0:3], v[16:19]\n"
"global_load_dwordx4 a[104:107], %[vl], s[20:21] offset:-2048\n"
"s_waitcnt lgkmcnt(0)\n"
"s_waitcnt vmcnt(41)\n"
"v_mfma_f32_16x16x32_bf16 v[0:3], a[108:111], a[16:19], v[0:3]\n"
"ds_read_b128 a[0:3], v247 offset:4096\n"
"v_mfma_f32_16x16x32_bf16 v[4:7], a[108:111], a[20:23], v[4:7]\n"
"ds_read_b128 a[4:7], v247 offset:5120\n"
"v_mfma_f32_16x16x32_bf16 v[8:11], a[108:111], a[24:27], v[8:11]\n"
"ds_read_b128 a[8:11], v247 offset:6144\n"
"v_mfma_f32_16x16x32_bf16 v[12:15], a[108:111], a[28:31], v[12:15]\n"
"global_load_dwordx4 a[108:111], %[vl], s[20:21] offset:-1024\n"
"ds_read_b128 a[12:15], v247 offset:7168\n"
"s_waitcnt vmcnt(41)\n"
"v_mfma_f32_16x16x32_bf16 v[28:31], a[112:115], a[28:31], v[28:31]\n"
"v_mfma_f32_16x16x32_bf16 v[24:27], a[112:115], a[24:27], v[24:27]\n"
"v_mfma_f32_16x16x32_bf16 v[20:23], a[112:115], a[20:23], v[20:23]\n"
"v_mfma_f32_16x16x32_bf16 v[16:19], a[112:115], a[16:19], v[16:19]\n"
"global_load_dwordx4 a[112:115], %[vl], s[20:21] offset:0\n"
"s_waitcnt lgkmcnt(0)\n"
"s_waitcnt vmcnt(41)\n"
"v_mfma_f32_16x16x32_bf16 v[0:3], a[116:119], a[0:3], v[0:3]\n"
"ds_read_b128 a[16:19], v247 offset:12288\n"
"v_mfma_f32_16x16x32_bf16 v[4:7], a[116:119], a[4:7], v[4:7]\n"
"ds_read_b128 a[20:23], v247 offset:13312\n"
"v_mfma_f32_16x16x32_bf16 v[8:11], a[116:119], a[8:11], v[8:11]\n"
"ds_read_b128 a[24:27], v247 offset:14336\n"
"v_mfma_f32_16x16x32_bf16 v[12:15], a[116:119], a[12:15], v[12:15]\n"
"ds_read_b128 a[28:31], v247 offset:15360\n"
"s_waitcnt vmcnt(40)\n"
"v_mfma_f32_16x16x32_bf16 v[28:31], a[120:123], a[12:15], v[28:31]\n"
"v_mfma_f32_16x16x32_bf16 v[24:27], a[120:123], a[8:11], v[24:27]\n"
"v_mfma_f32_16x16x32_bf16 v[20:23], a[120:123], a[4:7], v[20:23]\n"
"v_mfma_f32_16x16x32_bf16 v[16:19], a[120:123], a[0:3], v[16:19]\n"
"s_waitcnt lgkmcnt(0)\n"
"s_waitcnt vmcnt(39)\n"
"v_mfma_f32_16x16x32_bf16 v[0:3], a[124:127], a[16:19], v[0:3]\n"
"ds_read_b128 a[0:3], v246 offset:57344\n"
"v_mfma_f32_16x16x32_bf16 v[4:7], a[124:127], a[20:23], v[4:7]\n"
"ds_read_b128 a[4:7], v246 offset:58368\n"
"v_mfma_f32_16x16x32_bf16 v[8:11], a[124:127], a[24:27], v[8:11]\n"
"ds_read_b128 a[8:11], v246 offset:59392\n"
"v_mfma_f32_16x16x32_bf16 v[12:15], a[124:127], a[28:31], v[12:15]\n"
"ds_read_b128 a[12:15], v246 offset:60416\n"
"s_waitcnt vmcnt(38)\n"
"v_mfma_f32_16x16x32_bf16 v[28:31], a[128:131], a[28:31], v[28:31]\n"
"v_mfma_f32_16x16x32_bf16 v[24:27], a[128:131], a[24:27], v[24:27]\n"
"v_mfma_f32_16x16x32_bf16 v[20:23], a[128:131], a[20:23], v[20:23]\n"
"v_mfma_f32_16x16x32_bf16 v[16:19], a[128:131], a[16:19], v[16:19]\n"
"s_waitcnt lgkmcnt(0)\n"
"s_waitcnt vmcnt(37)\n"
"v_mfma_f32_16x16x32_bf16 v[0:3], a[132:135], a[0:3], v[0:3]\n"
"ds_read_b128 a[16:19], v247 offset:0\n"
"v_mfma_f32_16x16x32_bf16 v[4:7], a[132:135], a[4:7], v[4:7]\n"
"ds_read_b128 a[20:23], v247 offset:1024\n"
"v_mfma_f32_16x16x32_bf16 v[8:11], a[132:135], a[8:11], v[8:11]\n"
"ds_read_b128 a[24:27], v247 offset:2048\n"
"v_mfma_f32_16x16x32_bf16 v[12:15], a[132:135], a[12:15], v[12:15]\n"
"ds_read_b128 a[28:31], v247 offset:3072\n"
"s_waitcnt vmcnt(36)\n"
"v_mfma_f32_16x16x32_bf16 v[28:31], a[136:139], a[12:15], v[28:31]\n"
"v_mfma_f32_16x16x32_bf16 v[24:27], a[136:139], a[8:11], v[24:27]\n"
"v_mfma_f32_16x16x32_bf16 v[20:23], a[136:139], a[4:7], v[20:23]\n"
"v_mfma_f32_16x16x32_bf16 v[16:19], a[136:139], a[0:3], v[16:19]\n"
"s_waitcnt lgkmcnt(0)\n"
"s_waitcnt vmcnt(35)\n"
"v_mfma_f32_16x16x32_bf16 v[0:3], a[140:143], a[16:19], v[0:3]\n"
"ds_read_b128 a[0:3], v247 offset:8192\n"
"v_mfma_f32_16x16x32_bf16 v[4:7], a[140:143], a[20:23], v[4:7]\n"
"ds_read_b128 a[4:7], v247 offset:9216\n"
"v_mfma_f32_16x16x32_bf16 v[8:11], a[140:143], a[24:27], v[8:11]\n"
"ds_read_b128 a[8:11], v247 offset:10240\n"
"v_mfma_f32_16x16x32_bf16 v[12:15], a[140:143], a[28:31], v[12:15]\n"
"ds_read_b128 a[12:15], v247 offset:11264\n"
"s_waitcnt vmcnt(33)\n"
"v_mfma_f32_16x16x32_bf16 v[28:31], a[144:147], a[28:31], v[28:31]\n"
"v_mfma_f32_16x16x32_bf16 v[24:27], a[144:147], a[24:27], v[24:27]\n"
"v_mfma_f32_16x16x32_bf16 v[20:23], a[144:147], a[20:23], v[20:23]\n"
"v_mfma_f32_16x16x32_bf16 v[16:19], a[144:147], a[16:19], v[16:19]\n"
"s_waitcnt lgkmcnt(0)\n"
"s_waitcnt vmcnt(32)\n"
"v_mfma_f32_16x16x32_bf16 v[0:3], a[148:151], a[0:3], v[0:3]\n"
"ds_read_b128 a[16:19], v247 offset:16384\n"
"v_mfma_f32_16x16x32_bf16 v[4:7], a[148:151], a[4:7], v[4:7]\n"
"ds_read_b128 a[20:23], v247 offset:17408\n"
"v_mfma_f32_16x16x32_bf16 v[8:11], a[148:151], a[8:11], v[8:11]\n"
"ds_read_b128 a[24:27], v247 offset:18432\n"
"v_mfma_f32_16x16x32_bf16 v[12:15], a[148:151], a[12:15], v[12:15]\n"
"ds_read_b128 a[28:31], v247 offset:19456\n"
"s_waitcnt vmcnt(30)\n"
"v_mfma_f32_16x16x32_bf16 v[28:31], a[152:155], a[12:15], v[28:31]\n"
"v_mfma_f32_16x16x32_bf16 v[24:27], a[152:155], a[8:11], v[24:27]\n"
"v_mfma_f32_16x16x32_bf16 v[20:23], a[152:155], a[4:7], v[20:23]\n"
"v_mfma_f32_16x16x32_bf16 v[16:19], a[152:155], a[0:3], v[16:19]\n"
"s_waitcnt lgkmcnt(0)\n"
"s_waitcnt vmcnt(29)\n"
"v_mfma_f32_16x16x32_bf16 v[0:3], a[156:159], a[16:19], v[0:3]\n"
"ds_read_b128 a[0:3], v246 offset:45056\n"
"v_mfma_f32_16x16x32_bf16 v[4:7], a[156:159], a[20:23], v[4:7]\n"
"ds_read_b128 a[4:7], v246 offset:46080\n"
"v_mfma_f32_16x16x32_bf16 v[8:11], a[156:159], a[24:27], v[8:11]\n"
"ds_read_b128 a[8:11], v246 offset:47104\n"
"v_mfma_f32_16x16x32_bf16 v[12:15], a[156:159], a[28:31], v[12:15]\n"
"ds_read_b128 a[12:15], v246 offset:48128\n"
"s_waitcnt vmcnt(27)\n"
"v_mfma_f32_16x16x32_bf16 v[28:31], a[160:163], a[28:31], v[28:31]\n"
"v_mfma_f32_16x16x32_bf16 v[24:27], a[160:163], a[24:27], v[24:27]\n"
"v_mfma_f32_16x16x32_bf16 v[20:23], a[160:163], a[20:23], v[20:23]\n"
"v_mfma_f32_16x16x32_bf16 v[16:19], a[160:163], a[16:19], v[16:19]\n"
"s_waitcnt lgkmcnt(0)\n"
"s_waitcnt vmcnt(26)\n"
"v_mfma_f32_16x16x32_bf16 v[176:179], a[164:167], a[0:3], v[176:179]\n"
"ds_read_b128 a[16:19], v246 offset:49152\n"
"v_mfma_f32_16x16x32_bf16 v[180:183], a[164:167], a[4:7], v[180:183]\n"
"ds_read_b128 a[20:23], v246 offset:50176\n"
"v_mfma_f32_16x16x32_bf16 v[184:187], a[164:167], a[8:11], v[184:187]\n"
"ds_read_b128 a[24:27], v246 offset:51200\n"
"v_mfma_f32_16x16x32_bf16 v[188:191], a[164:167], a[12:15], v[188:191]\n"
"ds_read_b128 a[28:31], v246 offset:52224\n"
"s_mov_b32 s38, %[n0]\n"
"s_mov_b32 s24, 4096\n"
"v_cvt_pk_bf16_f32 v238, v0, v1\n"
"v_cvt_pk_bf16_f32 v239, v2, v3\n"
"v_cvt_pk_bf16_f32 v240, v16, v17\n"
"s_waitcnt vmcnt(24)\n"
"v_mfma_f32_16x16x32_bf16 v[204:207], a[168:171], a[12:15], v[204:207]\n"
"v_cvt_pk_bf16_f32 v241, v18, v19\n"
"buffer_store_dwordx4 v[238:241], %[vl], s[36:39], s24 offen offset:0 sc1 ;S2\n"
"v_cvt_pk_bf16_f32 v242, v4, v5\n"
"v_mfma_f32_16x16x32_bf16 v[200:203], a[168:171], a[8:11], v[200:203]\n"
"v_cvt_pk_bf16_f32 v243, v6, v7\n"
"v_cvt_pk_bf16_f32 v244, v20, v21\n"
"v_mfma_f32_16x16x32_bf16 v[196:199], a[168:171], a[4:7], v[196:199]\n"
"v_cvt_pk_bf16_f32 v245, v22, v23\n"
"buffer_store_dwordx4 v[242:245], %[vl], s[36:39], s24 offen offset:1024 sc1 ;S2\n"
"v_cvt_pk_bf16_f32 v238, v8, v9\n"
"v_mfma_f32_16x16x32_bf16 v[192:195], a[168:171], a[0:3], v[192:195]\n"
"v_cvt_pk_bf16_f32 v239, v10, v11\n"
"v_cvt_pk_bf16_f32 v240, v24, v25\n"
"s_waitcnt lgkmcnt(0)\n"
"s_waitcnt vmcnt(25)\n"
"v_mfma_f32_16x16x32_bf16 v[176:179], a[172:175], a[16:19], v[176:179]\n"
"ds_read_b128 a[0:3], v246 offset:53248\n"
"v_mfma_f32_16x16x32_bf16 v[180:183], a[172:175], a[20:23], v[180:183]\n"
"ds_read_b128 a[4:7], v246 offset:54272\n"
"v_mfma_f32_16x16x32_bf16 v[184:187], a[172:175], a[24:27], v[184:187]\n"
"ds_read_b128 a[8:11], v246 offset:55296\n"
"v_mfma_f32_16x16x32_bf16 v[188:191], a[172:175], a[28:31], v[188:191]\n"
"ds_read_b128 a[12:15], v246 offset:56320\n"
"v_cvt_pk_bf16_f32 v241, v26, v27\n"
"buffer_store_dwordx4 v[238:241], %[vl], s[36:39], s24 offen offset:2048 sc1 ;S2\n"
"v_cvt_pk_bf16_f32 v242, v12, v13\n"
"s_waitcnt vmcnt(23)\n"
"v_mfma_f32_16x16x32_bf16 v[204:207], a[176:179], a[28:31], v[204:207]\n"
"v_cvt_pk_bf16_f32 v243, v14, v15\n"
"v_cvt_pk_bf16_f32 v244, v28, v29\n"
"v_cvt_pk_bf16_f32 v245, v30, v31\n"
"v_mfma_f32_16x16x32_bf16 v[200:203], a[176:179], a[24:27], v[200:203]\n"
"buffer_store_dwordx4 v[242:245], %[vl], s[36:39], s24 offen offset:3072 sc1 ;S2\n"
"v_mfma_f32_16x16x32_bf16 v[196:199], a[176:179], a[20:23], v[196:199]\n"
"v_mfma_f32_16x16x32_bf16 v[192:195], a[176:179], a[16:19], v[192:195]\n"
"s_waitcnt lgkmcnt(0)\n"
"s_waitcnt vmcnt(23)\n"
"v_mfma_f32_16x16x32_bf16 v[176:179], a[180:183], a[0:3], v[176:179]\n"
"ds_read_b128 a[16:19], v246 offset:61440\n"
"v_mfma_f32_16x16x32_bf16 v[180:183], a[180:183], a[4:7], v[180:183]\n"
"ds_read_b128 a[20:23], v246 offset:62464\n"
"v_mfma_f32_16x16x32_bf16 v[184:187], a[180:183], a[8:11], v[184:187]\n"
"ds_read_b128 a[24:27], v246 offset:63488\n"
"v_mfma_f32_16x16x32_bf16 v[188:191], a[180:183], a[12:15], v[188:191]\n"
"ds_read_b128 a[28:31], v246 offset:64512\n"
"s_waitcnt vmcnt(22)\n"
"v_mfma_f32_16x16x32_bf16 v[204:207], a[184:187], a[12:15], v[204:207]\n"
"v_mfma_f32_16x16x32_bf16 v[200:203], a[184:187], a[8:11], v[200:203]\n"
"v_mfma_f32_16x16x32_bf16 v[196:199], a[184:187], a[4:7], v[196:199]\n"
"v_mfma_f32_16x16x32_bf16 v[192:195], a[184:187], a[0:3], v[192:195]\n"
"s_waitcnt lgkmcnt(0)\n"
"s_waitcnt vmcnt(21)\n"
"v_mfma_f32_16x16x32_bf16 v[176:179], a[188:191], a[16:19], v[176:179]\n"
"ds_read_b128 a[0:3], v247 offset:4096\n"
"v_mfma_f32_16x16x32_bf16 v[180:183], a[188:191], a[20:23], v[180:183]\n"
"ds_read_b128 a[4:7], v247 offset:5120\n"
"v_mfma_f32_16x16x32_bf16 v[184:187], a[188:191], a[24:27], v[184:187]\n"
"ds_read_b128 a[8:11], v247 offset:6144\n"
"v_mfma_f32_16x16x32_bf16 v[188:191], a[188:191], a[28:31], v[188:191]\n"
"ds_read_b128 a[12:15], v247 offset:7168\n"
"s_waitcnt vmcnt(20)\n"
"v_mfma_f32_16x16x32_bf16 v[204:207], a[64:67], a[28:31], v[204:207]\n"
"v_mfma_f32_16x16x32_bf16 v[200:203], a[64:67], a[24:27], v[200:203]\n"
"v_mfma_f32_16x16x32_bf16 v[196:199], a[64:67], a[20:23], v[196:199]\n"
"v_mfma_f32_16x16x32_bf16 v[192:195], a[64:67], a[16:19], v[192:195]\n"
"s_waitcnt lgkmcnt(0)\n"
"s_waitcnt vmcnt(19)\n"
"v_mfma_f32_16x16x32_bf16 v[176:179], a[68:71], a[0:3], v[176:179]\n"
"ds_read_b128 a[16:19], v247 offset:12288\n"
"v_mfma_f32_16x16x32_bf16 v[180:183], a[68:71], a[4:7], v[180:183]\n"
"ds_read_b128 a[20:23], v247 offset:13312\n"
"v_mfma_f32_16x16x32_bf16 v[184:187], a[68:71], a[8:11], v[184:187]\n"
"ds_read_b128 a[24:27], v247 offset:14336\n"
"v_mfma_f32_16x16x32_bf16 v[188:191], a[68:71], a[12:15], v[188:191]\n"
"ds_read_b128 a[28:31], v247 offset:15360\n"
"s_waitcnt vmcnt(18)\n"
"v_mfma_f32_16x16x32_bf16 v[204:207], a[72:75], a[12:15], v[204:207]\n"
"v_mfma_f32_16x16x32_bf16 v[200:203], a[72:75], a[8:11], v[200:203]\n"
"v_mfma_f32_16x16x32_bf16 v[196:199], a[72:75], a[4:7], v[196:199]\n"
"v_mfma_f32_16x16x32_bf16 v[192:195], a[72:75], a[0:3], v[192:195]\n"
"s_waitcnt vmcnt(9)\n"
"s_mov_b64 exec, 255\n"
"global_store_dword v230, v231, %[f1] sc1\n"
"s_mov_b64 exec, -1\n"
"s_waitcnt lgkmcnt(0)\n"
"v_mfma_f32_16x16x32_bf16 v[176:179], a[76:79], a[16:19], v[176:179]\n"
"ds_read_b128 a[0:3], v246 offset:57344\n"
"v_mfma_f32_16x16x32_bf16 v[180:183], a[76:79], a[20:23], v[180:183]\n"
"ds_read_b128 a[4:7], v246 offset:58368\n"
"v_mfma_f32_16x16x32_bf16 v[184:187], a[76:79], a[24:27], v[184:187]\n"
"ds_read_b128 a[8:11], v246 offset:59392\n"
"v_mfma_f32_16x16x32_bf16 v[188:191], a[76:79], a[28:31], v[188:191]\n"
"ds_read_b128 a[12:15], v246 offset:60416\n"
"v_mfma_f32_16x16x32_bf16 v[204:207], a[80:83], a[28:31], v[204:207]\n"
"v_mfma_f32_16x16x32_bf16 v[200:203], a[80:83], a[24:27], v[200:203]\n"
"v_mfma_f32_16x16x32_bf16 v[196:199], a[80:83], a[20:23], v[196:199]\n"
"v_mfma_f32_16x16x32_bf16 v[192:195], a[80:83], a[16:19], v[192:195]\n"
"s_waitcnt lgkmcnt(0)\n"
"v_mfma_f32_16x16x32_bf16 v[176:179], a[84:87], a[0:3], v[176:179]\n"
"ds_read_b128 a[16:19], v247 offset:0\n"
"v_mfma_f32_16x16x32_bf16 v[180:183], a[84:87], a[4:7], v[180:183]\n"
"ds_read_b128 a[20:23], v247 offset:1024\n"
"v_mfma_f32_16x16x32_bf16 v[184:187], a[84:87], a[8:11], v[184:187]\n"
"ds_read_b128 a[24:27], v247 offset:2048\n"
"v_mfma_f32_16x16x32_bf16 v[188:191], a[84:87], a[12:15], v[188:191]\n"
"ds_read_b128 a[28:31], v247 offset:3072\n"
"v_mfma_f32_16x16x32_bf16 v[204:207], a[88:91], a[12:15], v[204:207]\n"
"v_mfma_f32_16x16x32_bf16 v[200:203], a[88:91], a[8:11], v[200:203]\n"
"v_mfma_f32_16x16x32_bf16 v[196:199], a[88:91], a[4:7], v[196:199]\n"
"v_mfma_f32_16x16x32_bf16 v[192:195], a[88:91], a[0:3], v[192:195]\n"
"s_waitcnt lgkmcnt(0)\n"
"v_mfma_f32_16x16x32_bf16 v[176:179], a[92:95], a[16:19], v[176:179]\n"
"ds_read_b128 a[0:3], v247 offset:8192\n"
"v_mfma_f32_16x16x32_bf16 v[180:183], a[92:95], a[20:23], v[180:183]\n"
"ds_read_b128 a[4:7], v247 offset:9216\n"
"v_mfma_f32_16x16x32_bf16 v[184:187], a[92:95], a[24:27], v[184:187]\n"
"ds_read_b128 a[8:11], v247 offset:10240\n"
"v_mfma_f32_16x16x32_bf16 v[188:191], a[92:95], a[28:31], v[188:191]\n"
"ds_read_b128 a[12:15], v247 offset:11264\n"
"s_waitcnt vmcnt(9)\n"
"v_mfma_f32_16x16x32_bf16 v[204:207], a[96:99], a[28:31], v[204:207]\n"
"v_mfma_f32_16x16x32_bf16 v[200:203], a[96:99], a[24:27], v[200:203]\n"
"v_mfma_f32_16x16x32_bf16 v[196:199], a[96:99], a[20:23], v[196:199]\n"
"v_mfma_f32_16x16x32_bf16 v[192:195], a[96:99], a[16:19], v[192:195]\n"
"s_waitcnt lgkmcnt(0)\n"
"s_waitcnt vmcnt(8)\n"
"v_mfma_f32_16x16x32_bf16 v[176:179], a[100:103], a[0:3], v[176:179]\n"
"ds_read_b128 a[16:19], v247 offset:16384\n"
"v_mfma_f32_16x16x32_bf16 v[180:183], a[100:103], a[4:7], v[180:183]\n"
"ds_read_b128 a[20:23], v247 offset:17408\n"
"v_mfma_f32_16x16x32_bf16 v[184:187], a[100:103], a[8:11], v[184:187]\n"
"ds_read_b128 a[24:27], v247 offset:18432\n"
"v_mfma_f32_16x16x32_bf16 v[188:191], a[100:103], a[12:15], v[188:191]\n"
"ds_read_b128 a[28:31], v247 offset:19456\n"
"s_waitcnt vmcnt(7)\n"
"v_mfma_f32_16x16x32_bf16 v[204:207], a[104:107], a[12:15], v[204:207]\n"
"v_mfma_f32_16x16x32_bf16 v[200:203], a[104:107], a[8:11], v[200:203]\n"
"v_mfma_f32_16x16x32_bf16 v[196:199], a[104:107], a[4:7], v[196:199]\n"
"v_mfma_f32_16x16x32_bf16 v[192:195], a[104:107], a[0:3], v[192:195]\n"
"s_waitcnt lgkmcnt(0)\n"
"s_waitcnt vmcnt(6)\n"
"v_mfma_f32_16x16x32_bf16 v[176:179], a[108:111], a[16:19], v[176:179]\n"
"v_mfma_f32_16x16x32_bf16 v[180:183], a[108:111], a[20:23], v[180:183]\n"
"v_mfma_f32_16x16x32_bf16 v[184:187], a[108:111], a[24:27], v[184:187]\n"
"v_mfma_f32_16x16x32_bf16 v[188:191], a[108:111], a[28:31], v[188:191]\n"
"s_waitcnt vmcnt(5)\n"
"v_mfma_f32_16x16x32_bf16 v[204:207], a[112:115], a[28:31], v[204:207]\n"
"v_mfma_f32_16x16x32_bf16 v[200:203], a[112:115], a[24:27], v[200:203]\n"
"v_mfma_f32_16x16x32_bf16 v[196:199], a[112:115], a[20:23], v[196:199]\n"
"v_mfma_f32_16x16x32_bf16 v[192:195], a[112:115], a[16:19], v[192:195]\n"
"s_mov_b32 s38, %[n1]\n"
"s_mov_b32 s24, 12288\n"
"v_cvt_pk_bf16_f32 v238, v176, v177\n"
"v_cvt_pk_bf16_f32 v239, v178, v179\n"
"s_nop 3\n"
"v_cvt_pk_bf16_f32 v240, v192, v193\n"
"v_cvt_pk_bf16_f32 v241, v194, v195\n"
"buffer_store_dwordx4 v[238:241], %[vl], s[36:39], s24 offen offset:0 sc1\n"
"v_cvt_pk_bf16_f32 v242, v180, v181\n"
"v_cvt_pk_bf16_f32 v243, v182, v183\n"
"v_cvt_pk_bf16_f32 v244, v196, v197\n"
"v_cvt_pk_bf16_f32 v245, v198, v199\n"
"buffer_store_dwordx4 v[242:245], %[vl], s[36:39], s24 offen offset:1024 sc1\n"
"v_cvt_pk_bf16_f32 v238, v184, v185\n"
"v_cvt_pk_bf16_f32 v239, v186, v187\n"
"v_cvt_pk_bf16_f32 v240, v200, v201\n"
"v_cvt_pk_bf16_f32 v241, v202, v203\n"
"buffer_store_dwordx4 v[238:241], %[vl], s[36:39], s24 offen offset:2048 sc1\n"
"v_cvt_pk_bf16_f32 v242, v188, v189\n"
"v_cvt_pk_bf16_f32 v243, v190, v191\n"
"v_cvt_pk_bf16_f32 v244, v204, v205\n"
"v_cvt_pk_bf16_f32 v245, v206, v207\n"
"buffer_store_dwordx4 v[242:245], %[vl], s[36:39], s24 offen offset:3072 sc1\n"
"s_mov_b32 s27, 0x100000\n"
"s_cmp_lg_u32 %[n0], 0\n"
"s_cbranch_scc1 .Lk0_%=\n"
"v_accvgpr_read_b32 v64, a32\n"
"v_accvgpr_read_b32 v65, a33\n"
"v_accvgpr_read_b32 v66, a34\n"
"v_accvgpr_read_b32 v67, a35\n"
"v_cvt_pk_bf16_f32 v32, v64, v65\n"
"v_cvt_pk_bf16_f32 v33, v66, v67\n"
"v_accvgpr_read_b32 v68, a48\n"
"v_accvgpr_read_b32 v69, a49\n"
"v_accvgpr_read_b32 v70, a50\n"
"v_accvgpr_read_b32 v71, a51\n"
"v_cvt_pk_bf16_f32 v34, v68, v69\n"
"v_cvt_pk_bf16_f32 v35, v70, v71\n"
"ds_write_b128 v228, v[32:35] offset:0\n"
"v_accvgpr_read_b32 v72, a36\n"
"v_accvgpr_read_b32 v73, a37\n"
"v_accvgpr_read_b32 v74, a38\n"
"v_accvgpr_read_b32 v75, a39\n"
"v_cvt_pk_bf16_f32 v36, v72, v73\n"
"v_cvt_pk_bf16_f32 v37, v74, v75\n"
"v_accvgpr_read_b32 v76, a52\n"
"v_accvgpr_read_b32 v77, a53\n"
"v_accvgpr_read_b32 v78, a54\n"
"v_accvgpr_read_b32 v79, a55\n"
"v_cvt_pk_bf16_f32 v38, v76, v77\n"
"v_cvt_pk_bf16_f32 v39, v78, v79\n"
"ds_write_b128 v228, v[36:39] offset:1024\n"
"v_accvgpr_read_b32 v80, a40\n"
"v_accvgpr_read_b32 v81, a41\n"
"v_accvgpr_read_b32 v82, a42\n"
"v_accvgpr_read_b32 v83, a43\n"
"v_cvt_pk_bf16_f32 v40, v80, v81\n"
"v_cvt_pk_bf16_f32 v41, v82, v83\n"
"v_accvgpr_read_b32 v84, a56\n"
"v_accvgpr_read_b32 v85, a57\n"
"v_accvgpr_read_b32 v86, a58\n"
"v_accvgpr_read_b32 v87, a59\n"
"v_cvt_pk_bf16_f32 v42, v84, v85\n"
"v_cvt_pk_bf16_f32 v43, v86, v87\n"
"ds_write_b128 v228, v[40:43] offset:2048\n"
"v_accvgpr_read_b32 v88, a44\n"
"v_accvgpr_read_b32 v89, a45\n"
"v_accvgpr_read_b32 v90, a46\n"
"v_accvgpr_read_b32 v91, a47\n"
"v_cvt_pk_bf16_f32 v44, v88, v89\n"
"v_cvt_pk_bf16_f32 v45, v90, v91\n"
"v_accvgpr_read_b32 v92, a60\n"
"v_accvgpr_read_b32 v93, a61\n"
"v_accvgpr_read_b32 v94, a62\n"
"v_accvgpr_read_b32 v95, a63\n"
"v_cvt_pk_bf16_f32 v46, v92, v93\n"
"v_cvt_pk_bf16_f32 v47, v94, v95\n"
"ds_write_b128 v228, v[44:47] offset:3072\n"
"v_cvt_pk_bf16_f32 v48, v0, v1\n"
"v_cvt_pk_bf16_f32 v49, v2, v3\n"
"v_cvt_pk_bf16_f32 v50, v16, v17\n"
"v_cvt_pk_bf16_f32 v51, v18, v19\n"
"ds_write_b128 v228, v[48:51] offset:4096\n"
"v_cvt_pk_bf16_f32 v52, v4, v5\n"
"v_cvt_pk_bf16_f32 v53, v6, v7\n"
"v_cvt_pk_bf16_f32 v54, v20, v21\n"
"v_cvt_pk_bf16_f32 v55, v22, v23\n"
"ds_write_b128 v228, v[52:55] offset:5120\n"
"v_cvt_pk_bf16_f32 v56, v8, v9\n"
"v_cvt_pk_bf16_f32 v57, v10, v11\n"
"v_cvt_pk_bf16_f32 v58, v24, v25\n"
"v_cvt_pk_bf16_f32 v59, v26, v27\n"
"ds_write_b128 v228, v[56:59] offset:6144\n"
"v_cvt_pk_bf16_f32 v60, v12, v13\n"
"v_cvt_pk_bf16_f32 v61, v14, v15\n"
"v_cvt_pk_bf16_f32 v62, v28, v29\n"
"v_cvt_pk_bf16_f32 v63, v30, v31\n"
"ds_write_b128 v228, v[60:63] offset:7168\n"
"s_waitcnt lgkmcnt(0)\n"
".Lk0_%=:\n"
"s_cmp_lg_u32 %[n1], 0\n"
"s_cbranch_scc1 .Lk1_%=\n"
"v_cvt_pk_bf16_f32 v32, v144, v145\n"
"v_cvt_pk_bf16_f32 v33, v146, v147\n"
"v_cvt_pk_bf16_f32 v34, v160, v161\n"
"v_cvt_pk_bf16_f32 v35, v162, v163\n"
"ds_write_b128 v228, v[32:35] offset:0\n"
"v_cvt_pk_bf16_f32 v36, v148, v149\n"
"v_cvt_pk_bf16_f32 v37, v150, v151\n"
"v_cvt_pk_bf16_f32 v38, v164, v165\n"
"v_cvt_pk_bf16_f32 v39, v166, v167\n"
"ds_write_b128 v228, v[36:39] offset:1024\n"
"v_cvt_pk_bf16_f32 v40, v152, v153\n"
"v_cvt_pk_bf16_f32 v41, v154, v155\n"
"v_cvt_pk_bf16_f32 v42, v168, v169\n"
"v_cvt_pk_bf16_f32 v43, v170, v171\n"
"ds_write_b128 v228, v[40:43] offset:2048\n"
"v_cvt_pk_bf16_f32 v44, v156, v157\n"
"v_cvt_pk_bf16_f32 v45, v158, v159\n"
"v_cvt_pk_bf16_f32 v46, v172, v173\n"
"v_cvt_pk_bf16_f32 v47, v174, v175\n"
"ds_write_b128 v228, v[44:47] offset:3072\n"
"v_cvt_pk_bf16_f32 v48, v176, v177\n"
"v_cvt_pk_bf16_f32 v49, v178, v179\n"
"v_cvt_pk_bf16_f32 v50, v192, v193\n"
"v_cvt_pk_bf16_f32 v51, v194, v195\n"
"ds_write_b128 v228, v[48:51] offset:4096\n"
"v_cvt_pk_bf16_f32 v52, v180, v181\n"
"v_cvt_pk_bf16_f32 v53, v182, v183\n"
"v_cvt_pk_bf16_f32 v54, v196, v197\n"
"v_cvt_pk_bf16_f32 v55, v198, v199\n"
"ds_write_b128 v228, v[52:55] offset:5120\n"
"v_cvt_pk_bf16_f32 v56, v184, v185\n"
"v_cvt_pk_bf16_f32 v57, v186, v187\n"
"v_cvt_pk_bf16_f32 v58, v200, v201\n"
"v_cvt_pk_bf16_f32 v59, v202, v203\n"
"ds_write_b128 v228, v[56:59] offset:6144\n"
"v_cvt_pk_bf16_f32 v60, v188, v189\n"
"v_cvt_pk_bf16_f32 v61, v190, v191\n"
"v_cvt_pk_bf16_f32 v62, v204, v205\n"
"v_cvt_pk_bf16_f32 v63, v206, v207\n"
"ds_write_b128 v228, v[60:63] offset:7168\n"
"s_waitcnt lgkmcnt(0)\n"
".Lk1_%=:\n"
"s_waitcnt vmcnt(5)\n"
"s_mov_b64 exec, 1\n"
"global_store_dword v230, v231, %[f2] sc1\n"
"s_mov_b64 exec, -1\n"
"s_cmp_eq_u32 %[w], 0\n"
"s_cbranch_scc1 .Lw0_%=\n"
"v_bfe_u32 v3, %[vl], 2, 7\n"
"global_load_dword v2, v3, %[cnt] sc1\n"
"s_waitcnt vmcnt(0)\n"
"s_mov_b64 exec, 2\n"
"global_store_dword v230, v231, %[f2] sc1\n"
"s_mov_b64 exec, -1\n"
"ds_read_b128 v[8:11], v226 offset:0\n"
"ds_read_b128 v[12:15], v226 offset:256\n"
".La_%=:\n"
"v_cmp_ne_u32 vcc, 1, v2\n"
"s_sub_u32 s27, s27, 1\n"
"s_cbranch_vccz .Lak_%=\n"
"s_cbranch_scc1 .Lak_%=\n"
"s_sleep 2\n"
"global_load_dword v2, v3, %[cnt] offset:0 sc1\n"
"s_waitcnt vmcnt(0)\n"
"s_branch .La_%=\n"
".Lak_%=:\n"
"buffer_load_dwordx4 v[36:39], %[vl], %[rp], s40 offen sc1\n"
"buffer_load_dwordx4 v[40:43], %[vl], %[rp], s41 offen sc1\n"
"buffer_load_dwordx4 v[44:47], %[vl], %[rp], s42 offen sc1\n"
"buffer_load_dwordx4 v[48:51], %[vl], %[rp], s43 offen sc1\n"
"buffer_load_dwordx4 v[52:55], %[vl], %[rp], s44 offen sc1\n"
"buffer_load_dwordx4 v[56:59], %[vl], %[rp], s45 offen sc1\n"
"buffer_load_dwordx4 v[60:63], %[vl], %[rp], s46 offen sc1\n"
"s_cmp_lg_u32 %[w], 3\n"
"s_cbranch_scc1 .Ln3_%=\n"
"v_bfe_u32 v16, %[vl], 4, 2\n"
"v_bfe_u32 v17, %[vl], 6, 2\n"
"v_mov_b32 v18, 18432\n"
"v_mul_u32_u24 v20, v18, v16\n"
"v_lshl_add_u32 v20, v224, 2, v20\n"
"v_lshl_add_u32 v20, v17, 4, v20\n"
"s_mov_b32 s26, %[so8]\n"
"buffer_load_dwordx4 v[96:99], v20, %[rp], s26 offen sc1\n"
"s_add_u32 s26, s26, 2359296\n"
"buffer_load_dwordx4 v[100:103], v20, %[rp], s26 offen sc1\n"
"s_add_u32 s26, s26, 2359296\n"
"buffer_load_dwordx4 v[104:107], v20, %[rp], s26 offen sc1\n"
"s_add_u32 s26, s26, 2359296\n"
"buffer_load_dwordx4 v[108:111], v20, %[rp], s26 offen sc1\n"
"s_add_u32 s26, s26, 2359296\n"
"buffer_load_dwordx4 v[112:115], v20, %[rp], s26 offen sc1\n"
"s_add_u32 s26, s26, 2359296\n"
"buffer_load_dwordx4 v[116:119], v20, %[rp], s26 offen sc1\n"
"s_add_u32 s26, s26, 2359296\n"
"buffer_load_dwordx4 v[120:123], v20, %[rp], s26 offen sc1\n"
"s_add_u32 s26, s26, 2359296\n"
"buffer_load_dwordx4 v[124:127], v20, %[rp], s26 offen sc1\n"
"v_and_b32 v19, 32, %[vl]\n"
"v_add_u32 v18, v19, v224\n"
"global_load_dwordx4 v[24:27], v18, %[gb] offset:2048\n"
"global_load_dwordx4 v[28:31], v18, %[gb] offset:2064\n"
"v_and_b32 v16, 1, v16\n"
"v_mov_b32 v21, 19456\n"
"v_mov_b32 v22, 18432\n"
"v_mul_u32_u24 v21, v21, v16\n"
"v_mul_u32_u24 v22, v22, v16\n"
"v_lshl_add_u32 v21, v18, 3, v21\n"
"v_lshl_add_u32 v21, v17, 4, v21\n"
"v_add_u32 v21, %[xs8], v21\n"
"v_lshrrev_b32 v18, 1, v18\n"
"v_add_u32 v22, v22, v18\n"
"v_mov_b32 v19, 1152\n"
"v_mad_u32_u24 v22, v19, v17, v22\n"
"ds_read_b128 v[4:7], v21\n"
".Ln3_%=:\n"
"s_barrier\n"
"buffer_load_dwordx4 v[68:71], v229, %[rp], s40 offen sc1\n"
"buffer_load_dwordx4 v[72:75], v229, %[rp], s41 offen sc1\n"
"buffer_load_dwordx4 v[76:79], v229, %[rp], s42 offen sc1\n"
"buffer_load_dwordx4 v[80:83], v229, %[rp], s43 offen sc1\n"
"buffer_load_dwordx4 v[84:87], v229, %[rp], s44 offen sc1\n"
"buffer_load_dwordx4 v[88:91], v229, %[rp], s45 offen sc1\n"
"buffer_load_dwordx4 v[92:95], v229, %[rp], s46 offen sc1\n"
"s_branch .Lsum_%=\n"
".Lw0_%=:\n"
"s_waitcnt vmcnt(0)\n"
"s_mov_b64 exec, 2\n"
"global_store_dword v230, v231, %[f2] sc1\n"
"s_mov_b64 exec, -1\n"
"v_bfe_u32 v3, %[vl], 2, 5\n"
"s_waitcnt lgkmcnt(2)\n"
"ds_read_b128 v[8:11], v226 offset:0\n"
"s_waitcnt lgkmcnt(2)\n"
"ds_read_b128 v[12:15], v226 offset:256\n"
"global_load_dword v2, v3, %[cnt] offset:128 sc1\n"
"s_waitcnt vmcnt(0)\n"
".Lb_%=:\n"
"v_cmp_ne_u32 vcc, 1, v2\n"
"s_sub_u32 s27, s27, 1\n"
"s_cbranch_vccz .Lbk_%=\n"
"s_cbranch_scc1 .Lbk_%=\n"
"s_sleep 2\n"
"global_load_dword v2, v3, %[cnt] offset:128 sc1\n"
"s_waitcnt vmcnt(0)\n"
"s_branch .Lb_%=\n"
".Lbk_%=:\n"
"s_barrier\n"
"buffer_load_dwordx4 v[36:39], %[vl], %[rp], s40 offen sc1\n"
"buffer_load_dwordx4 v[40:43], %[vl], %[rp], s41 offen sc1\n"
"buffer_load_dwordx4 v[44:47], %[vl], %[rp], s42 offen sc1\n"
"buffer_load_dwordx4 v[48:51], %[vl], %[rp], s43 offen sc1\n"
"buffer_load_dwordx4 v[52:55], %[vl], %[rp], s44 offen sc1\n"
"buffer_load_dwordx4 v[56:59], %[vl], %[rp], s45 offen sc1\n"
"buffer_load_dwordx4 v[60:63], %[vl], %[rp], s46 offen sc1\n"
"buffer_load_dwordx4 v[68:71], v229, %[rp], s40 offen sc1\n"
"buffer_load_dwordx4 v[72:75], v229, %[rp], s41 offen sc1\n"
"buffer_load_dwordx4 v[76:79], v229, %[rp], s42 offen sc1\n"
"buffer_load_dwordx4 v[80:83], v229, %[rp], s43 offen sc1\n"
"buffer_load_dwordx4 v[84:87], v229, %[rp], s44 offen sc1\n"
"buffer_load_dwordx4 v[88:91], v229, %[rp], s45 offen sc1\n"
"buffer_load_dwordx4 v[92:95], v229, %[rp], s46 offen sc1\n"
".Lsum_%=:\n"
"ds_read_b128 v[32:35], v225\n"
"ds_read_b128 v[64:67], v225 offset:4096\n"
"s_waitcnt lgkmcnt(1)\n"
"v_lshlrev_b32 v144, 16, v32\n"
"v_and_b32 v145, 0xffff0000, v32\n"
"v_lshlrev_b32 v146, 16, v33\n"
"v_and_b32 v147, 0xffff0000, v33\n"
"v_lshlrev_b32 v148, 16, v34\n"
"v_and_b32 v149, 0xffff0000, v34\n"
"v_lshlrev_b32 v150, 16, v35\n"
"v_and_b32 v151, 0xffff0000, v35\n"
"s_waitcnt vmcnt(13)\n"
"v_lshlrev_b32 v160, 16, v36\n"
"v_and_b32 v161, 0xffff0000, v36\n"
"v_lshlrev_b32 v162, 16, v37\n"
"v_and_b32 v163, 0xffff0000, v37\n"
"v_pk_add_f32 v[144:145], v[144:145], v[160:161]\n"
"v_pk_add_f32 v[146:147], v[146:147], v[162:163]\n"
"v_lshlrev_b32 v160, 16, v38\n"
"v_and_b32 v161, 0xffff0000, v38\n"
"v_lshlrev_b32 v162, 16, v39\n"
"v_and_b32 v163, 0xffff0000, v39\n"
"v_pk_add_f32 v[148:149], v[148:149], v[160:161]\n"
"v_pk_add_f32 v[150:151], v[150:151], v[162:163]\n"
"s_waitcnt vmcnt(12)\n"
"v_lshlrev_b32 v160, 16, v40\n"
"v_and_b32 v161, 0xffff0000, v40\n"
"v_lshlrev_b32 v162, 16, v41\n"
"v_and_b32 v163, 0xffff0000, v41\n"
"v_pk_add_f32 v[144:145], v[144:145], v[160:161]\n"
"v_pk_add_f32 v[146:147], v[146:147], v[162:163]\n"
"v_lshlrev_b32 v160, 16, v42\n"
"v_and_b32 v161, 0xffff0000, v42\n"
"v_lshlrev_b32 v162, 16, v43\n"
"v_and_b32 v163, 0xffff0000, v43\n"
"v_pk_add_f32 v[148:149], v[148:149], v[160:161]\n"
"v_pk_add_f32 v[150:151], v[150:151], v[162:163]\n"
"s_waitcnt vmcnt(11)\n"
"v_lshlrev_b32 v160, 16, v44\n"
"v_and_b32 v161, 0xffff0000, v44\n"
"v_lshlrev_b32 v162, 16, v45\n"
"v_and_b32 v163, 0xffff0000, v45\n"
"v_pk_add_f32 v[144:145], v[144:145], v[160:161]\n"
"v_pk_add_f32 v[146:147], v[146:147], v[162:163]\n"
"v_lshlrev_b32 v160, 16, v46\n"
"v_and_b32 v161, 0xffff0000, v46\n"
"v_lshlrev_b32 v162, 16, v47\n"
"v_and_b32 v163, 0xffff0000, v47\n"
"v_pk_add_f32 v[148:149], v[148:149], v[160:161]\n"
"v_pk_add_f32 v[150:151], v[150:151], v[162:163]\n"
"s_waitcnt vmcnt(10)\n"
"v_lshlrev_b32 v160, 16, v48\n"
"v_and_b32 v161, 0xffff0000, v48\n"
"v_lshlrev_b32 v162, 16, v49\n"
"v_and_b32 v163, 0xffff0000, v49\n"
"v_pk_add_f32 v[144:145], v[144:145], v[160:161]\n"
"v_pk_add_f32 v[146:147], v[146:147], v[162:163]\n"
"v_lshlrev_b32 v160, 16, v50\n"
"v_and_b32 v161, 0xffff0000, v50\n"
"v_lshlrev_b32 v162, 16, v51\n"
"v_and_b32 v163, 0xffff0000, v51\n"
"v_pk_add_f32 v[148:149], v[148:149], v[160:161]\n"
"v_pk_add_f32 v[150:151], v[150:151], v[162:163]\n"
"s_waitcnt vmcnt(9)\n"
"v_lshlrev_b32 v160, 16, v52\n"
"v_and_b32 v161, 0xffff0000, v52\n"
"v_lshlrev_b32 v162, 16, v53\n"
"v_and_b32 v163, 0xffff0000, v53\n"
"v_pk_add_f32 v[144:145], v[144:145], v[160:161]\n"
"v_pk_add_f32 v[146:147], v[146:147], v[162:163]\n"
"v_lshlrev_b32 v160, 16, v54\n"
"v_and_b32 v161, 0xffff0000, v54\n"
"v_lshlrev_b32 v162, 16, v55\n"
"v_and_b32 v163, 0xffff0000, v55\n"
"v_pk_add_f32 v[148:149], v[148:149], v[160:161]\n"
"v_pk_add_f32 v[150:151], v[150:151], v[162:163]\n"
"s_waitcnt vmcnt(8)\n"
"v_lshlrev_b32 v160, 16, v56\n"
"v_and_b32 v161, 0xffff0000, v56\n"
"v_lshlrev_b32 v162, 16, v57\n"
"v_and_b32 v163, 0xffff0000, v57\n"
"v_pk_add_f32 v[144:145], v[144:145], v[160:161]\n"
"v_pk_add_f32 v[146:147], v[146:147], v[162:163]\n"
"v_lshlrev_b32 v160, 16, v58\n"
"v_and_b32 v161, 0xffff0000, v58\n"
"v_lshlrev_b32 v162, 16, v59\n"
"v_and_b32 v163, 0xffff0000, v59\n"
"v_pk_add_f32 v[148:149], v[148:149], v[160:161]\n"
"v_pk_add_f32 v[150:151], v[150:151], v[162:163]\n"
"s_waitcnt vmcnt(7)\n"
"v_lshlrev_b32 v160, 16, v60\n"
"v_and_b32 v161, 0xffff0000, v60\n"
"v_lshlrev_b32 v162, 16, v61\n"
"v_and_b32 v163, 0xffff0000, v61\n"
"v_pk_add_f32 v[144:145], v[144:145], v[160:161]\n"
"v_pk_add_f32 v[146:147], v[146:147], v[162:163]\n"
"v_lshlrev_b32 v160, 16, v62\n"
"v_and_b32 v161, 0xffff0000, v62\n"
"v_lshlrev_b32 v162, 16, v63\n"
"v_and_b32 v163, 0xffff0000, v63\n"
"v_pk_add_f32 v[148:149], v[148:149], v[160:161]\n"
"v_pk_add_f32 v[150:151], v[150:151], v[162:163]\n"
"s_cmp_lg_u32 %[w], 3\n"
"s_cbranch_scc1 .Lm3_%=\n"
"s_waitcnt vmcnt(7) lgkmcnt(0)\n"
"v_and_b32 v16, 16, %[vl]\n"
"v_and_b32 v17, 32, %[vl]\n"
"v_cmp_ne_u32 s[30:31], 0, v16\n"
"v_cmp_ne_u32 vcc, 0, v17\n"
"v_lshlrev_b32 v176, 16, v96\n"
"v_and_b32 v177, 0xffff0000, v96\n"
"v_lshlrev_b32 v178, 16, v97\n"
"v_and_b32 v179, 0xffff0000, v97\n"
"v_lshlrev_b32 v180, 16, v98\n"
"v_and_b32 v181, 0xffff0000, v98\n"
"v_lshlrev_b32 v182, 16, v99\n"
"v_and_b32 v183, 0xffff0000, v99\n"
"v_lshlrev_b32 v160, 16, v100\n"
"v_and_b32 v161, 0xffff0000, v100\n"
"v_lshlrev_b32 v162, 16, v101\n"
"v_and_b32 v163, 0xffff0000, v101\n"
"v_pk_add_f32 v[176:177], v[176:177], v[160:161]\n"
"v_pk_add_f32 v[178:179], v[178:179], v[162:163]\n"
"v_lshlrev_b32 v160, 16, v102\n"
"v_and_b32 v161, 0xffff0000, v102\n"
"v_lshlrev_b32 v162, 16, v103\n"
"v_and_b32 v163, 0xffff0000, v103\n"
"v_pk_add_f32 v[180:181], v[180:181], v[160:161]\n"
"v_pk_add_f32 v[182:183], v[182:183], v[162:163]\n"
"v_lshlrev_b32 v160, 16, v104\n"
"v_and_b32 v161, 0xffff0000, v104\n"
"v_lshlrev_b32 v162, 16, v105\n"
"v_and_b32 v163, 0xffff0000, v105\n"
"v_pk_add_f32 v[176:177], v[176:177], v[160:161]\n"
"v_pk_add_f32 v[178:179], v[178:179], v[162:163]\n"
"v_lshlrev_b32 v160, 16, v106\n"
"v_and_b32 v161, 0xffff0000, v106\n"
"v_lshlrev_b32 v162, 16, v107\n"
"v_and_b32 v163, 0xffff0000, v107\n"
"v_pk_add_f32 v[180:181], v[180:181], v[160:161]\n"
"v_pk_add_f32 v[182:183], v[182:183], v[162:163]\n"
"v_lshlrev_b32 v160, 16, v108\n"
"v_and_b32 v161, 0xffff0000, v108\n"
"v_lshlrev_b32 v162, 16, v109\n"
"v_and_b32 v163, 0xffff0000, v109\n"
"v_pk_add_f32 v[176:177], v[176:177], v[160:161]\n"
"v_pk_add_f32 v[178:179], v[178:179], v[162:163]\n"
"v_lshlrev_b32 v160, 16, v110\n"
"v_and_b32 v161, 0xffff0000, v110\n"
"v_lshlrev_b32 v162, 16, v111\n"
"v_and_b32 v163, 0xffff0000, v111\n"
"v_pk_add_f32 v[180:181], v[180:181], v[160:161]\n"
"v_pk_add_f32 v[182:183], v[182:183], v[162:163]\n"
"v_lshlrev_b32 v160, 16, v112\n"
"v_and_b32 v161, 0xffff0000, v112\n"
"v_lshlrev_b32 v162, 16, v113\n"
"v_and_b32 v163, 0xffff0000, v113\n"
"v_pk_add_f32 v[176:177], v[176:177], v[160:161]\n"
"v_pk_add_f32 v[178:179], v[178:179], v[162:163]\n"
"v_lshlrev_b32 v160, 16, v114\n"
"v_and_b32 v161, 0xffff0000, v114\n"
"v_lshlrev_b32 v162, 16, v115\n"
"v_and_b32 v163, 0xffff0000, v115\n"
"v_pk_add_f32 v[180:181], v[180:181], v[160:161]\n"
"v_pk_add_f32 v[182:183], v[182:183], v[162:163]\n"
"v_lshlrev_b32 v160, 16, v116\n"
"v_and_b32 v161, 0xffff0000, v116\n"
"v_lshlrev_b32 v162, 16, v117\n"
"v_and_b32 v163, 0xffff0000, v117\n"
"v_pk_add_f32 v[176:177], v[176:177], v[160:161]\n"
"v_pk_add_f32 v[178:179], v[178:179], v[162:163]\n"
"v_lshlrev_b32 v160, 16, v118\n"
"v_and_b32 v161, 0xffff0000, v118\n"
"v_lshlrev_b32 v162, 16, v119\n"
"v_and_b32 v163, 0xffff0000, v119\n"
"v_pk_add_f32 v[180:181], v[180:181], v[160:161]\n"
"v_pk_add_f32 v[182:183], v[182:183], v[162:163]\n"
"v_lshlrev_b32 v160, 16, v120\n"
"v_and_b32 v161, 0xffff0000, v120\n"
"v_lshlrev_b32 v162, 16, v121\n"
"v_and_b32 v163, 0xffff0000, v121\n"
"v_pk_add_f32 v[176:177], v[176:177], v[160:161]\n"
"v_pk_add_f32 v[178:179], v[178:179], v[162:163]\n"
"v_lshlrev_b32 v160, 16, v122\n"
"v_and_b32 v161, 0xffff0000, v122\n"
"v_lshlrev_b32 v162, 16, v123\n"
"v_and_b32 v163, 0xffff0000, v123\n"
"v_pk_add_f32 v[180:181], v[180:181], v[160:161]\n"
"v_pk_add_f32 v[182:183], v[182:183], v[162:163]\n"
"v_lshlrev_b32 v160, 16, v124\n"
"v_and_b32 v161, 0xffff0000, v124\n"
"v_lshlrev_b32 v162, 16, v125\n"
"v_and_b32 v163, 0xffff0000, v125\n"
"v_pk_add_f32 v[176:177], v[176:177], v[160:161]\n"
"v_pk_add_f32 v[178:179], v[178:179], v[162:163]\n"
"v_lshlrev_b32 v160, 16, v126\n"
"v_and_b32 v161, 0xffff0000, v126\n"
"v_lshlrev_b32 v162, 16, v127\n"
"v_and_b32 v163, 0xffff0000, v127\n"
"v_pk_add_f32 v[180:181], v[180:181], v[160:161]\n"
"v_pk_add_f32 v[182:183], v[182:183], v[162:163]\n"
"v_cndmask_b32 v200, v180, v176, s[30:31]\n"
"v_cndmask_b32 v176, v176, v180, s[30:31]\n"
"s_nop 0\n"
"v_mov_b32_dpp v201, v200 quad_perm:[1,0,3,2] row_mask:0xf bank_mask:0xf\n"
"v_cndmask_b32 v184, v176, v201, s[30:31]\n"
"v_cndmask_b32 v185, v201, v176, s[30:31]\n"
"v_cndmask_b32 v202, v181, v177, s[30:31]\n"
"v_cndmask_b32 v177, v177, v181, s[30:31]\n"
"s_nop 0\n"
"v_mov_b32_dpp v203, v202 quad_perm:[1,0,3,2] row_mask:0xf bank_mask:0xf\n"
"v_cndmask_b32 v186, v177, v203, s[30:31]\n"
"v_cndmask_b32 v187, v203, v177, s[30:31]\n"
"v_cndmask_b32 v200, v182, v178, s[30:31]\n"
"v_cndmask_b32 v178, v178, v182, s[30:31]\n"
"s_nop 0\n"
"v_mov_b32_dpp v201, v200 quad_perm:[1,0,3,2] row_mask:0xf bank_mask:0xf\n"
"v_cndmask_b32 v188, v178, v201, s[30:31]\n"
"v_cndmask_b32 v189, v201, v178, s[30:31]\n"
"v_cndmask_b32 v202, v183, v179, s[30:31]\n"
"v_cndmask_b32 v179, v179, v183, s[30:31]\n"
"s_nop 0\n"
"v_mov_b32_dpp v203, v202 quad_perm:[1,0,3,2] row_mask:0xf bank_mask:0xf\n"
"v_cndmask_b32 v190, v179, v203, s[30:31]\n"
"v_cndmask_b32 v191, v203, v179, s[30:31]\n"
"v_cndmask_b32 v200, v188, v184, vcc\n"
"v_cndmask_b32 v184, v184, v188, vcc\n"
"s_nop 0\n"
"v_mov_b32_dpp v201, v200 quad_perm:[2,3,0,1] row_mask:0xf bank_mask:0xf\n"
"v_cndmask_b32 v192, v184, v201, vcc\n"
"v_cndmask_b32 v194, v201, v184, vcc\n"
"v_cndmask_b32 v202, v189, v185, vcc\n"
"v_cndmask_b32 v185, v185, v189, vcc\n"
"s_nop 0\n"
"v_mov_b32_dpp v203, v202 quad_perm:[2,3,0,1] row_mask:0xf bank_mask:0xf\n"
"v_cndmask_b32 v193, v185, v203, vcc\n"
"v_cndmask_b32 v195, v203, v185, vcc\n"
"v_cndmask_b32 v200, v190, v186, vcc\n"
"v_cndmask_b32 v186, v186, v190, vcc\n"
"s_nop 0\n"
"v_mov_b32_dpp v201, v200 quad_perm:[2,3,0,1] row_mask:0xf bank_mask:0xf\n"
"v_cndmask_b32 v196, v186, v201, vcc\n"
"v_cndmask_b32 v198, v201, v186, vcc\n"
"v_cndmask_b32 v202, v191, v187, vcc\n"
"v_cndmask_b32 v187, v187, v191, vcc\n"
"s_nop 0\n"
"v_mov_b32_dpp v203, v202 quad_perm:[2,3,0,1] row_mask:0xf bank_mask:0xf\n"
"v_cndmask_b32 v197, v187, v203, vcc\n"
"v_cndmask_b32 v199, v203, v187, vcc\n"
"v_lshlrev_b32 v160, 16, v4\n"
"v_and_b32 v161, 0xffff0000, v4\n"
"v_lshlrev_b32 v162, 16, v5\n"
"v_and_b32 v163, 0xffff0000, v5\n"
"v_lshlrev_b32 v164, 16, v6\n"
"v_and_b32 v165, 0xffff0000, v6\n"
"v_lshlrev_b32 v166, 16, v7\n"
"v_and_b32 v167, 0xffff0000, v7\n"
"v_pk_fma_f32 v[160:161], v[24:25], v[192:193], v[160:161]\n"
"v_pk_fma_f32 v[162:163], v[26:27], v[194:195], v[162:163]\n"
"v_pk_fma_f32 v[164:165], v[28:29], v[196:197], v[164:165]\n"
"v_pk_fma_f32 v[166:167], v[30:31], v[198:199], v[166:167]\n"
"v_cvt_pk_bf16_f32 v4, v160, v161\n"
"v_cvt_pk_bf16_f32 v5, v162, v163\n"
"v_cvt_pk_bf16_f32 v6, v164, v165\n"
"v_cvt_pk_bf16_f32 v7, v166, v167\n"
"global_store_dwordx4 v22, v[4:7], %[ob8]\n"
"s_nop 1\n"
".Lm3_%=:\n"
"s_waitcnt lgkmcnt(0)\n"
"v_lshlrev_b32 v208, 16, v8\n"
"v_and_b32 v209, 0xffff0000, v8\n"
"v_lshlrev_b32 v210, 16, v9\n"
"v_and_b32 v211, 0xffff0000, v9\n"
"v_lshlrev_b32 v212, 16, v10\n"
"v_and_b32 v213, 0xffff0000, v10\n"
"v_lshlrev_b32 v214, 16, v11\n"
"v_and_b32 v215, 0xffff0000, v11\n"
"v_lshlrev_b32 v216, 16, v12\n"
"v_and_b32 v217, 0xffff0000, v12\n"
"v_lshlrev_b32 v218, 16, v13\n"
"v_and_b32 v219, 0xffff0000, v13\n"
"v_lshlrev_b32 v220, 16, v14\n"
"v_and_b32 v221, 0xffff0000, v14\n"
"v_lshlrev_b32 v222, 16, v15\n"
"v_and_b32 v223, 0xffff0000, v15\n"
"v_lshlrev_b32 v152, 16, v64\n"
"v_and_b32 v153, 0xffff0000, v64\n"
"v_lshlrev_b32 v154, 16, v65\n"
"v_and_b32 v155, 0xffff0000, v65\n"
"v_lshlrev_b32 v156, 16, v66\n"
"v_and_b32 v157, 0xffff0000, v66\n"
"v_lshlrev_b32 v158, 16, v67\n"
"v_and_b32 v159, 0xffff0000, v67\n"
"s_waitcnt vmcnt(6)\n"
"v_lshlrev_b32 v160, 16, v68\n"
"v_and_b32 v161, 0xffff0000, v68\n"
"v_lshlrev_b32 v162, 16, v69\n"
"v_and_b32 v163, 0xffff0000, v69\n"
"v_pk_add_f32 v[152:153], v[152:153], v[160:161]\n"
"v_pk_add_f32 v[154:155], v[154:155], v[162:163]\n"
"v_lshlrev_b32 v160, 16, v70\n"
"v_and_b32 v161, 0xffff0000, v70\n"
"v_lshlrev_b32 v162, 16, v71\n"
"v_and_b32 v163, 0xffff0000, v71\n"
"v_pk_add_f32 v[156:157], v[156:157], v[160:161]\n"
"v_pk_add_f32 v[158:159], v[158:159], v[162:163]\n"
"s_waitcnt vmcnt(5)\n"
"v_lshlrev_b32 v160, 16, v72\n"
"v_and_b32 v161, 0xffff0000, v72\n"
"v_lshlrev_b32 v162, 16, v73\n"
"v_and_b32 v163, 0xffff0000, v73\n"
"v_pk_add_f32 v[152:153], v[152:153], v[160:161]\n"
"v_pk_add_f32 v[154:155], v[154:155], v[162:163]\n"
"v_lshlrev_b32 v160, 16, v74\n"
"v_and_b32 v161, 0xffff0000, v74\n"
"v_lshlrev_b32 v162, 16, v75\n"
"v_and_b32 v163, 0xffff0000, v75\n"
"v_pk_add_f32 v[156:157], v[156:157], v[160:161]\n"
"v_pk_add_f32 v[158:159], v[158:159], v[162:163]\n"
"s_waitcnt vmcnt(4)\n"
"v_lshlrev_b32 v160, 16, v76\n"
"v_and_b32 v161, 0xffff0000, v76\n"
"v_lshlrev_b32 v162, 16, v77\n"
"v_and_b32 v163, 0xffff0000, v77\n"
"v_pk_add_f32 v[152:153], v[152:153], v[160:161]\n"
"v_pk_add_f32 v[154:155], v[154:155], v[162:163]\n"
"v_lshlrev_b32 v160, 16, v78\n"
"v_and_b32 v161, 0xffff0000, v78\n"
"v_lshlrev_b32 v162, 16, v79\n"
"v_and_b32 v163, 0xffff0000, v79\n"
"v_pk_add_f32 v[156:157], v[156:157], v[160:161]\n"
"v_pk_add_f32 v[158:159], v[158:159], v[162:163]\n"
"s_waitcnt vmcnt(3)\n"
"v_lshlrev_b32 v160, 16, v80\n"
"v_and_b32 v161, 0xffff0000, v80\n"
"v_lshlrev_b32 v162, 16, v81\n"
"v_and_b32 v163, 0xffff0000, v81\n"
"v_pk_add_f32 v[152:153], v[152:153], v[160:161]\n"
"v_pk_add_f32 v[154:155], v[154:155], v[162:163]\n"
"v_lshlrev_b32 v160, 16, v82\n"
"v_and_b32 v161, 0xffff0000, v82\n"
"v_lshlrev_b32 v162, 16, v83\n"
"v_and_b32 v163, 0xffff0000, v83\n"
"v_pk_add_f32 v[156:157], v[156:157], v[160:161]\n"
"v_pk_add_f32 v[158:159], v[158:159], v[162:163]\n"
"s_waitcnt vmcnt(2)\n"
"v_lshlrev_b32 v160, 16, v84\n"
"v_and_b32 v161, 0xffff0000, v84\n"
"v_lshlrev_b32 v162, 16, v85\n"
"v_and_b32 v163, 0xffff0000, v85\n"
"v_pk_add_f32 v[152:153], v[152:153], v[160:161]\n"
"v_pk_add_f32 v[154:155], v[154:155], v[162:163]\n"
"v_lshlrev_b32 v160, 16, v86\n"
"v_and_b32 v161, 0xffff0000, v86\n"
"v_lshlrev_b32 v162, 16, v87\n"
"v_and_b32 v163, 0xffff0000, v87\n"
"v_pk_add_f32 v[156:157], v[156:157], v[160:161]\n"
"v_pk_add_f32 v[158:159], v[158:159], v[162:163]\n"
"s_waitcnt vmcnt(1)\n"
"v_lshlrev_b32 v160, 16, v88\n"
"v_and_b32 v161, 0xffff0000, v88\n"
"v_lshlrev_b32 v162, 16, v89\n"
"v_and_b32 v163, 0xffff0000, v89\n"
"v_pk_add_f32 v[152:153], v[152:153], v[160:161]\n"
"v_pk_add_f32 v[154:155], v[154:155], v[162:163]\n"
"v_lshlrev_b32 v160, 16, v90\n"
"v_and_b32 v161, 0xffff0000, v90\n"
"v_lshlrev_b32 v162, 16, v91\n"
"v_and_b32 v163, 0xffff0000, v91\n"
"v_pk_add_f32 v[156:157], v[156:157], v[160:161]\n"
"v_pk_add_f32 v[158:159], v[158:159], v[162:163]\n"
"s_waitcnt vmcnt(0)\n"
"v_lshlrev_b32 v160, 16, v92\n"
"v_and_b32 v161, 0xffff0000, v92\n"
"v_lshlrev_b32 v162, 16, v93\n"
"v_and_b32 v163, 0xffff0000, v93\n"
"v_pk_add_f32 v[152:153], v[152:153], v[160:161]\n"
"v_pk_add_f32 v[154:155], v[154:155], v[162:163]\n"
"v_lshlrev_b32 v160, 16, v94\n"
"v_and_b32 v161, 0xffff0000, v94\n"
"v_lshlrev_b32 v162, 16, v95\n"
"v_and_b32 v163, 0xffff0000, v95\n"
"v_pk_add_f32 v[156:157], v[156:157], v[160:161]\n"
"v_pk_add_f32 v[158:159], v[158:159], v[162:163]\n"
"v_fma_f32 v208, v128, v144, v208\n"
"v_fma_f32 v209, v129, v148, v209\n"
"v_fma_f32 v210, v130, v152, v210\n"
"v_fma_f32 v211, v131, v156, v211\n"
"v_fma_f32 v212, v132, v145, v212\n"
"v_fma_f32 v213, v133, v149, v213\n"
"v_fma_f32 v214, v134, v153, v214\n"
"v_fma_f32 v215, v135, v157, v215\n"
"v_cvt_pk_bf16_f32 v8, v208, v209\n"
"v_cvt_pk_bf16_f32 v9, v210, v211\n"
"v_cvt_pk_bf16_f32 v10, v212, v213\n"
"v_cvt_pk_bf16_f32 v11, v214, v215\n"
"global_store_dwordx4 v227, v[8:11], %[ob] offset:0\n"
"v_fma_f32 v216, v136, v146, v216\n"
"v_fma_f32 v217, v137, v150, v217\n"
"v_fma_f32 v218, v138, v154, v218\n"
"v_fma_f32 v219, v139, v158, v219\n"
"v_fma_f32 v220, v140, v147, v220\n"
"v_fma_f32 v221, v141, v151, v221\n"
"v_fma_f32 v222, v142, v155, v222\n"
"v_fma_f32 v223, v143, v159, v223\n"
"v_cvt_pk_bf16_f32 v12, v216, v217\n"
"v_cvt_pk_bf16_f32 v13, v218, v219\n"
"v_cvt_pk_bf16_f32 v14, v220, v221\n"
"v_cvt_pk_bf16_f32 v15, v222, v223\n"
"global_store_dwordx4 v227, v[12:15], %[ob] offset:16\n"
"s_endpgm\n"
"s_waitcnt lgkmcnt(0)\n"
    : "+{s[22:23]}"(ra), "+{s[20:21]}"(rb)
    : [sb] "s"(sb), [pb] "s"(pb), [vl] "v"(vl), [vx0] "v"(vx0), [vst] "v"(vst),
      [vsw] "v"(vsw), [vsr] "v"(vsr), [vxe] "v"(vxe), [vhw] "v"(vhw), [vh4] "v"(vh4),
      [cnt] "s"(fl + 256 * s), [c0] "s"(fl + 65536 + 256 * s + 64 * (w >> 1)), [xl] "s"(xl), [f1] "s"(fl + 16 * s + 4 * w), [f2] "s"(fl + 512 * w + 128 + 4 * s), [rp] "s"(rp), [w] "s"(w), [gb] "s"(ws + (u32)(O_MOD + 5 * D_ * 4)), [gs] "s"(256 * s), [s] "s"(s), [ol] "s"(lb + 152064),
      [n0] "s"(s == 2 * w ? 0 : 18432), [n1] "s"(s == 2 * w + 1 ? 0 : 18432),
      [so] "s"(((s >> 1) * 18 + 8 * (s & 1) + w) * 1024), [so8] "s"((16 + (s >> 2)) * 1024 + 64 * (s & 3)),
      [xs] "s"(xl + 2048 * s), [xs8] "s"(lb + (38 * (s >> 2) + 16) * 1024 + 64 * (s & 3)),
      [ob] "s"(ob + (u32)(16 * w * 1152 + 128 * s)), [ob8] "s"(ob + (u32)((32 * (s >> 2) + 4 * (s & 3)) * 1152 + 1024))
    : "memory","scc","a0","a1","a2","a3","a4","a5","a6","a7","a8","a9","a10","a11","a12","a13","a14","a15","a16","a17","a18","a19","a20","a21","a22","a23","a24","a25","a26","a27","a28","a29","a30","a31","a32","a33","a34","a35","a36","a37","a38","a39","a40","a41","a42","a43","a44","a45","a46","a47","a48","a49","a50","a51","a52","a53","a54","a55","a56","a57","a58","a59","a60","a61","a62","a63","a64","a65","a66","a67","a68","a69","a70","a71","a72","a73","a74","a75","a76","a77","a78","a79","a80","a81","a82","a83","a84","a85","a86","a87","a88","a89","a90","a91","a92","a93","a94","a95","a96","a97","a98","a99","a100","a101","a102","a103","a104","a105","a106","a107","a108","a109","a110","a111","a112","a113","a114","a115","a116","a117","a118","a119","a120","a121","a122","a123","a124","a125","a126","a127","a128","a129","a130","a131","a132","a133","a134","a135","a136","a137","a138","a139","a140","a141","a142","a143","a144","a145","a146","a147","a148","a149","a150","a151","a152","a153","a154","a155","a156","a157","a158","a159","a160","a161","a162","a163","a164","a165","a166","a167","a168","a169","a170","a171","a172","a173","a174","a175","a176","a177","a178","a179","a180","a181","a182","a183","a184","a185","a186","a187","a188","a189","a190","a191","s24","s26","s27","s28","s29","s30","s31","s36","s37","s38","s39","s40","s41","s42","s43","s44","s45","s46","v0","vcc","v1","v2","v3","v4","v5","v6","v7","v8","v9","v10","v11","v12","v13","v14","v15","v16","v17","v18","v19","v20","v21","v22","v23","v24","v25","v26","v27","v28","v29","v30","v31","v32","v33","v34","v35","v36","v37","v38","v39","v40","v41","v42","v43","v44","v45","v46","v47","v48","v49","v50","v51","v52","v53","v54","v55","v56","v57","v58","v59","v60","v61","v62","v63","v64","v65","v66","v67","v68","v69","v70","v71","v72","v73","v74","v75","v76","v77","v78","v79","v80","v81","v82","v83","v84","v85","v86","v87","v88","v89","v90","v91","v92","v93","v94","v95","v96","v97","v98","v99","v100","v101","v102","v103","v104","v105","v106","v107","v108","v109","v110","v111","v112","v113","v114","v115","v116","v117","v118","v119","v120","v121","v122","v123","v124","v125","v126","v127","v128","v129","v130","v131","v132","v133","v134","v135","v136","v137","v138","v139","v140","v141","v142","v143","v144","v145","v146","v147","v148","v149","v150","v151","v152","v153","v154","v155","v156","v157","v158","v159","v160","v161","v162","v163","v164","v165","v166","v167","v168","v169","v170","v171","v172","v173","v174","v175","v176","v177","v178","v179","v180","v181","v182","v183","v184","v185","v186","v187","v188","v189","v190","v191","v192","v193","v194","v195","v196","v197","v198","v199","v200","v201","v202","v203","v204","v205","v206","v207","v208","v209","v210","v211","v212","v213","v214","v215","v216","v217","v218","v219","v220","v221","v222","v223","v224","v225","v226","v227","v228","v229","v230","v231","v232","v233","v234","v235","v236","v237","v238","v239","v240","v241","v242","v243","v244","v245","v246","v247");
}

extern "C" void kernel_launch(void* const* d_in, const int* in_sizes, int n_in, void* d_out, int out_size, void* d_ws,
                              size_t ws_size, hipStream_t stream) {
  const u16* x = (const u16*)d_in[0]; const u16* wqkv = (const u16*)d_in[1]; const u16* wo = (const u16*)d_in[2];
  const u16* w1 = (const u16*)d_in[4]; const u16* w2 = (const u16*)d_in[5]; const u16* c = (const u16*)d_in[6]; const u16* wm = (const u16*)d_in[7];
  char* ws = (char*)d_ws;
  k12_qkv<<<256, 256, 0, stream>>>(x, wqkv, c, wm, ws, wo, w1, w2, (u16*)d_out);
  k3_attn<<<256, 768, 0, stream>>>(w1, w2, wo, ws);
  k5_ffn<<<256, 256, 0, stream>>>(ws, (u16*)d_out);
}
```
